```python
import torch

def _softmax(x, axis=-1):
    return torch.softmax(x, dim=axis)
def _gelu(x):
    return torch.nn.functional.gelu(x, approximate="tanh")
def _silu(x):
    return torch.nn.functional.silu(x)
def _sigmoid(x):
    return torch.sigmoid(x)
def _layernorm(x, axis=-1, eps=1e-5):
    m = x.mean(dim=axis, keepdim=True)
    v = x.var(dim=axis, keepdim=True, unbiased=False)
    return (x - m) / torch.sqrt(v + eps)
def _scatter_add(tgt, idx, src, axis=0):
    return tgt.clone().index_add_(axis, idx, src)
def _segment_sum(src, seg, n):
    out = torch.zeros((n,) + src.shape[1:], dtype=src.dtype)
    return out.index_add_(0, seg, src)
def _pad(x, pw):
    flat = tuple(v for pair in reversed(pw) for v in pair)
    return torch.nn.functional.pad(x, flat)

kernel_name = 'gated_mix__ssm_conv_s5__linear_kernel_hedgehog__prompt'

def setup_inputs():

    _w = (lambda a: (lambda t: t.to(torch.bfloat16) if t.is_floating_point() else t)(torch.as_tensor(a)))
    def _A_setup():
        B, S, D = 1, 2048, 576
        P = D
        Lambda_re = -0.5 * (lambda _x: torch.exp(torch.as_tensor(_x)))((lambda *s: torch.randn(*s, dtype=torch.bfloat16) * 0.8)(P))
        Lambda_im = (lambda *s: torch.randn(*s, dtype=torch.bfloat16) * 0.8)(P) * 1.0
        B_re = (lambda *s: torch.randn(*s, dtype=torch.bfloat16) * 0.8)(P, D) * (D ** -0.5)
        B_im = (lambda *s: torch.randn(*s, dtype=torch.bfloat16) * 0.8)(P, D) * (D ** -0.5)
        C_re = (lambda *s: torch.randn(*s, dtype=torch.bfloat16) * 0.8)(D, P) * (P ** -0.5)
        C_im = (lambda *s: torch.randn(*s, dtype=torch.bfloat16) * 0.8)(D, P) * (P ** -0.5)
        log_dt = (lambda *s: torch.randn(*s, dtype=torch.bfloat16) * 0.8)(P) * 0.3 - 2.0
        Dskip = (lambda *s: torch.randn(*s, dtype=torch.bfloat16) * 0.8)(D)
        return {
            "x": _w((lambda *s: torch.randn(*s, dtype=torch.bfloat16) * 0.05)(B, S, D)),
            "Lambda_re": _w(Lambda_re),
            "Lambda_im": _w(Lambda_im),
            "B_re": _w(B_re),
            "B_im": _w(B_im),
            "C_re": _w(C_re),
            "C_im": _w(C_im),
            "log_dt": _w(log_dt),
            "Dskip": _w(Dskip),
        }
    _A_in = _A_setup()

    def _B_setup():
        B, S, D, H = 1, 2048, 576, 32
        Dh = D // H
        return {
            "x": _w((lambda *s: torch.randn(*s, dtype=torch.bfloat16) * 0.05)(B, S, D)),
            "wqkv": _w((lambda *s: torch.randn(*s, dtype=torch.bfloat16) * 1.6)(D, 3 * D)),
            "w_phi": _w((lambda *s: torch.randn(*s, dtype=torch.bfloat16) * 1.6)(H, Dh, Dh) * (Dh ** -0.5)),
            "wo": _w((lambda *s: torch.randn(*s, dtype=torch.bfloat16) * 0.05)(D, D)),
        }
    _B_in = _B_setup()

    return {
        "A_x": _A_in["x"],
        "A_Lambda_re": _A_in["Lambda_re"],
        "A_Lambda_im": _A_in["Lambda_im"],
        "A_B_re": _A_in["B_re"],
        "A_B_im": _A_in["B_im"],
        "A_C_re": _A_in["C_re"],
        "A_C_im": _A_in["C_im"],
        "A_log_dt": _A_in["log_dt"],
        "A_Dskip": _A_in["Dskip"],
        "B_x": _B_in["x"],
        "B_wqkv": _B_in["wqkv"],
        "B_w_phi": _B_in["w_phi"],
        "B_wo": _B_in["wo"],
        "gate_w": _w((lambda *s: torch.randn(*s, dtype=torch.bfloat16) * 0.05)(576) * 16.0),
    }

def reference(A_x, A_Lambda_re, A_Lambda_im, A_B_re, A_B_im, A_C_re, A_C_im, A_log_dt, A_Dskip, B_x, B_wqkv, B_w_phi, B_wo, gate_w):
    def _A_ref(x, Lambda_re, Lambda_im, B_re, B_im, C_re, C_im, log_dt, Dskip):
        Bsz, L, H = x.shape
        Lam = Lambda_re + 1j * Lambda_im
        Bmat = B_re + 1j * B_im
        Cmat = C_re + 1j * C_im
        dt = torch.exp(log_dt)
        Lam_bar = torch.exp(dt * Lam)
        B_bar = ((Lam_bar - 1.0) / Lam)[:, None] * Bmat
        Bu = torch.einsum("bld,pd->blp", x + 0j, B_bar)
        h = torch.zeros_like(Bu[:, 0, :])
        hs = []
        for t in range(L):
            h = Lam_bar[None, :] * h + Bu[:, t, :]
            hs.append(h)
        hseq = torch.stack(hs, 1)
        y = 2.0 * torch.einsum("blp,dp->bld", hseq, Cmat).real + x * Dskip[None, None, :]
        return y

    def _B_ref(x, wqkv, w_phi, wo):
        B, S, D = x.shape
        H = 32; Dh = D // H
        qkv = (lambda x, *d: x.permute(*d))((x @ wqkv).reshape(B, S, 3, H, Dh), 2, 0, 3, 1, 4)
        q, k, v = qkv[0], qkv[1], qkv[2]
        pq = torch.einsum("bhsd,hdf->bhsf", q, w_phi)
        pk = torch.einsum("bhsd,hdf->bhsf", k, w_phi)
        phi_q = torch.concatenate([_softmax(pq, axis=-1), _softmax(-pq, axis=-1)], axis=-1)
        phi_k = torch.concatenate([_softmax(pk, axis=-1), _softmax(-pk, axis=-1)], axis=-1)
        kv = torch.einsum("bhsf,bhsd->bhfd", phi_k, v)
        z  = torch.einsum("bhsf->bhf", phi_k)
        num = torch.einsum("bhqf,bhfd->bhqd", phi_q, kv)
        den = torch.einsum("bhqf,bhf->bhq", phi_q, z)[..., None] + 1e-6
        o = num / den
        return (lambda x, *d: x.permute(*d))(o, 0, 2, 1, 3).reshape(B, S, D) @ wo

    x = A_x
    g = _sigmoid((x * gate_w).sum(-1, keepdims=True))
    a = _A_ref(x, A_Lambda_re, A_Lambda_im, A_B_re, A_B_im, A_C_re, A_C_im, A_log_dt, A_Dskip)
    b = _B_ref(x, B_wqkv, B_w_phi, B_wo)
    return g * a + (1 - g) * b
```

```cpp
#include <hip/hip_runtime.h>
#include <hip/hip_bf16.h>
#define ROUND_DT 1
typedef __hip_bfloat16 bf16;
constexpr int S=2048, D=576, H=32, DH=18;
__device__ inline float b2f(bf16 v){ return __bfloat162float(v);}
__device__ inline float rbf(float v){ return __bfloat162float(__float2bfloat16(v));}

__global__ void k_bu(const bf16* x, const bf16* Bre, const bf16* Bim, const bf16* lre, const bf16* lim, const bf16* ldt, float* bu){
  int p = blockIdx.x*64+threadIdx.x; int t = blockIdx.y;
  float ar=0, ai=0;
  for(int k=0;k<D;k++){ float xv=b2f(x[t*D+k]); ar+=xv*b2f(Bre[p*D+k]); ai+=xv*b2f(Bim[p*D+k]); }
  float dt = expf(b2f(ldt[p])); if(ROUND_DT) dt=rbf(dt);
  float a=b2f(lre[p]), b=b2f(lim[p]);
  float e=expf(dt*a); float lr=e*cosf(dt*b), li=e*sinf(dt*b);
  float nr=lr-1.f, ni=li; float den=a*a+b*b;
  float cr=(nr*a+ni*b)/den, ci=(ni*a-nr*b)/den;
  bu[(t*D+p)*2]=cr*ar-ci*ai; bu[(t*D+p)*2+1]=cr*ai+ci*ar;
}
__global__ void k_scan(float* bu, const bf16* lre, const bf16* lim, const bf16* ldt){
  int p = blockIdx.x*64+threadIdx.x;
  float dt = expf(b2f(ldt[p])); if(ROUND_DT) dt=rbf(dt);
  float a=b2f(lre[p]), b=b2f(lim[p]);
  float e=expf(dt*a); float lr=e*cosf(dt*b), li=e*sinf(dt*b);
  float hr=0, hi=0;
  for(int t=0;t<S;t++){ float2 u=((float2*)bu)[t*D+p]; float nr=lr*hr-li*hi+u.x, ni=lr*hi+li*hr+u.y; hr=nr; hi=ni; ((float2*)bu)[t*D+p]=make_float2(hr,hi);}
}
__global__ void k_qkv(const bf16* x, const bf16* w, float* qkv){
  int n = blockIdx.x*64+threadIdx.x; int t=blockIdx.y;
  float a=0; for(int k=0;k<D;k++) a+=b2f(x[t*D+k])*b2f(w[k*3*D+n]);
  qkv[t*3*D+n]=a;
}
__global__ __launch_bounds__(64) void k_phi(const float* qkv, const bf16* wphi, float* phi){
  int idx = blockIdx.x*64+threadIdx.x;
  int h=idx%H, t=(idx/H)%S, w=idx/(H*S);
  float pq[DH];
  #pragma unroll
  for(int f=0;f<DH;f++){ float a=0; for(int d=0;d<DH;d++) a+=qkv[t*3*D+w*D+h*DH+d]*b2f(wphi[(h*DH+d)*DH+f]); pq[f]=a; }
  float mx=-1e30f,mn=1e30f;
  #pragma unroll
  for(int f=0;f<DH;f++){mx=fmaxf(mx,pq[f]); mn=fminf(mn,pq[f]);}
  float s1=0,s2=0;
  #pragma unroll
  for(int f=0;f<DH;f++){s1+=expf(pq[f]-mx); s2+=expf(mn-pq[f]);}
  #pragma unroll
  for(int f=0;f<DH;f++){ phi[(size_t)idx*36+f]=expf(pq[f]-mx)/s1; phi[(size_t)idx*36+18+f]=expf(mn-pq[f])/s2; }
}
__global__ void k_kv(const float* qkv, const float* phi, float* kv){
  int idx = blockIdx.x*64+threadIdx.x; if(idx>=H*36*19) return;
  int d=idx%19, f=(idx/19)%36, h=idx/(19*36);
  float a=0; for(int t=0;t<S;t++){ float p=phi[((size_t)(S+t)*H+h)*36+f]; a+= p*(d<18? qkv[t*3*D+2*D+h*DH+d]:1.f);} kv[idx]=a;
}
__global__ void k_o(const float* phi, const float* kv, float* o){
  int n = blockIdx.x*64+threadIdx.x; int t=blockIdx.y; int h=n/DH, d=n%DH;
  float num=0, den=0; for(int f=0;f<36;f++){ float p=phi[((size_t)t*H+h)*36+f]; num+=p*kv[(h*36+f)*19+d]; den+=p*kv[(h*36+f)*19+18]; }
  o[t*D+n]=num/(den+1e-6f);
}
__global__ void k_out(const bf16* x, const float* hs, const float* o, const bf16* Cre, const bf16* Cim, const bf16* wo, const bf16* dsk, const bf16* gw, float* out){
  int n = blockIdx.x*64+threadIdx.x; int t=blockIdx.y;
  float g=0; for(int k=0;k<D;k++) g+=b2f(x[t*D+k])*b2f(gw[k]); g=1.f/(1.f+expf(-g));
  float a=0,b=0; for(int k=0;k<D;k++){ a+=hs[(t*D+k)*2]*b2f(Cre[n*D+k])-hs[(t*D+k)*2+1]*b2f(Cim[n*D+k]); b+=o[t*D+k]*b2f(wo[k*D+n]); }
  a=2*a+b2f(x[t*D+n])*b2f(dsk[n]);
  out[t*D+n]=g*a+(1-g)*b;
}
extern "C" void kernel_launch(void* const* d_in, const int* in_sizes, int n_in, void* d_out, int out_size, void* d_ws, size_t ws_size, hipStream_t stream) {
  const bf16* x=(const bf16*)d_in[0]; const bf16* lre=(const bf16*)d_in[1]; const bf16* lim=(const bf16*)d_in[2];
  const bf16* Bre=(const bf16*)d_in[3]; const bf16* Bim=(const bf16*)d_in[4]; const bf16* Cre=(const bf16*)d_in[5]; const bf16* Cim=(const bf16*)d_in[6];
  const bf16* ldt=(const bf16*)d_in[7]; const bf16* dsk=(const bf16*)d_in[8]; const bf16* wqkv=(const bf16*)d_in[10]; const bf16* wphi=(const bf16*)d_in[11];
  const bf16* wo=(const bf16*)d_in[12]; const bf16* gw=(const bf16*)d_in[13];
  float* ws=(float*)d_ws; float* bu=ws; float* qkv=bu+S*D*2; float* phi=qkv+S*D*3; float* kv=phi+2*S*H*36; float* o=kv+H*36*19+64;
  k_bu<<<dim3(D/64,S),64,0,stream>>>(x,Bre,Bim,lre,lim,ldt,bu);
  k_scan<<<D/64,64,0,stream>>>(bu,lre,lim,ldt);
  k_qkv<<<dim3(3*D/64,S),64,0,stream>>>(x,wqkv,qkv);
  k_phi<<<2*S*H/64,64,0,stream>>>(qkv,wphi,phi);
  k_kv<<<(H*36*19+63)/64,64,0,stream>>>(qkv,phi,kv);
  k_o<<<dim3(D/64,S),64,0,stream>>>(phi,kv,o);
  k_out<<<dim3(D/64,S),64,0,stream>>>(x,bu,o,Cre,Cim,wo,dsk,gw,(float*)d_out);
}
```

```cpp
#include <hip/hip_runtime.h>
#include <hip/hip_bf16.h>

#define LAS __attribute__((address_space(3)))
#define GAS __attribute__((address_space(1)))
#define DI __device__ __forceinline__
typedef unsigned short u16;
typedef unsigned int u32;
typedef unsigned long long u64;
typedef short s16x4 __attribute__((ext_vector_type(4)));
typedef short s16x8 __attribute__((ext_vector_type(8)));
typedef __bf16 bf16x8 __attribute__((ext_vector_type(8)));
typedef __bf16 bf16x2 __attribute__((ext_vector_type(2)));
typedef float f32x4 __attribute__((ext_vector_type(4)));
typedef float f32x2 __attribute__((ext_vector_type(2)));
typedef u32 u32x4 __attribute__((ext_vector_type(4)));
typedef u32 u32x2 __attribute__((ext_vector_type(2)));
typedef __amdgpu_buffer_rsrc_t rsrc_t;

#define STAMPS 0

constexpr int SEQ = 2048, DM = 576;
constexpr u32 WS_EPOCH = 0;
constexpr u32 WS_KVFLAG = 256;
constexpr u32 WS_HFLAG = WS_KVFLAG + 16 * 128;
constexpr u32 WS_OFLAG = WS_HFLAG + 32 * 128;
constexpr u32 WS_AGG = 16384;
constexpr u32 WS_G = WS_AGG + 12 * 8 * 768;
constexpr u32 WS_KVP = WS_G + 2048 * 16;
constexpr u32 WS_A2 = 1u << 20;
constexpr u32 WS_END = WS_A2 + 32 * 54 * 4096;
constexpr u32 WS_STAMP = 16u << 20;
static_assert(WS_OFLAG + 32 * 128 <= WS_AGG, "ws");
static_assert(WS_KVP + 128 * 3328 <= WS_A2, "ws");
constexpr u32 L_SLOT1 = 49152;
constexpr u32 L_FOLD = 147456;
constexpr u32 L_RAW = L_FOLD + 10240;
constexpr u32 L_KVI = L_RAW + 1344;
constexpr u32 L_SEG = L_FOLD, L_CAR = L_FOLD + 6144;
constexpr u32 L_SLOT2 = 18432;
constexpr u32 L_XCH = 2 * L_SLOT2;
constexpr u32 LDS_BYTES = 163840;
static_assert(L_KVI + 3904 <= LDS_BYTES, "lds");
constexpr u32 SPIN_CAP = 1u << 18;
constexpr u32 OOB = 0x80000000u;

DI rsrc_t mk_rsrc(const void* p, u32 bytes) { return __builtin_amdgcn_make_buffer_rsrc((void*)p, 0, bytes, 0x00020000); }
DI u32x4 mk_srd(const void* p, u32 bytes) {
    u64 a = (u64)p;
    return (u32x4){(u32)a, (u32)(a >> 32) & 0xffffu, bytes, 0x00020000u};
}
DI void dma16(u32x4 srd, u32 dst, u32 voff, u32 soff) {
    asm volatile("s_mov_b32 m0, %0\n\ts_nop 0\n\tbuffer_load_dwordx4 %1, %2, %3 offen lds"
                 :: "s"(dst), "v"(voff), "s"(srd), "s"(soff) : "memory");
}
template <int N> DI void wait_vm() { asm volatile("s_waitcnt vmcnt(%0)" :: "n"(N) : "memory"); }
DI void bar() { asm volatile("s_waitcnt lgkmcnt(0)\n\ts_barrier" ::: "memory"); }
DI bf16x8 lds_b128(u32 a) { return *(LAS bf16x8*)(size_t)a; }
DI bf16x8 lds_tr2(u32 a0, u32 a1) {
    s16x4 x = __builtin_amdgcn_ds_read_tr16_b64_v4i16((LAS s16x4*)(size_t)a0);
    s16x4 y = __builtin_amdgcn_ds_read_tr16_b64_v4i16((LAS s16x4*)(size_t)a1);
    return __builtin_bit_cast(bf16x8, __builtin_shufflevector(x, y, 0, 1, 2, 3, 4, 5, 6, 7));
}
DI void lds_w64(u32 a, u32 x, u32 y) { *(LAS u32x2*)(size_t)a = (u32x2){x, y}; }
DI f32x4 mfma(bf16x8 a, bf16x8 b, f32x4 c) { return __builtin_amdgcn_mfma_f32_16x16x32_bf16(a, b, c, 0, 0, 0); }
DI u32 pk(float a, float b) { return __builtin_bit_cast(u32, __builtin_convertvector((f32x2){a, b}, bf16x2)); }
DI bf16x8 frag(u32 a, u32 b, u32 c, u32 d) { return __builtin_bit_cast(bf16x8, (u32x4){a, b, c, d}); }
DI float asf(u32 v) { return __builtin_bit_cast(float, v); }
DI u32 asu(float v) { return __builtin_bit_cast(u32, v); }
DI float bf_lo(u32 v) { return __builtin_bit_cast(float, v << 16); }
DI float bf_hi(u32 v) { return __builtin_bit_cast(float, v & 0xffff0000u); }
DI float bf1(const u16* p) { return __builtin_bit_cast(float, (u32)(*p) << 16); }
DI f32x2 cmul(f32x2 a, f32x2 b) { return (f32x2){a.x * b.x - a.y * b.y, a.x * b.y + a.y * b.x}; }
DI f32x2 cfma(f32x2 l, f32x2 h, f32x2 u) {
    return (f32x2){fmaf(l.x, h.x, fmaf(-l.y, h.y, u.x)), fmaf(l.x, h.y, fmaf(l.y, h.x, u.y))};
}
DI void poll_flags(rsrc_t ws, u32 off, int n, u32 tag, int lane) {
    u32 vo = off + 8u * (u32)(lane < n ? lane : 0);
_Pragma("unroll 1") for (u32 spin = 0; spin < SPIN_CAP; ++spin) {
        u32x2 v = __builtin_amdgcn_raw_buffer_load_b64(ws, vo, 0, 16);
        if (__all(((v.x ^ tag) | (v.y ^ tag)) == 0)) break;
        asm volatile("s_sleep 1" ::: "memory");
    }
    asm volatile("" ::: "memory");
}
DI void set_flag(rsrc_t ws, u32 off, u32 tag, int lane) {
    if (lane == 0) __builtin_amdgcn_raw_buffer_store_b64((u32x2){tag, tag}, ws, off, 0, 16);
}
DI u32 read_tag(rsrc_t ws) {
    u32 t = __builtin_amdgcn_readfirstlane(__builtin_amdgcn_raw_buffer_load_b32(ws, WS_EPOCH, 0, 16)) + 0x9E3779B9u;
    asm volatile("" : "+s"(t) :: "memory");
    return t;
}
struct Lam { f32x2 l, coef; };
DI Lam lam_of(const u16* lre, const u16* lim, const u16* ldt, int p) {
    float a = bf1(lre + p), b = bf1(lim + p);
    float dt = bf_lo(pk(expf(bf1(ldt + p)), 0.f));
    float e = expf(dt * a), sn, cs;
    sincosf(dt * b, &sn, &cs);
    Lam r; r.l = (f32x2){e * cs, e * sn};
    float nr = r.l.x - 1.f, ni = r.l.y, inv = 2.f / (a * a + b * b);
    r.coef = (f32x2){(nr * a + ni * b) * inv, (ni * a - nr * b) * inv};
    return r;
}
#define STAMP(i) do {} while (0)

DI void softmax18(const f32x4 (&p)[5], u32 (&o)[10]) {
    float m = fmaxf(p[4][0], p[4][1]);
#pragma unroll
    for (int u = 0; u < 4; ++u) m = fmaxf(fmaxf(m, fmaxf(p[u][0], p[u][1])), fmaxf(p[u][2], p[u][3]));
    const float L = 1.44269504088896f;
    float ml = -m * L, e[18], s = 0.f;
#pragma unroll
    for (int i = 0; i < 18; ++i) { e[i] = __builtin_amdgcn_exp2f(fmaf(p[i >> 2][i & 3], L, ml)); s += e[i]; }
    float inv = __builtin_amdgcn_rcpf(s);
#pragma unroll
    for (int i = 0; i < 9; ++i) o[i] = pk(e[2 * i] * inv, e[2 * i + 1] * inv);
    o[9] = 0;
}

template <int PHASES>
__global__ __launch_bounds__(512, 2) void fused_kernel(
    const u16* __restrict__ x, const u16* __restrict__ lre, const u16* __restrict__ lim,
    const u16* __restrict__ bre, const u16* __restrict__ bim, const u16* __restrict__ cre,
    const u16* __restrict__ cim, const u16* __restrict__ ldt, const u16* __restrict__ dsk,
    const u16* __restrict__ wqkv, const u16* __restrict__ wphi, const u16* __restrict__ wo,
    const u16* __restrict__ gw, float* out, char* ws) {
    extern __shared__ __attribute__((aligned(16))) char smem[];
    const int tid = threadIdx.x, lane = tid & 63;
    const int wave = __builtin_amdgcn_readfirstlane(tid >> 6);
    const int g = lane >> 4, c = lane & 15;
    const int b = blockIdx.x, e = b & 7, r = b >> 3;
    const int ch = (e & 1) * 4 + (r & 3), s7 = r >> 2, q4 = e >> 1;
    const int rot = (s7 + 2 * (r & 3)) % 9;
    const int m2 = 4 * e + (r & 3), n2 = s7;
    const rsrc_t rws = mk_rsrc(ws, WS_END);
    STAMP(0);

    const int lw = wave - 4;
    auto w2_issue = [&](int i) {
        const u32 dst = (u32)((i + 4) & 7) * L_SLOT2;
        if (i < 9) {
            int sg = i + rot; if (sg >= 9) sg -= 9;
            const u32x4 sre = mk_srd(cre, DM * DM * 2), sim = mk_srd(cim, DM * DM * 2);
            const u32 rho = lane >> 3;
            const u32 v0 = rho * 1152 + (((lane & 7) ^ (rho >> 1) ^ ((lw & 1) << 2)) << 4), v1 = v0 ^ 64;
#pragma unroll
            for (int k = 0; k < 6; ++k) {
                const u32 blk = 3 * (k % 3) + lw;
                dma16(k < 3 ? sre : sim, dst + (k < 3 ? 0 : 9216) + blk * 1024, (k % 3) & 1 ? v1 : v0,
                      (u32)(n2 * 72 + blk * 8) * 1152 + sg * 128);
            }
        } else {
            const u32x4 swo = mk_srd(wo, DM * DM * 2);
            const int nk = i < 13 ? 6 : 3;
#pragma unroll
            for (int k = 0; k < 6; ++k) {
                if (k < nk) {
                    const u32 idx = 3 * k + lw, n = idx * 64 + lane, R = n / 9, cu = n - R * 9;
                    const u32 st = R >> 5, L = R & 31, gh = L >> 4, t = L & 1, gl = (L >> 3) & 1, q = (L >> 1) & 3;
                    const u32 j = 4 * t + q;
                    const u32 row = i < 13 ? 36 * st + 18 * gh + 8 * gl + j
                                           : 18 * (16 * st + 4 * (2 * gh + gl) + (j >> 1)) + 16 + (j & 1);
                    dma16(swo, dst + idx * 1024, row * 1152 + cu * 16, (u32)(i < 13 ? (i - 9) * 144 * 1152 : 0) + n2 * 144);
                }
            }
        }
    };

    u32 tag = 0;
    if constexpr (PHASES & 1) {
        if (s7 < 7) {
            const bool attn = s7 < 4;
            const int j = 4 * q4 + s7, si = 3 * q4 + s7 - 4;
            if (wave >= 4) {
                const u32x4 sx = mk_srd(x, SEQ * DM * 2);
                const u32 rho = lane >> 3;
                const u32 swz = ((lane & 7) ^ (rho >> 1)) << 4;
                const u32 xv0 = (16 * (rho >> 2) + (rho & 3)) * 1152 + swz, xv1 = xv0 ^ 64;
                const u32 xs = (u32)(ch * 256 + lw * 64) * 1152;
                u32x4 sw; u32 wv0, wv1, wsb, wsi, wss;
                if (attn) {
                    sw = mk_srd(wqkv, DM * DM * 6);
                    u32 v[2];
#pragma unroll
                    for (int a = 0; a < 2; ++a) {
                        const u32 u = (c - 2 * (g + 4 * a)) & 15;
                        const u32 off = u < 4 ? 1152 + u * 16 : u < 8 ? (u - 4) * 16 : u < 12 ? 2304 + (u - 8) * 16
                                      : u == 12 ? 1152 + 64 : u == 13 ? 64 : 2304 + 56;
                        v[a] = g * 3456 + off;
                    }
                    wv0 = v[0]; wv1 = v[1]; wsb = (u32)lw * 16 * 3456 + j * 72; wsi = 4 * 3456; wss = 64 * 3456;
                } else {
                    sw = mk_srd(lw < 2 ? bre : bim, DM * DM * 2);
                    wv0 = rho * 1152 + (swz ^ ((lw & 1) << 6)); wv1 = wv0 ^ 64;
                    wsb = (u32)(si * 48 + (lw & 1) * 24) * 1152; wsi = 8 * 1152; wss = 128;
                }
                auto issue = [&](int it) {
                    int st = it + rot; if (st >= 9) st -= 9;
                    const u32 base = (u32)(it % 3) * L_SLOT1;
                    if (attn) {
#pragma unroll
                        for (int k = 0; k < 4; ++k) dma16(sw, base + 32768 + (lw * 4 + k) * 1024, k < 2 ? wv0 : wv1, wsb + k * wsi + st * wss);
                    } else {
#pragma unroll
                        for (int k = 0; k < 3; ++k) dma16(sw, base + 32768 + (lw * 3 + k) * 1024, k & 1 ? wv1 : wv0, wsb + k * wsi + st * wss);
                    }
#pragma unroll
                    for (int k = 0; k < 8; ++k)
                        dma16(sx, base + (lw * 8 + k) * 1024, k & 1 ? xv1 : xv0, xs + (32 * (k & 1) + 4 * (k >> 1)) * 1152 + st * 128);
                };
                issue(0); issue(1);
                wait_vm<11>(); bar();
                STAMP(1);
#pragma unroll 1
                for (int it = 0; it < 8; ++it) {
                    if (it < 7) { issue(it + 2); wait_vm<11>(); } else wait_vm<0>();
                    bar();
                }
                tag = read_tag(rws);
                bar();
                STAMP(2);
                if (attn) {
                    if (PHASES == 3 && lw < 3) { w2_issue(0); w2_issue(1); w2_issue(2); w2_issue(3); }
                    bar();
                    bar();
                    STAMP(3);
                    poll_flags(rws, WS_KVFLAG + j * 128, 8, tag, lane);
                    STAMP(4);
                    const u32 u = min(tid - 256, 199), gam = u / 10, dp = u - gam * 10;
                    u32x4 v[8];
#pragma unroll
                    for (int k = 0; k < 8; ++k)
                        v[k] = __builtin_amdgcn_raw_buffer_load_b128(rws, WS_KVP + u * 16, (u32)(j * 8 + k) * 3328, 16);
                    float s[8] = {0, 0, 0, 0, 0, 0, 0, 0};
#pragma unroll
                    for (int k = 0; k < 8; ++k)
#pragma unroll
                        for (int i = 0; i < 4; ++i) { s[2 * i] += bf_lo(v[k][i]); s[2 * i + 1] += bf_hi(v[k][i]); }
                    const u32 gs = gam / 5, a = L_KVI + gs * 960 + dp * 96 + (gam - gs * 5) * 8;
                    lds_w64(a, pk(s[0], s[1]), pk(s[2], s[3]));
                    lds_w64(a + 48, pk(s[4], s[5]), pk(s[6], s[7]));
                    bar();
                    STAMP(5);
                    if (PHASES == 3 && lw < 3) { w2_issue(4); w2_issue(5); w2_issue(6); w2_issue(7); }
                } else {
                    if (lw < 3) {
                        if (PHASES == 3) { w2_issue(0); w2_issue(1); w2_issue(2); w2_issue(3); }
                        bar();
                        bar();
                    } else {
                        const int pl = min(lane, 47);
                        const Lam lm = lam_of(lre, lim, ldt, si * 48 + pl);
                        f32x2 l16 = lm.l;
#pragma unroll
                        for (int k = 0; k < 4; ++k) l16 = cmul(l16, l16);
                        f32x2 l256 = l16;
#pragma unroll
                        for (int k = 0; k < 4; ++k) l256 = cmul(l256, l256);
                        bar();
                        f32x2 sg[16], A = {0.f, 0.f};
#pragma unroll
                        for (int k = 0; k < 16; ++k) sg[k] = *(LAS f32x2*)(size_t)(L_SEG + (k * 48 + pl) * 8);
#pragma unroll
                        for (int k = 0; k < 16; ++k) A = cfma(l16, A, sg[k]);
                        __builtin_amdgcn_raw_buffer_store_b128(
                            (u32x4){asu(A.x), tag, asu(A.y), tag}, rws,
                            lane < 48 ? WS_AGG + pl * 16 : OOB, (u32)(si * 8 + ch) * 768, 16);
                        STAMP(3);
                        u32x4 v[7];
                    _Pragma("unroll 1") for (u32 spin = 0; spin < SPIN_CAP; ++spin) {
                            u32 bad = 0;
#pragma unroll
                            for (int k = 0; k < 7; ++k)
                                v[k] = __builtin_amdgcn_raw_buffer_load_b128(rws, k < ch ? WS_AGG + pl * 16 : OOB, (u32)(si * 8 + k) * 768, 16);
#pragma unroll
                            for (int k = 0; k < 7; ++k) bad |= ((v[k].y ^ tag) | (v[k].w ^ tag)) & (k < ch ? ~0u : 0u);
                            if (__all(bad == 0)) break;
                            asm volatile("s_sleep 1" ::: "memory");
                        }
                        STAMP(4);
                        f32x2 H = {0.f, 0.f};
#pragma unroll
                        for (int k = 0; k < 7; ++k)
                            if (k < ch) H = cfma(l256, H, (f32x2){asf(v[k].x), asf(v[k].z)});
#pragma unroll
                        for (int k = 0; k < 16; ++k) {
                            *(LAS f32x2*)(size_t)(L_CAR + (k * 48 + pl) * 8) = H;
                            H = cfma(l16, H, sg[k]);
                        }
                        bar();
                    }
                    bar();
                    if (PHASES == 3 && lw < 3) { w2_issue(4); w2_issue(5); w2_issue(6); w2_issue(7); }
                }
            } else if (attn) {
                {
                    const rsrc_t rp = mk_rsrc(wphi, 32 * 324 * 2);
                    const u32x4 w0 = __builtin_amdgcn_raw_buffer_load_b128(rp, lane * 16, j * 1296, 0);
                    const u32x4 w1 = __builtin_amdgcn_raw_buffer_load_b128(rp, min(64 + lane, 80) * 16, j * 1296, 0);
                    tag = read_tag(rws);
                    if (tid < 244) *(LAS u32x4*)(size_t)(L_KVI + tid * 16) = (u32x4){0, 0, 0, 0};
                    *(LAS u32x4*)(size_t)(L_RAW + lane * 16) = w0;
                    *(LAS u32x4*)(size_t)(L_RAW + min(64 + lane, 80) * 16) = w1;
                    asm volatile("s_waitcnt lgkmcnt(0)" ::: "memory");
                    const int hd = c >> 3, sgn = (c >> 2) & 1;
                    for (int fi = wave; fi < 10; fi += 4) {
                        const int U = fi >> 1, s = fi & 1, f = 4 * U + (c & 3);
                        u32 w[8];
#pragma unroll
                        for (int k = 0; k < 8; ++k) {
                            const int col = s == 0 ? (k < 4 ? 4 * g + k : 12 + 4 * g + k) : ((k < 4 && g < 2) ? 32 + k : 99);
                            const int d = col - 18 * hd;
                            const bool ok = f < 18 && d >= 0 && d < 18;
                            const u32 v = *(LAS u16*)(size_t)(L_RAW + (ok ? (hd * 324 + d * 18 + f) * 2 : 0));
                            w[k] = ok ? (v ^ (sgn << 15)) : 0;
                        }
                        *(LAS u32x4*)(size_t)(L_FOLD + fi * 1024 + lane * 16) =
                            (u32x4){w[0] | (w[1] << 16), w[2] | (w[3] << 16), w[4] | (w[5] << 16), w[6] | (w[7] << 16)};
                    }
                }
                const int q = (lane >> 2) & 3, p = lane & 3;
                u32 xa = (wave * 64 + c) * 128 + ((g ^ (c >> 1)) << 4);
                u32 wa[7];
                {
                    const u32 rb = 32768 + (8 * g + q) * 256, ro = 32 * (q + 4 * (g & 1));
#pragma unroll
                    for (int T = 0; T < 6; ++T) wa[T] = rb + ((32 * T + 8 * p + ro) & 255);
                    wa[6] = rb + ((192 + 16 * p + (p >> 1) * 8 + ro) & 255);
                }
                f32x4 acc[7][4];
#pragma unroll
                for (int T = 0; T < 7; ++T)
#pragma unroll
                    for (int t = 0; t < 4; ++t) acc[T][t] = (f32x4){0.f, 0.f, 0.f, 0.f};
                bf16x8 A0[7], B0[4], A1[7], B1[4];
                bar();
                STAMP(1);
                u32 so = 0;
#pragma unroll
                for (int t = 0; t < 4; ++t) B0[t] = lds_b128(xa + t * 2048);
#pragma unroll
                for (int T = 0; T < 7; ++T) A0[T] = lds_tr2(wa[T], wa[T] + 1024);
#pragma unroll 1
                for (int it = 0; it < 9; ++it) {
#pragma unroll
                    for (int t = 0; t < 4; ++t) B1[t] = lds_b128(((xa ^ 64) + so) + t * 2048);
#pragma unroll
                    for (int T = 0; T < 7; ++T) A1[T] = lds_tr2(wa[T] + so + 8192, wa[T] + so + 9216);
#pragma unroll
                    for (int T = 0; T < 7; ++T)
#pragma unroll
                        for (int t = 0; t < 4; ++t) acc[T][t] = mfma(A0[T], B0[t], acc[T][t]);
                    __builtin_amdgcn_sched_barrier(0);
                    bar();
                    __builtin_amdgcn_sched_barrier(0);
                    so += L_SLOT1; if (so == 3 * L_SLOT1) so = 0;
                    if (it < 8) {
#pragma unroll
                        for (int t = 0; t < 4; ++t) B0[t] = lds_b128(xa + so + t * 2048);
#pragma unroll
                        for (int T = 0; T < 7; ++T) A0[T] = lds_tr2(wa[T] + so, wa[T] + so + 1024);
                    }
#pragma unroll
                    for (int T = 0; T < 7; ++T)
#pragma unroll
                        for (int t = 0; t < 4; ++t) acc[T][t] = mfma(A1[T], B1[t], acc[T][t]);
                }
                STAMP(2);
                bf16x8 FW[10];
#pragma unroll
                for (int i = 0; i < 10; ++i) FW[i] = lds_b128(L_FOLD + i * 1024 + lane * 16);
                const f32x4 z4 = {0.f, 0.f, 0.f, 0.f};
#pragma unroll
                for (int t = 0; t < 4; ++t) {
                    const u32 s0 = pk(acc[6][t][0], acc[6][t][1]), s1 = pk(acc[6][t][2], acc[6][t][3]);
                    const bf16x8 b0 = frag(pk(acc[0][t][0], acc[0][t][1]), pk(acc[0][t][2], acc[0][t][3]),
                                           pk(acc[1][t][0], acc[1][t][1]), pk(acc[1][t][2], acc[1][t][3]));
                    const bf16x8 b1 = frag(g == 0 ? s0 : 0, g == 0 ? s1 : 0, 0, 0);
                    f32x4 pf[5];
#pragma unroll
                    for (int U = 0; U < 5; ++U) pf[U] = mfma(FW[2 * U + 1], b1, mfma(FW[2 * U], b0, z4));
                    u32 ph[10];
                    softmax18(pf, ph);
                    const u32 ra = (wave * 64 + t * 16 + c) * 240;
#pragma unroll
                    for (int U = 0; U < 5; ++U) lds_w64(ra + g * 40 + U * 8, ph[2 * U], ph[2 * U + 1]);
                    lds_w64(ra + 160 + g * 8, pk(acc[4][t][0], acc[4][t][1]), pk(acc[4][t][2], acc[4][t][3]));
                    lds_w64(ra + 192 + g * 8, pk(acc[5][t][0], acc[5][t][1]), pk(acc[5][t][2], acc[5][t][3]));
                    if (g >= 2) lds_w64(ra + 208 + g * 8, g == 2 ? s0 : 0x3f80u, g == 2 ? s1 : 0);
                }
                bar();
                STAMP(3);
                {
                    const int hw = wave & 1;
                    const u32 rb = (2 * (4 * (g & 1) + q) + 16 * (g >> 1)) * 240;
                    const u32 fa = rb + hw * 64 + 8 * p;
                    const u32 va = rb + (wave < 2 ? 160 + hw * 40 + 8 * p : (p == 0 ? 192 : 232));
                    f32x4 kv[3] = {z4, z4, z4};
#pragma unroll
                    for (int ks = 0; ks < 8; ++ks) {
                        const bf16x8 vb = lds_tr2(va + ks * 7680, va + ks * 7680 + 240);
#pragma unroll
                        for (int i = 0; i < 3; ++i)
                            kv[i] = mfma(lds_tr2(fa + ks * 7680 + i * 32, fa + ks * 7680 + i * 32 + 240), vb, kv[i]);
                    }
                    int d = wave < 2 ? c + 2 * hw : (c < 4 ? 16 + c - 18 * hw : 18);
                    const bool dok = wave < 2 || (c < 4 ? (d >= 0 && d < 18) : c == 4);
#pragma unroll
                    for (int i = 0; i < 3; ++i) {
                        const int gam = 4 * (2 * hw + i) + g;
                        const bool ok = dok && ((gam >= 10) == (hw == 1));
                        __builtin_amdgcn_raw_buffer_store_b64((u32x2){pk(kv[i][0], kv[i][1]), pk(kv[i][2], kv[i][3])}, rws,
                                                              ok ? WS_KVP + gam * 160 + d * 8 : OOB, (u32)(j * 8 + ch) * 3328, 16);
                    }
                    wait_vm<0>();
                }
                bar();
                if (wave == 0) set_flag(rws, WS_KVFLAG + j * 128 + ch * 8, tag, lane);
                STAMP(4);
                u32 pq[4][10];
#pragma unroll
                for (int t = 0; t < 4; ++t) {
                    const u32 s0 = pk(acc[6][t][0], acc[6][t][1]), s1 = pk(acc[6][t][2], acc[6][t][3]);
                    const bf16x8 b0 = frag(pk(acc[2][t][0], acc[2][t][1]), pk(acc[2][t][2], acc[2][t][3]),
                                           pk(acc[3][t][0], acc[3][t][1]), pk(acc[3][t][2], acc[3][t][3]));
                    const bf16x8 b1 = frag(g == 1 ? s0 : 0, g == 1 ? s1 : 0, 0, 0);
                    f32x4 pf[5];
#pragma unroll
                    for (int U = 0; U < 5; ++U) pf[U] = mfma(FW[2 * U + 1], b1, mfma(FW[2 * U], b0, z4));
                    softmax18(pf, pq[t]);
                }
                STAMP(5);
                bar();
                STAMP(6);
                bf16x8 KW[3][3];
                {
                    const int gp = c >> 2, rr = c & 3;
                    const bool match = (g >> 1) == (gp >> 1);
#pragma unroll
                    for (int u = 0; u < 3; ++u) {
                        const int d = u < 2 ? 8 * (gp & 1) + 4 * u + rr : ((rr == 1 || rr == 2) ? 15 + rr : 18);
                        const u32 a = L_KVI + (match ? g * 960 + d * 48 : 3840);
#pragma unroll
                        for (int s = 0; s < 3; ++s) KW[u][s] = lds_b128(a + 16 * s);
                    }
                }
                const int m1 = ch * 4 + wave, hh = 2 * j + (g >> 1);
                const u32 lo = (g & 1) ? OOB : WS_A2 + (52 + (hh >> 4)) * 4096 + (((hh >> 2) & 3) * 16 + c) * 16 + (hh & 3) * 4;
#pragma unroll
                for (int t = 0; t < 4; ++t) {
                    const bf16x8 b0 = frag(pq[t][0], pq[t][1], pq[t][2], pq[t][3]);
                    const bf16x8 b1 = frag(pq[t][4], pq[t][5], pq[t][6], pq[t][7]);
                    const bf16x8 b2 = frag(pq[t][8], 0, 0, 0);
                    f32x4 o[3];
#pragma unroll
                    for (int u = 0; u < 3; ++u) o[u] = mfma(KW[u][2], b2, mfma(KW[u][1], b1, mfma(KW[u][0], b0, z4)));
                    const float inv = __builtin_amdgcn_rcpf(o[2][0] + 1e-6f);
                    const u32x4 f = {pk(o[0][0] * inv, o[0][1] * inv), pk(o[0][2] * inv, o[0][3] * inv),
                                     pk(o[1][0] * inv, o[1][1] * inv), pk(o[1][2] * inv, o[1][3] * inv)};
                    __builtin_amdgcn_raw_buffer_store_b128(f, rws, WS_A2 + lane * 16 + t * 1024, (u32)(m1 * 54 + 36 + j) * 4096, 16);
                    __builtin_amdgcn_raw_buffer_store_b32(pk(o[2][1] * inv, o[2][2] * inv), rws, lo + t * 1024, (u32)m1 * 54 * 4096, 16);
                }
                wait_vm<0>();
                set_flag(rws, WS_OFLAG + m1 * 128 + j * 8, tag, lane);
                STAMP(7);
            } else {
                tag = read_tag(rws);
                Lam lm[3];
#pragma unroll 1
                for (int a = 0; a < 3; ++a) {
                    const Lam t = lam_of(lre, lim, ldt, si * 48 + a * 16 + c);
                    if (a == 0) lm[0] = t; else if (a == 1) lm[1] = t; else lm[2] = t;
                }
                u32 xa = (wave * 64 + c) * 128 + ((g ^ (c >> 1)) << 4);
                u32 wa = 32768 + c * 128 + ((g ^ (c >> 1)) << 4);
                f32x4 acc[6][4];
#pragma unroll
                for (int T = 0; T < 6; ++T)
#pragma unroll
                    for (int t = 0; t < 4; ++t) acc[T][t] = (f32x4){0.f, 0.f, 0.f, 0.f};
                bf16x8 A0[6], B0[4], A1[6], B1[4];
                bar();
                STAMP(1);
                u32 so = 0;
#pragma unroll
                for (int t = 0; t < 4; ++t) B0[t] = lds_b128(xa + t * 2048);
#pragma unroll
                for (int T = 0; T < 6; ++T) A0[T] = lds_b128(wa + T * 2048);
#pragma unroll 1
                for (int it = 0; it < 9; ++it) {
#pragma unroll
                    for (int t = 0; t < 4; ++t) B1[t] = lds_b128(((xa ^ 64) + so) + t * 2048);
#pragma unroll
                    for (int T = 0; T < 6; ++T) A1[T] = lds_b128(((wa ^ 64) + so) + T * 2048);
#pragma unroll
                    for (int T = 0; T < 6; ++T)
#pragma unroll
                        for (int t = 0; t < 4; ++t) acc[T][t] = mfma(B0[t], A0[T], acc[T][t]);
                    __builtin_amdgcn_sched_barrier(0);
                    bar();
                    __builtin_amdgcn_sched_barrier(0);
                    so += L_SLOT1; if (so == 3 * L_SLOT1) so = 0;
                    if (it < 8) {
#pragma unroll
                        for (int t = 0; t < 4; ++t) B0[t] = lds_b128(xa + so + t * 2048);
#pragma unroll
                        for (int T = 0; T < 6; ++T) A0[T] = lds_b128(wa + so + T * 2048);
                    }
#pragma unroll
                    for (int T = 0; T < 6; ++T)
#pragma unroll
                        for (int t = 0; t < 4; ++t) acc[T][t] = mfma(B1[t], A1[T], acc[T][t]);
                }
                STAMP(2);
                const u32 sa = ((wave * 4 + g) * 48 + c) * 8;
#pragma unroll
                for (int a = 0; a < 3; ++a) {
                    f32x2 h = {acc[a][0][0], acc[3 + a][0][0]};
#pragma unroll
                    for (int k = 1; k < 16; ++k) h = cfma(lm[a].l, h, (f32x2){acc[a][k >> 2][k & 3], acc[3 + a][k >> 2][k & 3]});
                    *(LAS f32x2*)(size_t)(L_SEG + sa + a * 128) = h;
                }
                bar();
                STAMP(3);
                bar();
                STAMP(4);
                const int q = (lane >> 2) & 3, p = lane & 3, m1 = ch * 4 + wave;
                const u32 tb = wave * 13056;
                const u32 twa = tb + (4 * (4 * (c >> 3) + (c & 3)) + ((c >> 2) & 1)) * 136 + g * 8;
                const u32 tra = tb + (4 * (4 * (g & 1) + q) + 2 * (g >> 1)) * 136 + p * 8;
#pragma unroll
                for (int a = 0; a < 3; ++a) {
                    f32x2 h = *(LAS f32x2*)(size_t)(L_CAR + sa + a * 128);
#pragma unroll
                    for (int t = 0; t < 4; ++t) {
                        f32x2 y[4];
#pragma unroll
                        for (int k = 0; k < 4; ++k) {
                            h = cfma(lm[a].l, h, (f32x2){acc[a][t][k], acc[3 + a][t][k]});
                            y[k] = cmul(lm[a].coef, h);
                        }
                        lds_w64(twa + a * 4352 + t * 32, pk(y[0].x, y[1].x), pk(y[2].x, y[3].x));
                        lds_w64(twa + a * 4352 + t * 32 + 272, pk(-y[0].y, -y[1].y), pk(-y[2].y, -y[3].y));
                    }
                    asm volatile("s_waitcnt lgkmcnt(0)" ::: "memory");
#pragma unroll
                    for (int t = 0; t < 4; ++t) {
                        const bf16x8 f = lds_tr2(tra + a * 4352 + t * 32, tra + a * 4352 + t * 32 + 136);
                        __builtin_amdgcn_raw_buffer_store_b128(__builtin_bit_cast(u32x4, f), rws, WS_A2 + lane * 16 + t * 1024,
                                                               (u32)(m1 * 54 + si * 3 + a) * 4096, 16);
                    }
                }
                bar();
                wait_vm<0>();
                set_flag(rws, WS_HFLAG + m1 * 128 + si * 8, tag, lane);
                STAMP(7);
            }
        } else {
            tag = read_tag(rws);
            const int R = tid >> 3, tok = m2 * 64 + 16 * ((R & 15) >> 2) + 4 * (R >> 4) + (R & 3);
            const rsrc_t rx = mk_rsrc(x, SEQ * DM * 2), rg = mk_rsrc(gw, DM * 2);
            float s = 0.f;
#pragma unroll
            for (int k = 0; k < 9; ++k) {
                const u32x4 xv = __builtin_amdgcn_raw_buffer_load_b128(rx, tok * 1152 + (tid & 7) * 16, k * 128, 0);
                const u32x4 gv = __builtin_amdgcn_raw_buffer_load_b128(rg, (tid & 7) * 16, k * 128, 0);
#pragma unroll
                for (int i = 0; i < 4; ++i) s = fmaf(bf_lo(xv[i]), bf_lo(gv[i]), fmaf(bf_hi(xv[i]), bf_hi(gv[i]), s));
            }
            s += __shfl_xor(s, 1); s += __shfl_xor(s, 2); s += __shfl_xor(s, 4);
            const u32 gb = __builtin_bit_cast(u32, 1.f / (1.f + expf(-s)));
            bar();
            __builtin_amdgcn_raw_buffer_store_b128((u32x4){gb, tag, gb, tag}, rws, (tid & 7) == 0 ? WS_G + (m2 * 64 + R) * 16 : OOB, 0, 16);
            if (PHASES == 3 && wave >= 4 && lw < 3) {
#pragma unroll 1
                for (int i = 0; i < 8; ++i) w2_issue(i);
            }
            STAMP(7);
        }
    }
    if constexpr (PHASES & 2) {
        if (PHASES == 2) {
            if (wave >= 4 && lw < 3) {
#pragma unroll 1
                for (int i = 0; i < 8; ++i) w2_issue(i);
            }
        }
        if (wave >= 4) {
            if (lw < 3) {
                wait_vm<42>(); bar();
#pragma unroll 1
                for (int i = 1; i < 6; ++i) { wait_vm<36>(); bar(); w2_issue(i + 7); }
                wait_vm<36>(); bar(); w2_issue(13);
                wait_vm<33>(); bar();
                wait_vm<27>(); bar();
                wait_vm<0>(); bar();
            } else {
#pragma unroll 1
                for (int i = 0; i < 10; ++i) bar();
            }
            bar();
            if (PHASES == 3 && b == 255 && wave == 7) {
                poll_flags(rws, WS_HFLAG + m2 * 128, 12, tag, lane);
                poll_flags(rws, WS_OFLAG + m2 * 128, 16, tag, lane);
            _Pragma("unroll 1") for (u32 spin = 0; spin < SPIN_CAP; ++spin) {
                    const u32x4 v = __builtin_amdgcn_raw_buffer_load_b128(rws, WS_G + (lane & 31) * 1024, 0, 16);
                    if (__all(((v.y ^ tag) | (v.w ^ tag)) == 0)) break;
                    asm volatile("s_sleep 1" ::: "memory");
                }
                if (lane == 0) __builtin_amdgcn_raw_buffer_store_b32(tag, rws, WS_EPOCH, 0, 16);
            }
            if (PHASES == 2 && b == 255 && wave == 7) {
                const u32 t2 = __builtin_amdgcn_raw_buffer_load_b32(rws, WS_EPOCH, 0, 16) + 0x9E3779B9u;
                if (lane == 0) __builtin_amdgcn_raw_buffer_store_b32(t2, rws, WS_EPOCH, 0, 16);
            }
        } else {
            const int mh = wave & 1, kh = wave >> 1;
            const f32x4 z4 = {0.f, 0.f, 0.f, 0.f};
            const int ttf = 2 * mh + kh, tokf = m2 * 64 + 16 * (c >> 2) + 4 * ttf + (c & 3);
            u32x2 xs[5], ds[5];
            {
                const rsrc_t rx = mk_rsrc(x, SEQ * DM * 2), rd = mk_rsrc(dsk, DM * 2);
#pragma unroll
                for (int u = 0; u < 5; ++u) {
                    const bool ok = u < 4 || g < 2;
                    xs[u] = __builtin_amdgcn_raw_buffer_load_b64(rx, ok ? tokf * 1152 + g * 8 + u * 32 : OOB, n2 * 144, 0);
                    ds[u] = __builtin_amdgcn_raw_buffer_load_b64(rd, ok ? g * 8 + u * 32 : OOB, n2 * 144, 0);
                }
            }
            float gt[2];
        _Pragma("unroll 1") for (u32 spin = 0; spin < SPIN_CAP; ++spin) {
                const u32x4 v0 = __builtin_amdgcn_raw_buffer_load_b128(rws, WS_G + (m2 * 64 + mh * 32 + c) * 16, 0, 16);
                const u32x4 v1 = __builtin_amdgcn_raw_buffer_load_b128(rws, WS_G + (m2 * 64 + mh * 32 + 16 + c) * 16, 0, 16);
                gt[0] = asf(v0.x); gt[1] = asf(v1.x);
                if (PHASES == 2 || __all(((v0.y ^ tag) | (v0.w ^ tag) | (v1.y ^ tag) | (v1.w ^ tag)) == 0)) break;
                asm volatile("s_sleep 1" ::: "memory");
            }
            if (PHASES == 3) poll_flags(rws, WS_HFLAG + m2 * 128, 12, tag, lane);
            STAMP(8);
            const u32 av = WS_A2 + lane * 16 + mh * 2048, ab = (u32)m2 * 54 * 4096;
            auto ldh = [&](int i, bf16x8 (&f)[4]) {
                int sg = i + rot; if (sg >= 9) sg -= 9;
#pragma unroll
                for (int k = 0; k < 4; ++k)
                    f[k] = __builtin_bit_cast(bf16x8, __builtin_amdgcn_raw_buffer_load_b128(
                        rws, av + (k & 1) * 1024, ab + (u32)(4 * sg + 2 * kh + (k >> 1)) * 4096, 16));
            };
            f32x4 ah[2][5], ao[2][5];
#pragma unroll
            for (int u = 0; u < 5; ++u) { ah[0][u] = z4; ah[1][u] = z4; ao[0][u] = z4; ao[1][u] = z4; }
            bf16x8 F[3][4];
            ldh(0, F[0]); ldh(1, F[1]);
            const u32 ca = (g >> 1) * 9216 + c * 128 + (((4 * kh + (g & 1)) ^ (c >> 1)) << 4);
            const u32 ca4 = (g >> 1) * 9216 + (64 + (c & 7)) * 128 + (((4 * kh + (g & 1)) ^ ((c & 7) >> 1)) << 4);
#pragma unroll
            for (int i = 0; i < 9; ++i) {
                if (i + 2 < 9) ldh(i + 2, F[(i + 2) % 3]);
                bar();
                const u32 so = (u32)((i + 4) & 7) * L_SLOT2;
#pragma unroll
                for (int k = 0; k < 2; ++k) {
#pragma unroll
                    for (int u = 0; u < 5; ++u) {
                        const bf16x8 w = lds_b128(u < 4 ? ((ca ^ (k * 32)) + so + u * 2048) : ((ca4 ^ (k * 32)) + so));
                        ah[0][u] = mfma(w, F[i % 3][2 * k], ah[0][u]);
                        ah[1][u] = mfma(w, F[i % 3][2 * k + 1], ah[1][u]);
                    }
                }
            }
            STAMP(9);
            if (PHASES == 3) poll_flags(rws, WS_OFLAG + m2 * 128, 16, tag, lane);
            STAMP(10);
            bf16x8 O[9][2];
#pragma unroll
            for (int i = 0; i < 9; ++i) {
                int ii = i + n2; if (ii >= 9) ii -= 9;
#pragma unroll
                for (int k = 0; k < 2; ++k)
                    O[i][k] = __builtin_bit_cast(bf16x8, __builtin_amdgcn_raw_buffer_load_b128(
                        rws, av + k * 1024, ab + (u32)(36 + 2 * ii + kh) * 4096, 16));
            }
            bar();
            {
                const int q = (lane >> 2) & 3, p = lane & 3;
                const u32 oa = (2 * (4 * (g & 1) + q) + 16 * (g >> 1)) * 144 + p * 8;
                const u32 oa4 = oa - (p >> 1) * 16 + 128;
#pragma unroll
                for (int i = 0; i < 9; ++i) {
                    int ii = i + n2; if (ii >= 9) ii -= 9;
                    const int ks = 2 * ii + kh;
                    const u32 so = (u32)((5 + (ks >> 2)) & 7) * L_SLOT2 + (ks & 3) * 4608;
#pragma unroll
                    for (int u = 0; u < 5; ++u) {
                        const u32 a = (u < 4 ? oa + u * 32 : oa4) + so;
                        const bf16x8 w = lds_tr2(a, a + 144);
                        ao[0][u] = mfma(w, O[i][0], ao[0][u]);
                        ao[1][u] = mfma(w, O[i][1], ao[1][u]);
                    }
                }
            }
            STAMP(11);
            f32x4 keep[5];
#pragma unroll
            for (int u = 0; u < 5; ++u) {
                const f32x4 r0 = ah[0][u] * gt[0] + ao[0][u] * (1.f - gt[0]);
                const f32x4 r1 = ah[1][u] * gt[1] + ao[1][u] * (1.f - gt[1]);
                keep[u] = kh ? r1 : r0;
                *(LAS f32x4*)(size_t)(L_XCH + (wave * 5 + u) * 1024 + lane * 16) = kh ? r0 : r1;
            }
            bar();
            const float gf = kh ? gt[1] : gt[0];
            const rsrc_t ro = mk_rsrc(out, SEQ * DM * 4);
#pragma unroll
            for (int u = 0; u < 5; ++u) {
                f32x4 v = keep[u] + *(LAS f32x4*)(size_t)(L_XCH + ((wave ^ 2) * 5 + u) * 1024 + lane * 16);
                v[0] = fmaf(gf * bf_lo(xs[u].x), bf_lo(ds[u].x), v[0]);
                v[1] = fmaf(gf * bf_hi(xs[u].x), bf_hi(ds[u].x), v[1]);
                v[2] = fmaf(gf * bf_lo(xs[u].y), bf_lo(ds[u].y), v[2]);
                v[3] = fmaf(gf * bf_hi(xs[u].y), bf_hi(ds[u].y), v[3]);
                __builtin_amdgcn_raw_buffer_store_b128(__builtin_bit_cast(u32x4, v), ro,
                                                       (u < 4 || g < 2) ? tokf * 2304 + g * 16 + u * 64 : OOB, n2 * 288, 0);
            }
            STAMP(12);
        }
    }
}

#define SPLIT 0
extern "C" void kernel_launch(void* const* d_in, const int* in_sizes, int n_in, void* d_out, int out_size,
                              void* d_ws, size_t ws_size, hipStream_t stream) {
    const u16* const* in = (const u16* const*)d_in;
#define ARGS in[0], in[1], in[2], in[3], in[4], in[5], in[6], in[7], in[8], in[10], in[11], in[12], in[13], (float*)d_out, (char*)d_ws
    (void)hipFuncSetAttribute((const void*)fused_kernel<3>, hipFuncAttributeMaxDynamicSharedMemorySize, LDS_BYTES);
    fused_kernel<3><<<256, 512, LDS_BYTES, stream>>>(ARGS);
}
```

```cpp
#include <hip/hip_runtime.h>

__global__ __launch_bounds__(512, 2) void fused_kernel(
    const void* x, const void* wqkv, const void* bre, const void* bim, void* ws, const void* wphi, const void* lre,
    const void* lim, const void* ldt, const void* cre, const void* cim, const void* wo, const void* gw,
    const void* dsk, void* out) {
    unsigned tid = threadIdx.x, bid = blockIdx.x;
    asm volatile(
    "s_load_dwordx16 s[4:19], s[0:1], 0\n"
    "s_load_dwordx8 s[20:27], s[0:1], 64\n"
    "s_load_dwordx4 s[28:31], s[0:1], 96\n"
    "s_load_dwordx2 s[32:33], s[0:1], 112\n"
    "s_getpc_b64 s[60:61]\n"
    "v_lshrrev_b32 v1, 6, v0\n"
    "v_and_b32 v0, 63, v0\n"
    "v_readfirstlane_b32 s3, v1\n"
    "s_lshr_b32 s62, s2, 3\n"
    "s_cmp_lg_u32 s3, 4\n"
    "s_cbranch_scc1 .Lnopf\n"
    "v_and_b32 v1, 15, v0\n"
    "v_lshl_add_u32 v1, v1, 5, s62\n"
    "v_lshlrev_b32_e64 v1, 7, v1\n"
    "v_min_u32 v1, .Lcode_end-.Lnopf, v1\n"
    "global_load_dword v255, v1, s[60:61]\n"
    ".Lnopf:\n"
    "s_and_b32 s52, s2, 7\n"
    "s_and_b32 s53, s62, 3\n"
    "s_lshr_b32 s54, s62, 2\n"
    "s_and_b32 s55, s2, 1\n"
    "s_lshl2_add_u32 s55, s55, s53\n"
    "s_lshl_b32 s80, s54, 2\n"
    "s_lshr_b32 s80, 0x20202020, s80\n"
    "s_and_b32 s80, s80, 15\n"
    "s_lshl1_add_u32 s56, s53, s80\n"
    "s_sub_u32 s63, s56, 9\n"
    "s_cselect_b32 s56, s56, s63\n"
    "s_lshl_b32 s80, s54, 2\n"
    "s_lshr_b32 s80, 0x50505050, s80\n"
    "s_and_b32 s80, s80, 15\n"
    "s_min_u32 s80, s80, 7\n"
    "s_lshl1_add_u32 s80, s53, s80\n"
    "s_and_b32 s80, s80, 7\n"
    "s_lshl2_add_u32 s58, s52, s53\n"
    "s_and_b32 s35, s3, 3\n"
    "s_lshr_b32 s63, s52, 1\n"
    "s_lshr_b32 s65, s54, 1\n"
    "s_lshl1_add_u32 s57, s63, s65\n"
    "s_mul_i32 s64, s63, 3\n"
    "s_add_u32 s64, s64, s54\n"
    "s_sub_u32 s64, s64, 4\n"
    "s_and_b32 s66, s54, 1\n"
    "s_lshl1_add_u32 s66, s55, s66\n"
    "s_cmp_lt_u32 s54, 4\n"
    "s_cselect_b32 s57, s57, s64\n"
    "s_cselect_b32 s55, s66, s55\n"
    "s_mov_b32 s59, 0\n"
    "v_lshrrev_b32 v1, 3, v0\n"
    "v_lshrrev_b32 v3, 4, v0\n"
    "v_and_b32 v4, 15, v0\n"
    "v_xor_b32 v2, v0, v3\n"
    "v_and_b32 v2, 7, v2\n"
    "v_lshlrev_b32 v2, 4, v2\n"
    "s_and_b32 s62, s62, 15\n"
    "s_cmp_lt_u32 s54, 4\n"
    "s_cselect_b32 s65, 0, 0\n"
    "s_cselect_b32 s68, 0, 6\n"
    "s_add_u32 s65, s65, s3\n"
    "s_cmp_lt_u32 s65, s68\n"
    "s_cbranch_scc0 .Lspf1\n"
    "s_and_b32 s64, s2, 1\n"
    "s_lshl4_add_u32 s64, s64, s62\n"
    "s_mul_i32 s64, s64, 73728\n"
    "s_lshl_b32 s66, s65, 7\n"
    "s_add_u32 s64, s64, s66\n"
    "v_mul_u32_u24 v5, 1152, v0\n"
    "v_add_u32 v5, s64, v5\n"
    "v_min_u32 v6, 35, v0\n"
    "v_mul_u32_u24 v7, 86, v6\n"
    "v_lshrrev_b32 v7, 8, v7\n"
    "v_mad_u32_u24 v6, v7, 6, v6\n"
    "s_lshl4_add_u32 s65, s65, s62\n"
    "s_mul_i32 s65, s65, 13824\n"
    "s_lshl_b32 s66, s63, 8\n"
    "s_add_u32 s65, s65, s66\n"
    "v_lshl_add_u32 v6, v6, 7, s65\n"
    "s_waitcnt lgkmcnt(0)\n"
    "global_load_dword v250, v5, s[4:5]\n"
    "global_load_dword v251, v6, s[6:7]\n"
    ".Lspf1:\n"
    "s_cmp_lt_u32 s54, 4\n"
    "s_cbranch_scc1 .Lnospf\n"
    "s_lshl4_add_u32 s64, s63, s62\n"
    "s_mul_i32 s64, s64, 81\n"
    "s_mul_i32 s65, s35, 20\n"
    "s_add_u32 s64, s64, s65\n"
    "v_min_u32 v5, 20, v0\n"
    "v_add_lshl_u32 v5, v5, s64, 7\n"
    "s_lshl4_add_u32 s66, s52, s62\n"
    "s_mul_i32 s66, s66, 81\n"
    "s_lshr_b32 s66, s66, 1\n"
    "v_min_u32 v6, 40, v0\n"
    "v_add_lshl_u32 v6, v6, s66, 7\n"
    "s_waitcnt lgkmcnt(0)\n"
    "s_cmp_lt_u32 s3, 4\n"
    "s_cselect_b64 s[64:65], s[8:9], s[10:11]\n"
    "s_barrier\n"
    "global_load_dword v252, v5, s[64:65]\n"
    "s_barrier\n"
    ".Lnospf:\n"
    "s_cmp_ge_u32 s3, 4\n"
    "s_cbranch_scc1 .Lcompute\n"
    "s_cmp_eq_u32 s54, 7\n"
    "s_cbranch_scc1 .Lspare\n"
    "v_lshrrev_b32 v5, 2, v1\n"
    "v_lshlrev_b32_e64 v5, 1, v5\n"
    "v_and_or_b32 v5, v1, 1, v5\n"
    "v_bfe_u32 v6, v1, 1, 1\n"
    "v_lshl_add_u32 v5, v5, 3, v6\n"
    "s_cmp_lt_u32 s54, 4\n"
    "s_cbranch_scc0 .Lw_ssm\n"
    "s_lshr_b32 s61, s35, 1\n"
    "s_and_b32 s62, s35, 1\n"
    "s_lshl1_add_u32 s60, s55, s61\n"
    "s_lshl4_add_u32 s60, s60, s62\n"
    "s_mul_i32 s60, s60, 4608\n"
    "s_mul_i32 s70, s61, 6\n"
    "s_add_u32 s70, s70, s62\n"
    "s_lshl_b32 s70, s70, 12\n"
    "s_add_u32 s70, s70, 82944\n"
    "s_mul_i32 s71, s35, 7168\n"
    "s_mul_i32 s62, s35, 448\n"
    "s_mul_i32 s63, s57, 144\n"
    "v_add_u32 v23, s62, v0\n"
    "s_sub_u32 s72, s63, 0\n"
    "v_add_u32 v24, 0, v23\n"
    "v_add_u32_e64 v28, 64, v23\n"
    "v_mul_u32_u24 v25, 2428, v24\n"
    "v_add_u32 v32, 128, v23\n"
    "v_mul_u32_u24 v29, 2428, v28\n"
    "v_lshrrev_b32 v25, 16, v25\n"
    "v_mul_u32_u24 v33, 2428, v32\n"
    "v_lshrrev_b32 v29, 16, v29\n"
    "v_add_u32 v36, 192, v23\n"
    "v_add_u32 v40, 256, v23\n"
    "v_add_u32 v44, 320, v23\n"
    "v_add_u32 v48, 384, v23\n"
    "v_mul_u32_u24 v26, 27, v25\n"
    "v_lshrrev_b32 v33, 16, v33\n"
    "v_mul_u32_u24_e64 v30, 27, v29\n"
    "v_mul_u32_u24 v37, 2428, v36\n"
    "v_mul_u32_u24 v41, 2428, v40\n"
    "v_mul_u32_u24 v45, 2428, v44\n"
    "v_mul_u32_u24 v49, 2428, v48\n"
    "v_sub_u32 v24, v24, v26\n"
    "v_mul_u32_u24 v34, 27, v33\n"
    "v_sub_u32 v28, v28, v30\n"
    "v_lshrrev_b32 v37, 16, v37\n"
    "v_lshrrev_b32 v41, 16, v41\n"
    "v_lshrrev_b32 v45, 16, v45\n"
    "v_lshrrev_b32 v49, 16, v49\n"
    "v_mul_u32_u24 v26, 57, v24\n"
    "v_sub_u32 v32, v32, v34\n"
    "v_mul_u32_u24 v30, 57, v28\n"
    "v_mul_u32_u24 v38, 27, v37\n"
    "v_mul_u32_u24 v42, 27, v41\n"
    "v_mul_u32_u24 v46, 27, v45\n"
    "v_mul_u32_u24 v50, 27, v49\n"
    "v_lshrrev_b32 v26, 9, v26\n"
    "v_mul_u32_u24 v34, 57, v32\n"
    "v_lshrrev_b32 v30, 9, v30\n"
    "v_sub_u32 v36, v36, v38\n"
    "v_sub_u32 v40, v40, v42\n"
    "v_sub_u32 v44, v44, v46\n"
    "v_sub_u32 v48, v48, v50\n"
    "v_mad_u32_u24 v24, v26, 63, v24\n"
    "v_lshrrev_b32 v34, 9, v34\n"
    "v_bfe_u32 v26, v25, 1, 2\n"
    "v_mad_u32_u24 v28, v30, 63, v28\n"
    "v_mul_u32_u24 v38, 57, v36\n"
    "v_mul_u32_u24 v42, 57, v40\n"
    "v_mul_u32_u24 v46, 57, v44\n"
    "v_mul_u32_u24 v50, 57, v48\n"
    "v_bfe_u32 v30, v29, 1, 2\n"
    "v_mad_u32_u24 v32, v34, 63, v32\n"
    "v_and_or_b32 v26, v25, 56, v26\n"
    "v_and_b32 v27, 1, v25\n"
    "v_bfe_u32 v34, v33, 1, 2\n"
    "v_lshrrev_b32 v38, 9, v38\n"
    "v_lshrrev_b32 v42, 9, v42\n"
    "v_lshrrev_b32 v46, 9, v46\n"
    "v_lshrrev_b32 v50, 9, v50\n"
    "v_and_or_b32 v30, v29, 56, v30\n"
    "v_and_b32 v31, 1, v29\n"
    "v_lshl_add_u32 v26, v27, 2, v26\n"
    "v_and_or_b32 v34, v33, 56, v34\n"
    "v_and_b32_e64 v35, 1, v33\n"
    "v_mad_u32_u24 v36, v38, 63, v36\n"
    "v_mad_u32_u24 v40, v42, 63, v40\n"
    "v_mad_u32_u24 v44, v46, 63, v44\n"
    "v_mad_u32_u24 v48, v50, 63, v48\n"
    "v_lshl_add_u32 v30, v31, 2, v30\n"
    "v_bfe_u32 v38, v37, 1, 2\n"
    "v_bfe_u32 v42, v41, 1, 2\n"
    "v_bfe_u32 v46, v45, 1, 2\n"
    "v_bfe_u32 v50, v49, 1, 2\n"
    "v_mul_u32_u24 v26, 3456, v26\n"
    "v_lshl_add_u32 v34, v35, 2, v34\n"
    "v_mul_u32_u24 v30, 3456, v30\n"
    "v_and_or_b32 v38, v37, 56, v38\n"
    "v_and_b32 v39, 1, v37\n"
    "v_and_or_b32 v42, v41, 56, v42\n"
    "v_and_b32 v43, 1, v41\n"
    "v_and_or_b32 v46, v45, 56, v46\n"
    "v_and_b32 v47, 1, v45\n"
    "v_and_or_b32 v50, v49, 56, v50\n"
    "v_and_b32 v51, 1, v49\n"
    "s_sub_u32 s73, s63, 1024\n"
    "v_lshl_add_u32 v24, v24, 4, v26\n"
    "v_mul_u32_u24 v34, 3456, v34\n"
    "s_sub_u32 s74, s63, 2048\n"
    "v_lshl_add_u32 v28, v28, 4, v30\n"
    "v_lshl_add_u32 v38, v39, 2, v38\n"
    "v_lshl_add_u32 v42, v43, 2, v42\n"
    "v_lshl_add_u32 v46, v47, 2, v46\n"
    "v_lshl_add_u32 v50, v51, 2, v50\n"
    "v_add_u32_e64 v16, s72, v24\n"
    "s_sub_u32 s75, s63, 3072\n"
    "v_lshl_add_u32 v32, v32, 4, v34\n"
    "v_add_u32 v17, s73, v28\n"
    "s_sub_u32 s72, s63, 0\n"
    "v_mul_u32_u24 v38, 3456, v38\n"
    "v_mul_u32_u24 v42, 3456, v42\n"
    "v_mul_u32_u24 v46, 3456, v46\n"
    "v_mul_u32_u24 v50, 3456, v50\n"
    "v_add_u32_e64 v18, s74, v32\n"
    "s_sub_u32 s73, s63, 1024\n"
    "v_lshl_add_u32 v36, v36, 4, v38\n"
    "v_lshl_add_u32 v40, v40, 4, v42\n"
    "v_lshl_add_u32 v44, v44, 4, v46\n"
    "v_lshl_add_u32 v48, v48, 4, v50\n"
    "s_sub_u32 s74, s63, 2048\n"
    "v_add_u32 v19, s75, v36\n"
    "v_add_u32 v20, s72, v40\n"
    "v_add_u32 v21, s73, v44\n"
    "v_add_u32 v22, s74, v48\n"
    "v_mul_u32_u24 v5, 1152, v5\n"
    "v_add3_u32 v8, v5, v2, s60\n"
    "v_xor_b32_e64 v6, 64, v8\n"
    "v_add_u32 v9, 35840, v6\n"
    "v_add_u32 v10, 256, v8\n"
    "v_add_u32 v11, 36096, v6\n"
    "s_mov_b32 s67, s70\n"
    "s_mov_b32 s68, s71\n"
    "s_mov_b32 s64, s59\n"
    "s_mov_b32 s69, 0\n"
    "s_waitcnt lgkmcnt(0)\n"
    "s_mov_b32 s40, s4\n"
    "s_and_b32 s41, s5, 0xffff\n"
    "s_mov_b32 s42, 2359296\n"
    "s_mov_b32 s43, 0x20000\n"
    "s_lshl_b32 s65, s64, 7\n"
    "s_mov_b32 m0, s67\n"
    "s_nop 0\n"
    "buffer_load_dwordx4 v8, s[40:43], s65 offen offset:0 lds\n"
    "buffer_load_dwordx4 v9, s[40:43], s65 offen offset:1024 lds\n"
    "buffer_load_dwordx4 v10, s[40:43], s65 offen offset:2048 lds\n"
    "buffer_load_dwordx4 v11, s[40:43], s65 offen offset:3072 lds\n"
    "s_mov_b32 s44, s6\n"
    "s_and_b32 s45, s7, 0xffff\n"
    "s_mov_b32 s46, 1990656\n"
    "s_mov_b32 s47, 0x20000\n"
    "s_mov_b32 s36, s12\n"
    "s_and_b32 s37, s13, 0xffff\n"
    "s_mov_b32 s38, 9175040\n"
    "s_mov_b32 s39, 0x20000\n"
    "buffer_load_dword v253, off, s[36:39], 0 sc1\n"
    "s_branch .Lla_w\n"
    "s_nop 0\n"
    ".Lla_loop:\n"
    "s_lshl_b32 s65, s64, 7\n"
    "s_mov_b32 m0, s67\n"
    "s_nop 0\n"
    "buffer_load_dwordx4 v8, s[40:43], s65 offen offset:0 lds\n"
    "buffer_load_dwordx4 v9, s[40:43], s65 offen offset:1024 lds\n"
    "buffer_load_dwordx4 v10, s[40:43], s65 offen offset:2048 lds\n"
    "buffer_load_dwordx4 v11, s[40:43], s65 offen offset:3072 lds\n"
    ".Lla_w:\n"
    "s_mul_i32 s66, s64, 221184\n"
    "s_mov_b32 m0, s68\n"
    "s_nop 0\n"
    "buffer_load_dwordx4 v16, s[44:47], s66 offen offset:0 lds\n"
    "buffer_load_dwordx4 v17, s[44:47], s66 offen offset:1024 lds\n"
    "buffer_load_dwordx4 v18, s[44:47], s66 offen offset:2048 lds\n"
    "s_cmp_eq_u32 s69, 0\n"
    "s_cbranch_scc1 .Lla_m\n"
    "s_waitcnt vmcnt(7)\n"
    "s_barrier\n"
    ".Lla_m:\n"
    "buffer_load_dwordx4 v19, s[44:47], s66 offen offset:3072 lds\n"
    "s_add_u32 m0, s68, 4096\n"
    "s_nop 0\n"
    "buffer_load_dwordx4 v20, s[44:47], s66 offen offset:0 lds\n"
    "buffer_load_dwordx4 v21, s[44:47], s66 offen offset:1024 lds\n"
    "s_cmp_eq_u32 s35, 3\n"
    "s_cbranch_scc1 .Lla_no7\n"
    "buffer_load_dwordx4 v22, s[44:47], s66 offen offset:2048 lds\n"
    ".Lla_no7:\n"
    "s_add_u32 s64, s64, 1\n"
    "s_cmp_eq_u32 s64, 9\n"
    "s_cselect_b32 s64, 0, s64\n"
    "s_add_u32 s67, s67, 8192\n"
    "s_add_u32 s68, s68, 27648\n"
    "s_cmp_ge_u32 s68, 82944\n"
    "s_cselect_b32 s67, s70, s67\n"
    "s_cselect_b32 s68, s71, s68\n"
    "s_add_u32 s69, s69, 1\n"
    "s_cmp_eq_u32 s69, 1\n"
    "s_cbranch_scc1 .Lsetup5\n"
    ".Lsetup5r:\n"
    "s_cmp_lt_u32 s69, 9\n"
    "s_cbranch_scc1 .Lla_loop\n"
    "s_branch .Lld_done5\n"
    "s_nop 0\n"
    ".Lw_ssm:\n"
    "s_lshl2_add_u32 s60, s55, s35\n"
    "s_mul_i32 s60, s60, 73728\n"
    "s_mul_i32 s70, s35, 24576\n"
    "s_add_u32 s70, s70, 49152\n"
    "s_mul_i32 s71, s35, 3072\n"
    "v_mul_u32_u24 v5, 1152, v5\n"
    "v_add3_u32 v8, v5, v2, s60\n"
    "v_xor_b32_e64 v6, 64, v8\n"
    "v_add_u32 v9, 35840, v6\n"
    "v_add_u32 v10, 256, v8\n"
    "v_add_u32 v11, 36096, v6\n"
    "v_add_u32 v12, 4608, v8\n"
    "v_add_u32 v13, 40448, v6\n"
    "v_add_u32 v14, 4864, v8\n"
    "v_add_u32 v15, 40704, v6\n"
    "s_mov_b32 s67, s70\n"
    "s_mov_b32 s68, s71\n"
    "s_mov_b32 s64, s59\n"
    "s_mov_b32 s69, 0\n"
    "s_waitcnt lgkmcnt(0)\n"
    "s_mov_b32 s40, s4\n"
    "s_and_b32 s41, s5, 0xffff\n"
    "s_mov_b32 s42, 2359296\n"
    "s_mov_b32 s43, 0x20000\n"
    "s_and_b32 s60, s35, 1\n"
    "s_lshl_b32 s61, s60, 6\n"
    "s_lshl1_add_u32 s60, s57, s60\n"
    "s_mul_i32 s60, s60, 27648\n"
    "v_mul_u32_u24 v5, 1152, v1\n"
    "v_xor_b32 v6, s61, v2\n"
    "v_add3_u32 v16, v5, v6, s60\n"
    "v_xor_b32_e64 v5, 64, v16\n"
    "v_add_u32 v17, 8192, v5\n"
    "v_add_u32 v18, 16384, v16\n"
    "s_cmp_lt_u32 s35, 2\n"
    "s_cselect_b64 s[60:61], s[8:9], s[10:11]\n"
    "s_mov_b32 s44, s60\n"
    "s_and_b32 s45, s61, 0xffff\n"
    "s_mov_b32 s46, 663552\n"
    "s_mov_b32 s47, 0x20000\n"
    "s_mov_b32 s36, s12\n"
    "s_and_b32 s37, s13, 0xffff\n"
    "s_mov_b32 s38, 9175040\n"
    "s_mov_b32 s39, 0x20000\n"
    "buffer_load_dword v253, off, s[36:39], 0 sc1\n"
    "s_branch .Lsetup4\n"
    ".Lls_loop:\n"
    "s_lshl_b32 s65, s64, 7\n"
    "s_mov_b32 m0, s67\n"
    "s_nop 0\n"
    "buffer_load_dwordx4 v8, s[40:43], s65 offen offset:0 lds\n"
    "buffer_load_dwordx4 v9, s[40:43], s65 offen offset:1024 lds\n"
    "buffer_load_dwordx4 v10, s[40:43], s65 offen offset:2048 lds\n"
    "buffer_load_dwordx4 v11, s[40:43], s65 offen offset:3072 lds\n"
    "s_add_u32 m0, s67, 4096\n"
    "s_nop 0\n"
    "buffer_load_dwordx4 v12, s[40:43], s65 offen offset:0 lds\n"
    "buffer_load_dwordx4 v13, s[40:43], s65 offen offset:1024 lds\n"
    "buffer_load_dwordx4 v14, s[40:43], s65 offen offset:2048 lds\n"
    "buffer_load_dwordx4 v15, s[40:43], s65 offen offset:3072 lds\n"
    "s_mov_b32 m0, s68\n"
    "s_nop 0\n"
    "buffer_load_dwordx4 v16, s[44:47], s65 offen offset:0 lds\n"
    "buffer_load_dwordx4 v17, s[44:47], s65 offen offset:1024 lds\n"
    "buffer_load_dwordx4 v18, s[44:47], s65 offen offset:2048 lds\n"
    "s_add_u32 s64, s64, 1\n"
    "s_cmp_eq_u32 s64, 9\n"
    "s_cselect_b32 s64, 0, s64\n"
    "s_add_u32 s67, s67, 8192\n"
    "s_add_u32 s68, s68, 16384\n"
    "s_cmp_ge_u32 s68, 49152\n"
    "s_cselect_b32 s67, s70, s67\n"
    "s_cselect_b32 s68, s71, s68\n"
    "s_add_u32 s69, s69, 1\n"
    "s_cmp_le_u32 s69, 1\n"
    "s_cbranch_scc1 .Lls_loop\n"
    "s_waitcnt vmcnt(11)\n"
    "s_barrier\n"
    "s_cmp_lt_u32 s69, 9\n"
    "s_cbranch_scc1 .Lls_loop\n"
    "s_branch .Lld_done4\n"
    "s_nop 0\n"
    ".Lsetup4:\n"
    "s_and_b32 s83, s54, 3\n"
    "s_mul_i32 s83, s83, 160\n"
    "s_add_u32 s83, s83, 512\n"
    "s_lshr_b32 s62, s35, 1\n"
    "s_and_b32 s63, s35, 1\n"
    "s_mul_i32 s61, s63, 64\n"
    "s_mul_i32 s60, s63, 80\n"
    "s_add_u32 s60, s60, s83\n"
    "s_mul_i32 s60, s60, 576\n"
    "v_mul_u32_u24 v36, 1152, v1\n"
    "v_xor_b32 v37, s61, v2\n"
    "v_add3_u32 v92, v36, v37, s60\n"
    "v_xor_b32 v36, 64, v92\n"
    "v_add_u32 v93, 8192, v36\n"
    "v_add_u32 v94, 16384, v92\n"
    "v_add_u32 v95, 24576, v36\n"
    "v_add_u32 v96, 36864, v92\n"
    "s_mul_i32 s86, s35, 5120\n"
    "s_add_u32 s85, s86, 61440\n"
    "s_mul_i32 s81, s63, 5120\n"
    "s_add_u32 s84, s80, s62\n"
    "s_and_b32 s84, s84, 7\n"
    "s_cmp_eq_u32 s62, 0\n"
    "s_cselect_b64 s[60:61], s[22:23], s[24:25]\n"
    "s_mov_b32 s88, s60\n"
    "s_and_b32 s89, s61, 0xffff\n"
    "s_mov_b32 s90, 663552\n"
    "s_mov_b32 s91, 0x20000\n"
    "s_mov_b32 s48, s26\n"
    "s_and_b32 s49, s27, 0xffff\n"
    "s_mov_b32 s50, 663552\n"
    "s_mov_b32 s51, 0x20000\n"
    "s_and_b32 s60, s35, 1\n"
    "s_mul_i32 s60, s60, 320\n"
    "v_add_u32 v36, s60, v0\n"
    "v_add_u32_e64 v70, 64, v36\n"
    "v_add_u32 v74, 128, v36\n"
    "v_add_u32 v78, 192, v36\n"
    "v_mul_u32_u24 v71, 6554, v70\n"
    "v_mul_u32_u24 v75, 6554, v74\n"
    "v_mul_u32_u24 v79, 6554, v78\n"
    "v_add_u32 v82, 256, v36\n"
    "v_mul_u32_u24 v67, 6554, v36\n"
    "v_lshrrev_b32 v71, 16, v71\n"
    "v_lshrrev_b32 v75, 16, v75\n"
    "v_lshrrev_b32 v79, 16, v79\n"
    "v_mul_u32_u24 v83, 6554, v82\n"
    "v_lshrrev_b32 v67, 16, v67\n"
    "v_mul_u32_u24 v72, 10, v71\n"
    "v_mul_u32_u24 v76, 10, v75\n"
    "v_mul_u32_u24 v80, 10, v79\n"
    "v_lshrrev_b32 v83, 16, v83\n"
    "v_mul_u32_u24 v68, 10, v67\n"
    "v_sub_u32 v70, v70, v72\n"
    "v_sub_u32 v74, v74, v76\n"
    "v_sub_u32 v78, v78, v80\n"
    "v_mul_u32_u24 v84, 10, v83\n"
    "v_lshrrev_b32 v72, 4, v71\n"
    "v_lshrrev_b32 v76, 4, v75\n"
    "v_lshrrev_b32 v80, 4, v79\n"
    "v_sub_u32 v66, v36, v68\n"
    "v_sub_u32 v82, v82, v84\n"
    "v_lshrrev_b32_e64 v68, 4, v67\n"
    "v_lshl_add_u32 v72, v72, 1, v71\n"
    "v_lshl_add_u32 v76, v76, 1, v75\n"
    "v_lshl_add_u32 v80, v80, 1, v79\n"
    "v_lshrrev_b32_e64 v84, 4, v83\n"
    "v_lshl_add_u32 v68, v68, 1, v67\n"
    "v_mul_u32_u24 v72, 1152, v72\n"
    "v_mul_u32_u24 v76, 1152, v76\n"
    "v_mul_u32_u24 v80, 1152, v80\n"
    "v_lshl_add_u32 v84, v84, 1, v83\n"
    "v_mul_u32_u24 v68, 1152, v68\n"
    "v_lshl_add_u32 v98, v70, 4, v72\n"
    "v_lshl_add_u32 v99, v74, 4, v76\n"
    "v_lshl_add_u32 v100, v78, 4, v80\n"
    "v_mul_u32_u24 v84, 1152, v84\n"
    "v_lshl_add_u32 v97, v66, 4, v68\n"
    "v_add_u32 v98, -1024, v98\n"
    "v_add_u32 v99, -2048, v99\n"
    "v_add_u32 v100, -3072, v100\n"
    "v_lshl_add_u32 v101, v82, 4, v84\n"
    "s_cmp_lt_u32 s35, 2\n"
    "s_cbranch_scc0 .Lwl5x\n"
    "s_and_b32 s60, s35, 1\n"
    "s_mul_i32 s60, s60, 320\n"
    "v_add_u32 v36, s60, v0\n"
    "v_add_u32_e64 v70, 64, v36\n"
    "v_add_u32 v74, 128, v36\n"
    "v_add_u32 v78, 192, v36\n"
    "v_add_u32 v82, 256, v36\n"
    "v_mul_u32_u24 v67, 6554, v36\n"
    "v_mul_u32_u24 v71, 6554, v70\n"
    "v_mul_u32_u24 v75, 6554, v74\n"
    "v_mul_u32_u24 v79, 6554, v78\n"
    "v_mul_u32_u24 v83, 6554, v82\n"
    "v_lshrrev_b32 v67, 16, v67\n"
    "v_lshrrev_b32 v71, 16, v71\n"
    "v_lshrrev_b32 v75, 16, v75\n"
    "v_lshrrev_b32 v79, 16, v79\n"
    "v_lshrrev_b32 v83, 16, v83\n"
    "v_mul_u32_u24 v68, 10, v67\n"
    "v_mul_u32_u24 v72, 10, v71\n"
    "v_mul_u32_u24 v76, 10, v75\n"
    "v_mul_u32_u24 v80, 10, v79\n"
    "v_mul_u32_u24 v84, 10, v83\n"
    "v_sub_u32 v66, v36, v68\n"
    "v_sub_u32 v70, v70, v72\n"
    "v_sub_u32 v74, v74, v76\n"
    "v_sub_u32 v78, v78, v80\n"
    "v_sub_u32 v82, v82, v84\n"
    "v_lshrrev_b32 v68, 1, v67\n"
    "v_lshrrev_b32 v72, 1, v71\n"
    "v_lshrrev_b32 v76, 1, v75\n"
    "v_lshrrev_b32 v80, 1, v79\n"
    "v_lshrrev_b32 v84, 1, v83\n"
    "v_lshl_add_u32 v68, v68, 4, v67\n"
    "v_lshl_add_u32 v72, v72, 4, v71\n"
    "v_lshl_add_u32 v76, v76, 4, v75\n"
    "v_lshl_add_u32 v80, v80, 4, v79\n"
    "v_lshl_add_u32 v84, v84, 4, v83\n"
    "v_mul_u32_u24 v68, 1152, v68\n"
    "v_mul_u32_u24 v72, 1152, v72\n"
    "v_mul_u32_u24 v76, 1152, v76\n"
    "v_mul_u32_u24 v80, 1152, v80\n"
    "v_mul_u32_u24 v84, 1152, v84\n"
    "v_lshl_add_u32 v102, v66, 4, v68\n"
    "v_lshl_add_u32 v103, v70, 4, v72\n"
    "v_lshl_add_u32 v104, v74, 4, v76\n"
    "v_lshl_add_u32 v105, v78, 4, v80\n"
    "v_lshl_add_u32 v106, v82, 4, v84\n"
    "v_add_u32 v102, 18432, v102\n"
    "v_add_u32 v103, 17408, v103\n"
    "v_add_u32 v104, 16384, v104\n"
    "v_add_u32 v105, 15360, v105\n"
    "v_add_u32 v106, 18432, v106\n"
    "s_branch .Lwl5e\n"
    ".Lwl5x:\n"
    "s_and_b32 s60, s35, 1\n"
    "s_mul_i32 s60, s60, 320\n"
    "v_add_u32 v36, s60, v0\n"
    "v_add_u32 v70, 64, v36\n"
    "v_add_u32 v74, 128, v36\n"
    "v_add_u32 v78, 192, v36\n"
    "v_add_u32 v82, 256, v36\n"
    "v_mul_u32_u24 v67, 6554, v36\n"
    "v_mul_u32_u24 v71, 6554, v70\n"
    "v_mul_u32_u24 v75, 6554, v74\n"
    "v_mul_u32_u24 v79, 6554, v78\n"
    "v_mul_u32_u24 v83, 6554, v82\n"
    "v_lshrrev_b32 v67, 16, v67\n"
    "v_lshrrev_b32 v71, 16, v71\n"
    "v_lshrrev_b32 v75, 16, v75\n"
    "v_lshrrev_b32 v79, 16, v79\n"
    "v_lshrrev_b32 v83, 16, v83\n"
    "v_mul_u32_u24 v68, 10, v67\n"
    "v_lshrrev_b32 v102, 4, v67\n"
    "v_mul_u32_u24 v72, 10, v71\n"
    "v_lshrrev_b32 v103, 4, v71\n"
    "v_mul_u32_u24 v76, 10, v75\n"
    "v_lshrrev_b32 v104, 4, v75\n"
    "v_mul_u32_u24 v80, 10, v79\n"
    "v_lshrrev_b32 v105, 4, v79\n"
    "v_mul_u32_u24 v84, 10, v83\n"
    "v_lshrrev_b32 v106, 4, v83\n"
    "v_and_b32 v67, 15, v67\n"
    "v_and_b32 v71, 15, v71\n"
    "v_and_b32 v75, 15, v75\n"
    "v_and_b32 v79, 15, v79\n"
    "v_and_b32 v83, 15, v83\n"
    "v_sub_u32 v66, v36, v68\n"
    "v_sub_u32 v70, v70, v72\n"
    "v_sub_u32 v74, v74, v76\n"
    "v_sub_u32 v78, v78, v80\n"
    "v_sub_u32 v82, v82, v84\n"
    "v_lshrrev_b32 v68, 2, v67\n"
    "v_lshrrev_b32 v72, 2, v71\n"
    "v_lshrrev_b32 v76, 2, v75\n"
    "v_lshrrev_b32 v80, 2, v79\n"
    "v_lshrrev_b32 v84, 2, v83\n"
    "v_lshlrev_b32 v68, 1, v68\n"
    "v_lshlrev_b32 v72, 1, v72\n"
    "v_lshlrev_b32 v76, 1, v76\n"
    "v_lshlrev_b32 v80, 1, v80\n"
    "v_lshlrev_b32_e64 v84, 1, v84\n"
    "v_and_or_b32 v68, v67, 1, v68\n"
    "v_bfe_u32 v69, v67, 1, 1\n"
    "v_and_or_b32 v72, v71, 1, v72\n"
    "v_bfe_u32 v73, v71, 1, 1\n"
    "v_and_or_b32 v76, v75, 1, v76\n"
    "v_bfe_u32 v77, v75, 1, 1\n"
    "v_and_or_b32 v80, v79, 1, v80\n"
    "v_bfe_u32 v81, v79, 1, 1\n"
    "v_and_or_b32 v84, v83, 1, v84\n"
    "v_bfe_u32 v85, v83, 1, 1\n"
    "v_lshl_add_u32 v68, v68, 3, v69\n"
    "v_lshl_add_u32 v72, v72, 3, v73\n"
    "v_lshl_add_u32 v76, v76, 3, v77\n"
    "v_lshl_add_u32 v80, v80, 3, v81\n"
    "v_lshl_add_u32 v84, v84, 3, v85\n"
    "v_lshl_add_u32 v68, v102, 1, v68\n"
    "v_lshl_add_u32 v72, v103, 1, v72\n"
    "v_lshl_add_u32 v76, v104, 1, v76\n"
    "v_lshl_add_u32 v80, v105, 1, v80\n"
    "v_lshl_add_u32 v84, v106, 1, v84\n"
    "v_mul_u32_u24 v68, 1152, v68\n"
    "v_mul_u32_u24 v72, 1152, v72\n"
    "v_mul_u32_u24 v76, 1152, v76\n"
    "v_mul_u32_u24 v80, 1152, v80\n"
    "v_mul_u32_u24 v84, 1152, v84\n"
    "v_lshl_add_u32 v102, v66, 4, v68\n"
    "v_lshl_add_u32 v103, v70, 4, v72\n"
    "v_lshl_add_u32 v104, v74, 4, v76\n"
    "v_lshl_add_u32 v105, v78, 4, v80\n"
    "v_lshl_add_u32 v106, v82, 4, v84\n"
    ".Lwl5e:\n"
    "s_cmp_eq_u32 s54, 7\n"
    "s_cbranch_scc1 .Lhelp2\n"
    "s_branch .Lls_loop\n"
    ".Lsetup5:\n"
    "s_and_b32 s83, s54, 3\n"
    "s_mul_i32 s83, s83, 128\n"
    "s_add_u32 s83, s83, 0\n"
    "s_lshr_b32 s62, s35, 1\n"
    "s_and_b32 s63, s35, 1\n"
    "s_mul_i32 s61, s63, 0\n"
    "s_mul_i32 s60, s63, 64\n"
    "s_add_u32 s60, s60, s83\n"
    "s_mul_i32 s60, s60, 576\n"
    "v_mul_u32_u24 v36, 1152, v1\n"
    "v_xor_b32 v37, s61, v2\n"
    "v_add3_u32 v92, v36, v37, s60\n"
    "v_xor_b32 v36, 64, v92\n"
    "v_add_u32 v93, 8192, v36\n"
    "v_add_u32 v94, 16384, v92\n"
    "v_add_u32 v95, 24576, v36\n"
    "s_and_b32 s60, s35, 1\n"
    "s_mul_i32 s60, s60, 256\n"
    "v_add_u32 v36, s60, v0\n"
    "v_add_u32_e64 v70, 64, v36\n"
    "v_add_u32 v74, 128, v36\n"
    "v_add_u32 v78, 192, v36\n"
    "v_lshrrev_b32 v71, 3, v70\n"
    "v_lshrrev_b32 v75, 3, v74\n"
    "v_lshrrev_b32 v79, 3, v78\n"
    "v_lshrrev_b32 v67, 3, v36\n"
    "v_and_b32 v70, 7, v70\n"
    "v_and_b32 v72, 6, v71\n"
    "v_and_b32 v74, 7, v74\n"
    "v_and_b32 v76, 6, v75\n"
    "v_and_b32 v78, 7, v78\n"
    "v_and_b32 v80, 6, v79\n"
    "v_and_b32 v66, 7, v36\n"
    "v_and_b32 v68, 6, v67\n"
    "v_xor_b32 v70, v70, v72\n"
    "v_xor_b32 v74, v74, v76\n"
    "v_xor_b32 v78, v78, v80\n"
    "v_lshrrev_b32 v72, 4, v71\n"
    "v_lshrrev_b32 v76, 4, v75\n"
    "v_lshrrev_b32 v80, 4, v79\n"
    "v_xor_b32 v66, v66, v68\n"
    "v_lshrrev_b32 v68, 4, v67\n"
    "v_lshl_add_u32 v72, v72, 1, v71\n"
    "v_lshl_add_u32 v76, v76, 1, v75\n"
    "v_lshl_add_u32 v80, v80, 1, v79\n"
    "v_lshl_add_u32 v68, v68, 1, v67\n"
    "v_mul_u32_u24 v72, 1152, v72\n"
    "v_mul_u32_u24 v76, 1152, v76\n"
    "v_mul_u32_u24 v80, 1152, v80\n"
    "v_mul_u32_u24 v68, 1152, v68\n"
    "v_lshl_add_u32 v98, v70, 4, v72\n"
    "v_lshl_add_u32 v99, v74, 4, v76\n"
    "v_lshl_add_u32 v100, v78, 4, v80\n"
    "v_lshl_add_u32 v97, v66, 4, v68\n"
    "v_add_u32 v98, -1024, v98\n"
    "v_add_u32 v99, -2048, v99\n"
    "v_add_u32 v100, -3072, v100\n"
    "s_cmp_lt_u32 s35, 2\n"
    "s_cbranch_scc0 .Lwl4x\n"
    "s_and_b32 s60, s35, 1\n"
    "s_mul_i32 s60, s60, 256\n"
    "v_add_u32 v36, s60, v0\n"
    "v_add_u32_e64 v70, 64, v36\n"
    "v_add_u32 v74, 128, v36\n"
    "v_add_u32 v78, 192, v36\n"
    "v_lshrrev_b32 v67, 3, v36\n"
    "v_lshrrev_b32 v71, 3, v70\n"
    "v_lshrrev_b32 v75, 3, v74\n"
    "v_lshrrev_b32 v79, 3, v78\n"
    "v_and_b32 v66, 7, v36\n"
    "v_and_b32 v68, 6, v67\n"
    "v_and_b32 v70, 7, v70\n"
    "v_and_b32 v72, 6, v71\n"
    "v_and_b32 v74, 7, v74\n"
    "v_and_b32 v76, 6, v75\n"
    "v_and_b32 v78, 7, v78\n"
    "v_and_b32 v80, 6, v79\n"
    "v_xor_b32 v66, v66, v68\n"
    "v_xor_b32 v70, v70, v72\n"
    "v_xor_b32 v74, v74, v76\n"
    "v_xor_b32 v78, v78, v80\n"
    "v_lshrrev_b32 v68, 1, v67\n"
    "v_lshrrev_b32 v72, 1, v71\n"
    "v_lshrrev_b32 v76, 1, v75\n"
    "v_lshrrev_b32 v80, 1, v79\n"
    "v_lshl_add_u32 v68, v68, 4, v67\n"
    "v_lshl_add_u32 v72, v72, 4, v71\n"
    "v_lshl_add_u32 v76, v76, 4, v75\n"
    "v_lshl_add_u32 v80, v80, 4, v79\n"
    "v_mul_u32_u24 v68, 1152, v68\n"
    "v_mul_u32_u24 v72, 1152, v72\n"
    "v_mul_u32_u24 v76, 1152, v76\n"
    "v_mul_u32_u24 v80, 1152, v80\n"
    "v_lshl_add_u32 v102, v66, 4, v68\n"
    "v_lshl_add_u32 v103, v70, 4, v72\n"
    "v_lshl_add_u32 v104, v74, 4, v76\n"
    "v_lshl_add_u32 v105, v78, 4, v80\n"
    "v_add_u32 v102, 18432, v102\n"
    "v_add_u32 v103, 17408, v103\n"
    "v_add_u32 v104, 16384, v104\n"
    "v_add_u32 v105, 15360, v105\n"
    "s_branch .Lwl4e\n"
    ".Lwl4x:\n"
    "s_and_b32 s60, s35, 1\n"
    "s_mul_i32 s60, s60, 256\n"
    "v_add_u32 v36, s60, v0\n"
    "v_add_u32 v70, 64, v36\n"
    "v_add_u32 v74, 128, v36\n"
    "v_add_u32 v78, 192, v36\n"
    "v_lshrrev_b32 v67, 3, v36\n"
    "v_lshrrev_b32 v71, 3, v70\n"
    "v_lshrrev_b32 v75, 3, v74\n"
    "v_lshrrev_b32 v79, 3, v78\n"
    "v_lshrrev_b32 v102, 4, v67\n"
    "v_lshrrev_b32 v103, 4, v71\n"
    "v_lshrrev_b32 v104, 4, v75\n"
    "v_lshrrev_b32 v105, 4, v79\n"
    "v_and_b32 v67, 15, v67\n"
    "v_and_b32 v71, 15, v71\n"
    "v_and_b32 v75, 15, v75\n"
    "v_and_b32 v79, 15, v79\n"
    "v_lshrrev_b32 v68, 2, v67\n"
    "v_lshrrev_b32 v72, 2, v71\n"
    "v_lshrrev_b32 v76, 2, v75\n"
    "v_lshrrev_b32 v80, 2, v79\n"
    "v_lshlrev_b32 v68, 1, v68\n"
    "v_lshlrev_b32 v72, 1, v72\n"
    "v_lshlrev_b32 v76, 1, v76\n"
    "v_lshlrev_b32 v80, 1, v80\n"
    "v_and_or_b32 v68, v67, 1, v68\n"
    "v_bfe_u32 v69, v67, 1, 1\n"
    "v_and_or_b32 v72, v71, 1, v72\n"
    "v_bfe_u32 v73, v71, 1, 1\n"
    "v_and_or_b32 v76, v75, 1, v76\n"
    "v_bfe_u32 v77, v75, 1, 1\n"
    "v_and_or_b32 v80, v79, 1, v80\n"
    "v_bfe_u32 v81, v79, 1, 1\n"
    "v_lshl_add_u32 v68, v68, 3, v69\n"
    "v_lshl_add_u32 v72, v72, 3, v73\n"
    "v_lshl_add_u32 v76, v76, 3, v77\n"
    "v_lshl_add_u32 v80, v80, 3, v81\n"
    "v_lshl_add_u32 v68, v102, 1, v68\n"
    "v_lshl_add_u32 v72, v103, 1, v72\n"
    "v_lshl_add_u32 v76, v104, 1, v76\n"
    "v_lshl_add_u32 v80, v105, 1, v80\n"
    "v_and_b32 v66, 7, v36\n"
    "v_mul_u32_u24 v68, 1152, v68\n"
    "v_and_b32 v70, 7, v70\n"
    "v_mul_u32_u24 v72, 1152, v72\n"
    "v_and_b32 v74, 7, v74\n"
    "v_mul_u32_u24 v76, 1152, v76\n"
    "v_and_b32 v78, 7, v78\n"
    "v_mul_u32_u24 v80, 1152, v80\n"
    "v_lshl_add_u32 v102, v66, 4, v68\n"
    "v_lshl_add_u32 v103, v70, 4, v72\n"
    "v_lshl_add_u32 v104, v74, 4, v76\n"
    "v_lshl_add_u32 v105, v78, 4, v80\n"
    ".Lwl4e:\n"
    "s_branch .Lsetup5r\n"
    "s_nop 0\n"
    ".Lld_done5:\n"
    "v_readfirstlane_b32 s34, v253\n"
    "s_nop 0\n"
    "s_add_u32 s34, s34, 0x9E3779B9\n"
    "s_and_b32 s83, s54, 3\n"
    "s_mul_i32 s83, s83, 128\n"
    "s_add_u32 s83, s83, 0\n"
    "s_lshr_b32 s62, s35, 1\n"
    "s_and_b32 s63, s35, 1\n"
    "s_mul_i32 s86, s35, 4096\n"
    "s_add_u32 s85, s86, 61440\n"
    "s_mul_i32 s81, s63, 4096\n"
    "s_add_u32 s84, s80, s62\n"
    "s_and_b32 s84, s84, 7\n"
    "s_cmp_eq_u32 s62, 0\n"
    "s_cselect_b64 s[60:61], s[22:23], s[24:25]\n"
    "s_mov_b32 s88, s60\n"
    "s_and_b32 s89, s61, 0xffff\n"
    "s_mov_b32 s90, 663552\n"
    "s_mov_b32 s91, 0x20000\n"
    "s_mov_b32 s48, s26\n"
    "s_and_b32 s49, s27, 0xffff\n"
    "s_mov_b32 s50, 663552\n"
    "s_mov_b32 s51, 0x20000\n"
    "s_add_u32 s60, s56, 2\n"
    "s_sub_u32 s61, s60, 9\n"
    "s_cselect_b32 s60, s60, s61\n"
    "s_lshl_b32 s60, s60, 7\n"
    "buffer_load_dwordx4 v[108:111], v92, s[88:91], s60 offen offset:0\n"
    "buffer_load_dwordx4 v[112:115], v93, s[88:91], s60 offen offset:1024\n"
    "buffer_load_dwordx4 v[116:119], v94, s[88:91], s60 offen offset:2048\n"
    "buffer_load_dwordx4 v[120:123], v95, s[88:91], s60 offen offset:3072\n"
    "s_add_u32 s60, s56, 3\n"
    "s_sub_u32 s61, s60, 9\n"
    "s_cselect_b32 s60, s60, s61\n"
    "s_lshl_b32 s60, s60, 7\n"
    "buffer_load_dwordx4 v[124:127], v92, s[88:91], s60 offen offset:0\n"
    "buffer_load_dwordx4 v[128:131], v93, s[88:91], s60 offen offset:1024\n"
    "buffer_load_dwordx4 v[132:135], v94, s[88:91], s60 offen offset:2048\n"
    "buffer_load_dwordx4 v[136:139], v95, s[88:91], s60 offen offset:3072\n"
    "s_waitcnt vmcnt(8)\n"
    "s_barrier\n"
    "s_lshl4_add_u32 s60, s57, s55\n"
    "s_mul_i32 s60, s60, 12800\n"
    "s_lshl_b32 s61, s35, 12\n"
    "s_add_u32 s60, s60, s61\n"
    "s_add_u32 s60, s60, 262144\n"
    "v_lshlrev_b32 v5, 4, v0\n"
    "v_mov_b32 v8, 0\n"
    "v_mov_b32 v9, 0\n"
    "v_mov_b32 v10, 0\n"
    "v_mov_b32 v11, 1\n"
    "s_cmp_eq_u32 s35, 3\n"
    "s_cselect_b32 exec_hi, 0, -1\n"
    "buffer_store_dwordx4 v[8:11], v5, s[36:39], s60 offen sc1\n"
    "s_mov_b64 exec, -1\n"
    "s_cbranch_scc1 .Lkc\n"
    "buffer_store_dwordx4 v[8:11], v5, s[36:39], s60 offen offset:1024 sc1\n"
    "buffer_store_dwordx4 v[8:11], v5, s[36:39], s60 offen offset:2048 sc1\n"
    "buffer_store_dwordx4 v[8:11], v5, s[36:39], s60 offen offset:3072 sc1\n"
    ".Lkc:\n"
    "s_add_u32 s60, s56, 4\n"
    "s_sub_u32 s61, s60, 9\n"
    "s_cselect_b32 s60, s60, s61\n"
    "s_lshl_b32 s60, s60, 7\n"
    "buffer_load_dwordx4 v[140:143], v92, s[88:91], s60 offen offset:0\n"
    "buffer_load_dwordx4 v[144:147], v93, s[88:91], s60 offen offset:1024\n"
    "buffer_load_dwordx4 v[148:151], v94, s[88:91], s60 offen offset:2048\n"
    "buffer_load_dwordx4 v[152:155], v95, s[88:91], s60 offen offset:3072\n"
    "s_add_u32 s60, s56, 5\n"
    "s_sub_u32 s61, s60, 9\n"
    "s_cselect_b32 s60, s60, s61\n"
    "s_lshl_b32 s60, s60, 7\n"
    "buffer_load_dwordx4 v[156:159], v92, s[88:91], s60 offen offset:0\n"
    "buffer_load_dwordx4 v[160:163], v93, s[88:91], s60 offen offset:1024\n"
    "buffer_load_dwordx4 v[164:167], v94, s[88:91], s60 offen offset:2048\n"
    "buffer_load_dwordx4 v[168:171], v95, s[88:91], s60 offen offset:3072\n"
    "s_barrier\n"
    "s_mov_b32 s82, 1\n"
    ".Lwoa:\n"
    "s_mul_i32 s60, s84, 82944\n"
    "s_add_u32 s60, s60, s83\n"
    "s_mov_b32 m0, s85\n"
    "s_nop 0\n"
    "buffer_load_dwordx4 v97, s[48:51], s60 offen offset:0 lds\n"
    "buffer_load_dwordx4 v98, s[48:51], s60 offen offset:1024 lds\n"
    "buffer_load_dwordx4 v99, s[48:51], s60 offen offset:2048 lds\n"
    "buffer_load_dwordx4 v100, s[48:51], s60 offen offset:3072 lds\n"
    "s_add_u32 s84, s84, 2\n"
    "s_and_b32 s84, s84, 7\n"
    "s_add_u32 s85, s85, 16384\n"
    "s_sub_u32 s82, s82, 1\n"
    "s_cmp_lg_u32 s82, 0\n"
    "s_cbranch_scc1 .Lwoa\n"
    "v_mul_u32_u24 v24, 12800, v3\n"
    "v_lshlrev_b32 v25, 4, v4\n"
    "v_add_u32 v24, v24, v25\n"
    "s_lshl2_add_u32 s68, s55, s35\n"
    "s_lshl_b32 s69, s68, 8\n"
    "s_mul_i32 s60, s57, 204800\n"
    "s_nop 0\n"
    "s_add_u32 s60, s60, s69\n"
    "s_add_u32 s64, s60, 262144\n"
    "s_add_u32 s65, s64, 51200\n"
    "s_add_u32 s66, s64, 102400\n"
    "s_add_u32 s67, s64, 153600\n"
    "s_mul_i32 s70, s57, 12800\n"
    "s_add_u32 s70, s70, 131072\n"
    "s_add_u32 s69, s69, s70\n"
    "s_add_u32 s60, s34, 1\n"
    "v_mov_b32 v60, s60\n"
    "v_mov_b32 v61, s60\n"
    "v_mov_b32 v62, s60\n"
    "v_mov_b32 v63, s60\n"
    "s_cmp_lt_u32 s68, 50\n"
    "s_cbranch_scc0 .Lpre_t\n"
    "s_mov_b64 exec, 0xffff\n"
    "buffer_store_dwordx4 v[60:63], v25, s[36:39], s69 offen sc1\n"
    "s_mov_b64 exec, -1\n"
    ".Lpre_t:\n"
    "s_add_u32 s60, s56, 6\n"
    "s_sub_u32 s61, s60, 9\n"
    "s_cselect_b32 s60, s60, s61\n"
    "s_lshl_b32 s60, s60, 7\n"
    "buffer_load_dwordx4 v[172:175], v92, s[88:91], s60 offen offset:0\n"
    "buffer_load_dwordx4 v[176:179], v93, s[88:91], s60 offen offset:1024\n"
    "buffer_load_dwordx4 v[180:183], v94, s[88:91], s60 offen offset:2048\n"
    "buffer_load_dwordx4 v[184:187], v95, s[88:91], s60 offen offset:3072\n"
    "s_add_u32 s60, s56, 7\n"
    "s_sub_u32 s61, s60, 9\n"
    "s_cselect_b32 s60, s60, s61\n"
    "s_lshl_b32 s60, s60, 7\n"
    "buffer_load_dwordx4 v[188:191], v92, s[88:91], s60 offen offset:0\n"
    "buffer_load_dwordx4 v[192:195], v93, s[88:91], s60 offen offset:1024\n"
    "buffer_load_dwordx4 v[196:199], v94, s[88:91], s60 offen offset:2048\n"
    "buffer_load_dwordx4 v[200:203], v95, s[88:91], s60 offen offset:3072\n"
    "s_add_u32 s60, s56, 8\n"
    "s_sub_u32 s61, s60, 9\n"
    "s_cselect_b32 s60, s60, s61\n"
    "s_lshl_b32 s60, s60, 7\n"
    "buffer_load_dwordx4 v[204:207], v92, s[88:91], s60 offen offset:0\n"
    "buffer_load_dwordx4 v[208:211], v93, s[88:91], s60 offen offset:1024\n"
    "buffer_load_dwordx4 v[212:215], v94, s[88:91], s60 offen offset:2048\n"
    "buffer_load_dwordx4 v[216:219], v95, s[88:91], s60 offen offset:3072\n"
    "s_add_u32 s60, s56, 0\n"
    "s_sub_u32 s61, s60, 9\n"
    "s_cselect_b32 s60, s60, s61\n"
    "s_lshl_b32 s60, s60, 7\n"
    "buffer_load_dwordx4 v[220:223], v92, s[88:91], s60 offen offset:0\n"
    "buffer_load_dwordx4 v[224:227], v93, s[88:91], s60 offen offset:1024\n"
    "buffer_load_dwordx4 v[228:231], v94, s[88:91], s60 offen offset:2048\n"
    "buffer_load_dwordx4 v[232:235], v95, s[88:91], s60 offen offset:3072\n"
    "s_add_u32 s60, s56, 1\n"
    "s_sub_u32 s61, s60, 9\n"
    "s_cselect_b32 s60, s60, s61\n"
    "s_lshl_b32 s60, s60, 7\n"
    "buffer_load_dwordx4 v[236:239], v92, s[88:91], s60 offen offset:0\n"
    "buffer_load_dwordx4 v[240:243], v93, s[88:91], s60 offen offset:1024\n"
    "buffer_load_dwordx4 v[244:247], v94, s[88:91], s60 offen offset:2048\n"
    "buffer_load_dwordx4 v[248:251], v95, s[88:91], s60 offen offset:3072\n"
    "s_waitcnt vmcnt(8)\n"
    "s_barrier\n"
    "s_cmp_lt_u32 s35, 2\n"
    "s_cbranch_scc1 .Lxa\n"
    "s_mul_i32 s61, s58, 73728\n"
    "s_add_u32 s61, s61, s83\n"
    "s_mov_b32 s40, s4\n"
    "s_and_b32 s41, s5, 0xffff\n"
    "s_mov_b32 s42, 2359296\n"
    "s_mov_b32 s43, 0x20000\n"
    "s_mov_b32 s44, s30\n"
    "s_and_b32 s45, s31, 0xffff\n"
    "s_mov_b32 s46, 1152\n"
    "s_mov_b32 s47, 0x20000\n"
    "s_add_u32 m0, s81, 135552\n"
    "s_nop 0\n"
    "buffer_load_dwordx4 v102, s[40:43], s61 offen lds\n"
    "s_add_u32 m0, s81, 136576\n"
    "s_nop 0\n"
    "buffer_load_dwordx4 v103, s[40:43], s61 offen lds\n"
    "s_add_u32 m0, s81, 137600\n"
    "s_nop 0\n"
    "buffer_load_dwordx4 v104, s[40:43], s61 offen lds\n"
    "s_add_u32 m0, s81, 138624\n"
    "s_nop 0\n"
    "buffer_load_dwordx4 v105, s[40:43], s61 offen lds\n"
    "s_cmp_eq_u32 s35, 2\n"
    "s_cbranch_scc0 .Lxa\n"
    "v_lshlrev_b32_e64 v5, 4, v0\n"
    "s_mov_b32 m0, 143744\n"
    "s_mov_b64 exec, 0xff\n"
    "buffer_load_dwordx4 v5, s[44:47], s83 offen lds\n"
    "s_mov_b64 exec, -1\n"
    ".Lxa:\n"
    "s_barrier\n"
    "s_setprio 3\n"
    "v_lshlrev_b32 v5, 4, v0\n"
    "v_add_u32 v5, s86, v5\n"
    "s_waitcnt vmcnt(0)\n"
    "ds_write_b128 v5, v[220:223] offset:0\n"
    "ds_write_b128 v5, v[224:227] offset:1024\n"
    "ds_write_b128 v5, v[228:231] offset:2048\n"
    "ds_write_b128 v5, v[232:235] offset:3072\n"
    "ds_write_b128 v5, v[236:239] offset:16384\n"
    "ds_write_b128 v5, v[240:243] offset:17408\n"
    "ds_write_b128 v5, v[244:247] offset:18432\n"
    "ds_write_b128 v5, v[248:251] offset:19456\n"
    "s_waitcnt lgkmcnt(0)\n"
    "s_mov_b32 s82, 2\n"
    ".Lwob:\n"
    "s_mul_i32 s60, s84, 82944\n"
    "s_add_u32 s60, s60, s83\n"
    "s_mov_b32 m0, s85\n"
    "s_nop 0\n"
    "buffer_load_dwordx4 v97, s[48:51], s60 offen offset:0 lds\n"
    "buffer_load_dwordx4 v98, s[48:51], s60 offen offset:1024 lds\n"
    "buffer_load_dwordx4 v99, s[48:51], s60 offen offset:2048 lds\n"
    "buffer_load_dwordx4 v100, s[48:51], s60 offen offset:3072 lds\n"
    "s_add_u32 s84, s84, 2\n"
    "s_and_b32 s84, s84, 7\n"
    "s_add_u32 s85, s85, 16384\n"
    "s_sub_u32 s82, s82, 1\n"
    "s_cmp_lg_u32 s82, 0\n"
    "s_cbranch_scc1 .Lwob\n"
    "s_sleep 4\n"
    "s_cmp_lt_u32 s68, 50\n"
    "s_cbranch_scc1 .Lhop1\n"
    "s_sleep 24\n"
    "s_waitcnt vmcnt(0)\n"
    "s_branch .Lhop2\n"
    ".Lhop1:\n"
    "s_mov_b32 s77, 262144\n"
    ".Lspin1:\n"
    "buffer_load_dwordx4 v[40:43], v24, s[36:39], s64 offen sc1\n"
    "buffer_load_dwordx4 v[44:47], v24, s[36:39], s65 offen sc1\n"
    "buffer_load_dwordx4 v[48:51], v24, s[36:39], s66 offen sc1\n"
    "buffer_load_dwordx4 v[52:55], v24, s[36:39], s67 offen sc1\n"
    "s_waitcnt vmcnt(3)\n"
    "v_xor_b32 v41, s34, v41\n"
    "v_xor_b32 v43, s34, v43\n"
    "v_or_b32 v64, v41, v43\n"
    "s_waitcnt vmcnt(2)\n"
    "v_xor_b32 v45, s34, v45\n"
    "v_xor_b32 v47, s34, v47\n"
    "v_or3_b32 v64, v45, v47, v64\n"
    "s_waitcnt vmcnt(1)\n"
    "v_xor_b32 v49, s34, v49\n"
    "v_xor_b32 v51, s34, v51\n"
    "v_or3_b32 v64, v49, v51, v64\n"
    "s_waitcnt vmcnt(0)\n"
    "v_xor_b32 v53, s34, v53\n"
    "v_xor_b32 v55, s34, v55\n"
    "v_or3_b32 v64, v53, v55, v64\n"
    "v_cmp_ne_u32 vcc, 0, v64\n"
    "s_nop 0\n"
    "s_cbranch_vccz .Lspun1\n"
    "s_sleep 1\n"
    "s_sub_u32 s77, s77, 1\n"
    "s_cmp_lg_u32 s77, 0\n"
    "s_cbranch_scc1 .Lspin1\n"
    ".Lspun1:\n"
    "v_and_b32 v41, 0xffff0000, v40\n"
    "v_and_b32 v43, 0xffff0000, v42\n"
    "v_and_b32 v45, 0xffff0000, v44\n"
    "v_and_b32 v47, 0xffff0000, v46\n"
    "v_and_b32 v49, 0xffff0000, v48\n"
    "v_and_b32 v51, 0xffff0000, v50\n"
    "v_and_b32 v53, 0xffff0000, v52\n"
    "v_and_b32 v55, 0xffff0000, v54\n"
    "v_lshlrev_b32 v40, 16, v40\n"
    "v_lshlrev_b32 v42, 16, v42\n"
    "v_lshlrev_b32 v44, 16, v44\n"
    "v_lshlrev_b32 v46, 16, v46\n"
    "v_lshlrev_b32 v48, 16, v48\n"
    "v_lshlrev_b32 v50, 16, v50\n"
    "v_lshlrev_b32 v52, 16, v52\n"
    "v_lshlrev_b32 v54, 16, v54\n"
    "v_pk_add_f32 v[40:41], v[40:41], v[44:45]\n"
    "v_pk_add_f32 v[48:49], v[48:49], v[52:53]\n"
    "v_pk_add_f32 v[42:43], v[42:43], v[46:47]\n"
    "v_pk_add_f32 v[50:51], v[50:51], v[54:55]\n"
    "v_pk_add_f32 v[40:41], v[40:41], v[48:49]\n"
    "v_pk_add_f32 v[42:43], v[42:43], v[50:51]\n"
    "v_mov_b32 v61, s34\n"
    "v_mov_b32 v63, s34\n"
    "v_permlane32_swap_b32 v40, v42\n"
    "v_permlane32_swap_b32 v41, v43\n"
    "v_pk_add_f32 v[40:41], v[40:41], v[42:43]\n"
    "v_mov_b32 v42, v40\n"
    "v_mov_b32 v43, v41\n"
    "s_nop 0\n"
    "v_permlane16_swap_b32 v40, v42\n"
    "v_permlane16_swap_b32_e64 v41, v43\n"
    "v_pk_add_f32 v[40:41], v[40:41], v[42:43]\n"
    "v_cvt_pk_bf16_f32 v60, v40, v41\n"
    "v_mov_b32 v62, v60\n"
    "s_nop 1\n"
    "v_permlane32_swap_b32_e64 v60, v62\n"
    "s_mov_b64 exec, 0xffff\n"
    "buffer_store_dwordx4 v[60:63], v25, s[36:39], s69 offen sc1\n"
    "s_mov_b64 exec, -1\n"
    ".Lhop2:\n"
    "s_barrier\n"
    "s_mov_b32 s82, 1\n"
    ".Lwoh:\n"
    "s_mul_i32 s60, s84, 82944\n"
    "s_add_u32 s60, s60, s83\n"
    "s_mov_b32 m0, s85\n"
    "s_nop 0\n"
    "buffer_load_dwordx4 v97, s[48:51], s60 offen offset:0 lds\n"
    "buffer_load_dwordx4 v98, s[48:51], s60 offen offset:1024 lds\n"
    "buffer_load_dwordx4 v99, s[48:51], s60 offen offset:2048 lds\n"
    "buffer_load_dwordx4 v100, s[48:51], s60 offen offset:3072 lds\n"
    "s_add_u32 s84, s84, 2\n"
    "s_and_b32 s84, s84, 7\n"
    "s_add_u32 s85, s85, 16384\n"
    "s_sub_u32 s82, s82, 1\n"
    "s_cmp_lg_u32 s82, 0\n"
    "s_cbranch_scc1 .Lwoh\n"
    "s_cmp_lt_u32 s35, 2\n"
    "s_cbranch_scc0 .Lwol\n"
    "s_mov_b32 m0, s85\n"
    "s_nop 0\n"
    "buffer_load_dwordx4 v102, s[48:51], s83 offen offset:0 lds\n"
    "buffer_load_dwordx4 v103, s[48:51], s83 offen offset:1024 lds\n"
    "buffer_load_dwordx4 v104, s[48:51], s83 offen offset:2048 lds\n"
    "buffer_load_dwordx4 v105, s[48:51], s83 offen offset:3072 lds\n"
    ".Lwol:\n"
    "s_lshl_b32 s60, s35, 6\n"
    "v_add_u32_e64 v66, 0, v0\n"
    "v_add_u32 v74, 256, v0\n"
    "v_add_u32 v82, 512, v0\n"
    "v_add_u32 v66, s60, v66\n"
    "v_add_u32 v74, s60, v74\n"
    "v_add_u32_e64 v82, s60, v82\n"
    "v_min_u32 v66, 759, v66\n"
    "v_min_u32 v74, 759, v74\n"
    "v_min_u32 v82, 759, v82\n"
    "v_subrev_u32 v70, 380, v66\n"
    "v_subrev_u32 v78, 380, v74\n"
    "v_subrev_u32 v86, 380, v82\n"
    "v_min_u32 v66, v66, v70\n"
    "v_min_u32 v74, v74, v78\n"
    "v_min_u32_e64 v82, v82, v86\n"
    "v_subrev_u32 v72, 320, v66\n"
    "v_subrev_u32 v80, 320, v74\n"
    "v_subrev_u32 v88, 320, v82\n"
    "v_lshrrev_b32 v67, 31, v70\n"
    "v_lshrrev_b32 v75, 31, v78\n"
    "v_lshrrev_b32_e64 v83, 31, v86\n"
    "v_mul_u32_u24 v70, 86, v72\n"
    "v_mul_u32_u24 v78, 86, v80\n"
    "v_mul_u32_u24 v86, 86, v88\n"
    "v_cmp_gt_u32 s[72:73], 60, v72\n"
    "v_cmp_gt_u32 s[74:75], 60, v80\n"
    "v_cmp_gt_u32 s[76:77], 60, v88\n"
    "v_bfe_u32 v70, v70, 8, 8\n"
    "v_lshrrev_b32 v68, 4, v66\n"
    "v_bfe_u32 v78, v78, 8, 8\n"
    "v_lshrrev_b32 v76, 4, v74\n"
    "v_bfe_u32 v86, v86, 8, 8\n"
    "v_lshrrev_b32_e64 v84, 4, v82\n"
    "v_cndmask_b32_e64 v68, v68, v70, s[72:73]\n"
    "v_cndmask_b32_e64 v76, v76, v78, s[74:75]\n"
    "v_cndmask_b32_e64 v84, v84, v86, s[76:77]\n"
    "v_cndmask_b32_e64 v70, 0, v70, s[72:73]\n"
    "v_cndmask_b32_e64 v78, 0, v78, s[74:75]\n"
    "v_cndmask_b32_e64 v86, 0, v86, s[76:77]\n"
    "v_add_u32 v66, v66, v70\n"
    "v_add_u32 v74, v74, v78\n"
    "v_add_u32 v82, v82, v86\n"
    "v_mul_u32_u24 v70, 52, v68\n"
    "v_mul_u32_u24 v78, 52, v76\n"
    "v_mul_u32_u24 v86, 52, v84\n"
    "v_lshrrev_b32 v70, 8, v70\n"
    "v_lshrrev_b32 v78, 8, v78\n"
    "v_lshrrev_b32 v86, 8, v86\n"
    "v_and_b32 v71, 2, v70\n"
    "v_and_b32 v69, 15, v66\n"
    "v_and_b32 v79, 2, v78\n"
    "v_and_b32 v77, 15, v74\n"
    "v_and_b32 v87, 2, v86\n"
    "v_and_b32 v85, 15, v82\n"
    "v_add_u32 v69, v69, v71\n"
    "v_add_u32 v77, v77, v79\n"
    "v_add_u32 v85, v85, v87\n"
    "v_and_b32 v72, 3, v66\n"
    "v_xor_b32 v71, 2, v71\n"
    "v_and_b32 v80, 3, v74\n"
    "v_xor_b32 v79, 2, v79\n"
    "v_and_b32 v88, 3, v82\n"
    "v_xor_b32 v87, 2, v87\n"
    "v_or_b32 v71, v71, v72\n"
    "v_or_b32 v79, v79, v80\n"
    "v_or_b32 v87, v87, v88\n"
    "v_and_b32 v71, 2, v71\n"
    "v_and_b32 v79, 2, v79\n"
    "v_and_b32 v87, 2, v87\n"
    "v_lshl_add_u32 v72, v71, 3, v72\n"
    "v_lshl_add_u32 v80, v79, 3, v80\n"
    "v_lshl_add_u32 v88, v87, 3, v88\n"
    "v_cndmask_b32_e64 v69, v69, v72, s[72:73]\n"
    "v_mul_u32_u24 v70, 920, v70\n"
    "v_cndmask_b32_e64 v77, v77, v80, s[74:75]\n"
    "v_mul_u32_u24 v78, 920, v78\n"
    "v_cndmask_b32_e64 v85, v85, v88, s[76:77]\n"
    "v_mul_u32_u24 v86, 920, v86\n"
    "v_xor_b32 v67, 1, v67\n"
    "v_mad_u32_u24 v29, v69, 48, v70\n"
    "v_xor_b32 v75, 1, v75\n"
    "v_mad_u32_u24 v30, v77, 48, v78\n"
    "v_xor_b32_e64 v83, 1, v83\n"
    "v_mad_u32_u24 v31, v85, 48, v86\n"
    "v_lshl_add_u32 v29, v68, 3, v29\n"
    "v_mul_u32_u24 v70, 3840, v67\n"
    "v_lshl_add_u32 v30, v76, 3, v30\n"
    "v_mul_u32_u24 v78, 3840, v75\n"
    "v_lshl_add_u32 v31, v84, 3, v31\n"
    "v_mul_u32_u24 v86, 3840, v83\n"
    "v_add_u32 v29, v29, v70\n"
    "v_add_u32 v30, v30, v78\n"
    "v_add_u32_e64 v31, v31, v86\n"
    "v_mul_u32_u24 v70, 6400, v67\n"
    "v_mul_u32_u24 v78, 6400, v75\n"
    "v_mul_u32_u24 v86, 6400, v83\n"
    "v_add_u32 v29, 156032, v29\n"
    "v_lshl_add_u32 v26, v66, 4, v70\n"
    "v_add_u32 v30, 156032, v30\n"
    "v_lshl_add_u32 v27, v74, 4, v78\n"
    "v_add_u32 v31, 156032, v31\n"
    "v_lshl_add_u32 v28, v82, 4, v86\n"
    "s_sleep 8\n"
    "s_mov_b32 s77, 262144\n"
    ".Lspin2:\n"
    "buffer_load_dwordx4 v[40:43], v26, s[36:39], s70 offen sc1\n"
    "buffer_load_dwordx4 v[44:47], v27, s[36:39], s70 offen sc1\n"
    "buffer_load_dwordx4 v[48:51], v28, s[36:39], s70 offen sc1\n"
    "s_waitcnt vmcnt(2)\n"
    "v_xor_b32 v41, s34, v41\n"
    "v_xor_b32 v43, s34, v43\n"
    "v_or_b32 v64, v41, v43\n"
    "v_mov_b32 v41, v42\n"
    "s_waitcnt vmcnt(1)\n"
    "v_xor_b32 v45, s34, v45\n"
    "v_xor_b32 v47, s34, v47\n"
    "v_or3_b32 v64, v45, v47, v64\n"
    "v_mov_b32 v45, v46\n"
    "s_waitcnt vmcnt(0)\n"
    "v_xor_b32 v49, s34, v49\n"
    "v_xor_b32 v51, s34, v51\n"
    "v_or3_b32 v64, v49, v51, v64\n"
    "v_mov_b32 v49, v50\n"
    "v_cmp_ne_u32_e64 vcc, 0, v64\n"
    "s_nop 0\n"
    "s_cbranch_vccz .Lspun2\n"
    "s_sleep 1\n"
    "s_sub_u32 s77, s77, 1\n"
    "s_cmp_lg_u32 s77, 0\n"
    "s_cbranch_scc1 .Lspin2\n"
    ".Lspun2:\n"
    "ds_write_b64 v29, v[40:41]\n"
    "ds_write_b64 v30, v[44:45]\n"
    "ds_write_b64 v31, v[48:49]\n"
    "v_mov_b32 v13, 155648\n"
    "v_mov_b32 v14, 0\n"
    "ds_write_b32 v13, v14\n"
    "s_waitcnt lgkmcnt(0)\n"
    "s_barrier\n"
    "s_setprio 0\n"
    "s_cmp_eq_u32 s35, 3\n"
    "s_cbranch_scc0 .Lh_nopoll4\n"
    "v_lshlrev_b32 v12, 7, v4\n"
    "v_lshlrev_b32 v6, 4, v0\n"
    "v_add_u32 v6, 154624, v6\n"
    "v_mov_b32 v8, 0\n"
    "v_mov_b32 v9, 1\n"
    "ds_write_b64 v6, v[8:9]\n"
    "s_mov_b32 m0, 154624\n"
    "s_waitcnt lgkmcnt(0)\n"
    "v_min_u32 v5, 11, v0\n"
    "v_lshlrev_b32 v5, 7, v5\n"
    "s_lshl_b32 s76, s58, 11\n"
    "s_add_u32 s76, s76, 1900544\n"
    "s_mov_b32 s77, 262144\n"
    ".Lspin3:\n"
    "buffer_load_dwordx2 v[6:7], v5, s[36:39], s76 offen sc1\n"
    "s_waitcnt vmcnt(0)\n"
    "v_xor_b32 v6, s34, v6\n"
    "v_xor_b32 v7, s34, v7\n"
    "v_or_b32 v6, v6, v7\n"
    "v_cmp_ne_u32_e64 vcc, 0, v6\n"
    "s_nop 0\n"
    "s_cbranch_vccz .Lspun3\n"
    "s_sleep 1\n"
    "s_sub_u32 s77, s77, 1\n"
    "s_cmp_lg_u32 s77, 0\n"
    "s_cbranch_scc1 .Lspin3\n"
    ".Lspun3:\n"
    "s_add_u32 s76, s76, 65536\n"
    "ds_write_b32 v13, v13\n"
    "s_waitcnt lgkmcnt(0)\n"
    ".Lh_nopoll4:\n"
    "v_lshlrev_b32 v5, 4, v0\n"
    "v_add_u32_e64 v5, s86, v5\n"
    "s_waitcnt vmcnt(0)\n"
    "s_barrier\n"
    "ds_write_b128 v5, v[108:111] offset:0\n"
    "ds_write_b128 v5, v[112:115] offset:1024\n"
    "ds_write_b128 v5, v[116:119] offset:2048\n"
    "ds_write_b128 v5, v[120:123] offset:3072\n"
    "ds_write_b128 v5, v[124:127] offset:16384\n"
    "ds_write_b128 v5, v[128:131] offset:17408\n"
    "ds_write_b128 v5, v[132:135] offset:18432\n"
    "ds_write_b128 v5, v[136:139] offset:19456\n"
    "ds_write_b128 v5, v[140:143] offset:32768\n"
    "ds_write_b128 v5, v[144:147] offset:33792\n"
    "ds_write_b128 v5, v[148:151] offset:34816\n"
    "ds_write_b128 v5, v[152:155] offset:35840\n"
    "s_waitcnt lgkmcnt(8)\n"
    "s_barrier ; relaxed\n"
    "s_waitcnt lgkmcnt(4)\n"
    "s_barrier ; relaxed\n"
    "ds_write_b128 v5, v[156:159] offset:0\n"
    "ds_write_b128 v5, v[160:163] offset:1024\n"
    "ds_write_b128 v5, v[164:167] offset:2048\n"
    "ds_write_b128 v5, v[168:171] offset:3072\n"
    "s_waitcnt lgkmcnt(4)\n"
    "s_barrier ; relaxed\n"
    "s_cmp_eq_u32 s35, 3\n"
    "s_cbranch_scc0 .Ldp44\n"
    "s_mov_b64 exec, 0xffff\n"
    "buffer_load_dwordx4 v12, s[36:39], s76 offen sc1 lds ; poll\n"
    "s_mov_b64 exec, -1\n"
    ".Ldp44:\n"
    "ds_write_b128 v5, v[172:175] offset:16384\n"
    "ds_write_b128 v5, v[176:179] offset:17408\n"
    "ds_write_b128 v5, v[180:183] offset:18432\n"
    "ds_write_b128 v5, v[184:187] offset:19456\n"
    "s_waitcnt lgkmcnt(4)\n"
    "s_barrier ; relaxed\n"
    "s_cmp_eq_u32 s35, 3\n"
    "s_cbranch_scc0 .Ldp45\n"
    "s_mov_b64 exec, 0xffff\n"
    "buffer_load_dwordx4 v12, s[36:39], s76 offen sc1 lds ; poll\n"
    "s_mov_b64 exec, -1\n"
    ".Ldp45:\n"
    "ds_write_b128 v5, v[188:191] offset:32768\n"
    "ds_write_b128 v5, v[192:195] offset:33792\n"
    "ds_write_b128 v5, v[196:199] offset:34816\n"
    "ds_write_b128 v5, v[200:203] offset:35840\n"
    "s_waitcnt lgkmcnt(4)\n"
    "s_barrier ; relaxed\n"
    "s_cmp_eq_u32 s35, 3\n"
    "s_cbranch_scc0 .Ldp46\n"
    "s_mov_b64 exec, 0xffff\n"
    "buffer_load_dwordx4 v12, s[36:39], s76 offen sc1 lds ; poll\n"
    "s_mov_b64 exec, -1\n"
    ".Ldp46:\n"
    "ds_write_b128 v5, v[204:207] offset:0\n"
    "ds_write_b128 v5, v[208:211] offset:1024\n"
    "ds_write_b128 v5, v[212:215] offset:2048\n"
    "ds_write_b128 v5, v[216:219] offset:3072\n"
    "s_waitcnt lgkmcnt(4)\n"
    "s_barrier ; relaxed\n"
    "s_waitcnt lgkmcnt(0)\n"
    "s_barrier\n"
    "s_waitcnt vmcnt(0)\n"
    "s_endpgm\n"
    "s_nop 0\n"
    ".Lld_done4:\n"
    "v_readfirstlane_b32 s34, v253\n"
    "s_nop 0\n"
    "s_add_u32 s34, s34, 0x9E3779B9\n"
    ".Lhelp2:\n"
    "s_add_u32 s60, s56, 2\n"
    "s_sub_u32 s61, s60, 9\n"
    "s_cselect_b32 s60, s60, s61\n"
    "s_lshl_b32 s60, s60, 7\n"
    "buffer_load_dwordx4 v[108:111], v92, s[88:91], s60 offen offset:0\n"
    "buffer_load_dwordx4 v[112:115], v93, s[88:91], s60 offen offset:1024\n"
    "buffer_load_dwordx4 v[116:119], v94, s[88:91], s60 offen offset:2048\n"
    "buffer_load_dwordx4 v[120:123], v95, s[88:91], s60 offen offset:3072\n"
    "buffer_load_dwordx4 v[124:127], v96, s[88:91], s60 offen offset:0\n"
    "s_add_u32 s60, s56, 3\n"
    "s_sub_u32 s61, s60, 9\n"
    "s_cselect_b32 s60, s60, s61\n"
    "s_lshl_b32 s60, s60, 7\n"
    "buffer_load_dwordx4 v[128:131], v92, s[88:91], s60 offen offset:0\n"
    "buffer_load_dwordx4 v[132:135], v93, s[88:91], s60 offen offset:1024\n"
    "buffer_load_dwordx4 v[136:139], v94, s[88:91], s60 offen offset:2048\n"
    "buffer_load_dwordx4 v[140:143], v95, s[88:91], s60 offen offset:3072\n"
    "buffer_load_dwordx4 v[144:147], v96, s[88:91], s60 offen offset:0\n"
    "s_cmp_eq_u32 s54, 7\n"
    "s_cbranch_scc1 .Lh_nob8\n"
    "s_waitcnt vmcnt(10)\n"
    "s_barrier\n"
    ".Lh_nob8:\n"
    "s_add_u32 s60, s56, 4\n"
    "s_sub_u32 s61, s60, 9\n"
    "s_cselect_b32 s60, s60, s61\n"
    "s_lshl_b32 s60, s60, 7\n"
    "buffer_load_dwordx4 v[148:151], v92, s[88:91], s60 offen offset:0\n"
    "buffer_load_dwordx4 v[152:155], v93, s[88:91], s60 offen offset:1024\n"
    "buffer_load_dwordx4 v[156:159], v94, s[88:91], s60 offen offset:2048\n"
    "buffer_load_dwordx4 v[160:163], v95, s[88:91], s60 offen offset:3072\n"
    "buffer_load_dwordx4 v[164:167], v96, s[88:91], s60 offen offset:0\n"
    "s_add_u32 s60, s56, 5\n"
    "s_sub_u32 s61, s60, 9\n"
    "s_cselect_b32 s60, s60, s61\n"
    "s_lshl_b32 s60, s60, 7\n"
    "buffer_load_dwordx4 v[168:171], v92, s[88:91], s60 offen offset:0\n"
    "buffer_load_dwordx4 v[172:175], v93, s[88:91], s60 offen offset:1024\n"
    "buffer_load_dwordx4 v[176:179], v94, s[88:91], s60 offen offset:2048\n"
    "buffer_load_dwordx4 v[180:183], v95, s[88:91], s60 offen offset:3072\n"
    "buffer_load_dwordx4 v[184:187], v96, s[88:91], s60 offen offset:0\n"
    "s_cmp_eq_u32 s35, 3\n"
    "s_cbranch_scc0 .Lh_nocs\n"
    "s_cmp_eq_u32 s54, 7\n"
    "s_cbranch_scc1 .Lh_nocs\n"
    "v_min_u32 v24, 47, v0\n"
    "s_lshl_b32 s60, s57, 3\n"
    "v_mul_u32_u24_e64 v70, 48, v24\n"
    "s_mul_i32 s60, s60, 768\n"
    "v_add_u32 v70, 159744, v70\n"
    "s_add_u32 s60, s60, 16384\n"
    "ds_read_b128 v[58:61], v70 offset:32\n"
    "s_mul_i32 s68, s55, 768\n"
    "v_lshlrev_b32 v25, 3, v24\n"
    "s_add_u32 s68, s68, s60\n"
    "v_lshlrev_b32 v24, 4, v24\n"
    "s_sub_u32 s61, s68, 5376\n"
    "s_cmp_ge_u32 s55, 7\n"
    "v_add_u32 v25, 147456, v25\n"
    "s_cselect_b32 s61, s61, 0x40000000\n"
    "s_cselect_b32 s69, s34, 0\n"
    "s_sub_u32 s62, s68, 4608\n"
    "s_cmp_ge_u32 s55, 6\n"
    "v_mov_b32 v67, s34\n"
    "s_cselect_b32 s62, s62, 0x40000000\n"
    "s_cselect_b32 s70, s34, 0\n"
    "s_sub_u32 s63, s68, 3840\n"
    "s_cmp_ge_u32 s55, 5\n"
    "v_mov_b32 v69, s34\n"
    "s_cselect_b32 s63, s63, 0x40000000\n"
    "s_cselect_b32 s71, s34, 0\n"
    "s_sub_u32 s64, s68, 3072\n"
    "s_cmp_ge_u32 s55, 4\n"
    "v_add_u32 v71, s61, v24\n"
    "s_cselect_b32 s64, s64, 0x40000000\n"
    "s_cselect_b32 s72, s34, 0\n"
    "s_sub_u32 s65, s68, 2304\n"
    "s_cmp_ge_u32 s55, 3\n"
    "v_add_u32 v72, s62, v24\n"
    "s_cselect_b32 s65, s65, 0x40000000\n"
    "s_cselect_b32 s73, s34, 0\n"
    "s_sub_u32 s66, s68, 1536\n"
    "s_waitcnt lgkmcnt(0)\n"
    "v_mov_b32 v224, v60\n"
    "v_mov_b32 v225, v61\n"
    "s_cmp_ge_u32 s55, 2\n"
    "v_pk_mul_f32 v[62:63], v[224:225], v[60:61] op_sel:[1,1] op_sel_hi:[1,0] neg_lo:[1,0]\n"
    "s_cselect_b32 s66, s66, 0x40000000\n"
    "v_pk_fma_f32 v[226:227], v[224:225], v[60:61], v[62:63] op_sel_hi:[0,1,1]\n"
    "s_cselect_b32 s74, s34, 0\n"
    "v_pk_mul_f32 v[62:63], v[226:227], v[60:61] op_sel:[1,1] op_sel_hi:[1,0] neg_lo:[1,0]\n"
    "v_pk_mul_f32 v[64:65], v[58:59], v[58:59] op_sel:[1,1] op_sel_hi:[1,0] neg_lo:[1,0]\n"
    "v_pk_fma_f32 v[228:229], v[226:227], v[60:61], v[62:63] op_sel_hi:[0,1,1]\n"
    "s_sub_u32 s67, s68, 768\n"
    "v_pk_mul_f32 v[62:63], v[228:229], v[60:61] op_sel:[1,1] op_sel_hi:[1,0] neg_lo:[1,0]\n"
    "v_pk_fma_f32 v[86:87], v[58:59], v[58:59], v[64:65] op_sel_hi:[0,1,1]\n"
    "v_pk_fma_f32 v[230:231], v[228:229], v[60:61], v[62:63] op_sel_hi:[0,1,1]\n"
    "s_cmp_ge_u32 s55, 1\n"
    "v_pk_mul_f32 v[62:63], v[230:231], v[60:61] op_sel:[1,1] op_sel_hi:[1,0] neg_lo:[1,0]\n"
    "v_pk_mul_f32 v[64:65], v[86:87], v[86:87] op_sel:[1,1] op_sel_hi:[1,0] neg_lo:[1,0]\n"
    "v_pk_fma_f32 v[232:233], v[230:231], v[60:61], v[62:63] op_sel_hi:[0,1,1]\n"
    "s_cselect_b32 s67, s67, 0x40000000\n"
    "v_pk_mul_f32 v[62:63], v[232:233], v[60:61] op_sel:[1,1] op_sel_hi:[1,0] neg_lo:[1,0]\n"
    "v_pk_fma_f32 v[86:87], v[86:87], v[86:87], v[64:65] op_sel_hi:[0,1,1]\n"
    "v_pk_fma_f32 v[234:235], v[232:233], v[60:61], v[62:63] op_sel_hi:[0,1,1]\n"
    "v_add_u32 v73, s63, v24\n"
    "v_add_u32 v74, s64, v24\n"
    "v_add_u32 v75, s65, v24\n"
    "v_add_u32 v76, s66, v24\n"
    "s_cselect_b32 s75, s34, 0\n"
    "v_add_u32 v77, s67, v24\n"
    ".Lh_nocs:\n"
    "s_barrier\n"
    "s_cmp_lt_u32 s35, 2\n"
    "s_cbranch_scc1 .Lxb\n"
    "s_mul_i32 s61, s58, 73728\n"
    "s_add_u32 s61, s61, s83\n"
    "s_mov_b32 s40, s4\n"
    "s_and_b32 s41, s5, 0xffff\n"
    "s_mov_b32 s42, 2359296\n"
    "s_mov_b32 s43, 0x20000\n"
    "s_mov_b32 s44, s30\n"
    "s_and_b32 s45, s31, 0xffff\n"
    "s_mov_b32 s46, 1152\n"
    "s_mov_b32 s47, 0x20000\n"
    "s_add_u32 m0, s81, 40960\n"
    "s_nop 0\n"
    "buffer_load_dwordx4 v102, s[40:43], s61 offen lds\n"
    "s_add_u32 m0, s81, 41984\n"
    "s_nop 0\n"
    "buffer_load_dwordx4 v103, s[40:43], s61 offen lds\n"
    "s_add_u32 m0, s81, 43008\n"
    "s_nop 0\n"
    "buffer_load_dwordx4 v104, s[40:43], s61 offen lds\n"
    "s_add_u32 m0, s81, 44032\n"
    "s_nop 0\n"
    "buffer_load_dwordx4 v105, s[40:43], s61 offen lds\n"
    "s_add_u32 m0, s81, 45056\n"
    "s_nop 0\n"
    "buffer_load_dwordx4 v106, s[40:43], s61 offen lds\n"
    "s_cmp_eq_u32 s35, 2\n"
    "s_cbranch_scc0 .Lxb\n"
    "v_lshlrev_b32 v5, 4, v0\n"
    "s_mov_b32 m0, 51200\n"
    "s_mov_b64 exec, 0x3ff\n"
    "buffer_load_dwordx4 v5, s[44:47], s83 offen lds\n"
    "s_mov_b64 exec, -1\n"
    ".Lxb:\n"
    "s_add_u32 s60, s56, 0\n"
    "s_sub_u32 s61, s60, 9\n"
    "s_cselect_b32 s60, s60, s61\n"
    "s_lshl_b32 s60, s60, 7\n"
    "s_add_u32 m0, s86, 0\n"
    "s_nop 0\n"
    "buffer_load_dwordx4 v92, s[88:91], s60 offen offset:0 lds\n"
    "buffer_load_dwordx4 v93, s[88:91], s60 offen offset:1024 lds\n"
    "buffer_load_dwordx4 v94, s[88:91], s60 offen offset:2048 lds\n"
    "buffer_load_dwordx4 v95, s[88:91], s60 offen offset:3072 lds\n"
    "s_add_u32 m0, s86, 4096\n"
    "s_nop 0\n"
    "buffer_load_dwordx4 v96, s[88:91], s60 offen offset:0 lds\n"
    "s_add_u32 s60, s56, 1\n"
    "s_sub_u32 s61, s60, 9\n"
    "s_cselect_b32 s60, s60, s61\n"
    "s_lshl_b32 s60, s60, 7\n"
    "s_add_u32 m0, s86, 20480\n"
    "s_nop 0\n"
    "buffer_load_dwordx4 v92, s[88:91], s60 offen offset:0 lds\n"
    "buffer_load_dwordx4 v93, s[88:91], s60 offen offset:1024 lds\n"
    "buffer_load_dwordx4 v94, s[88:91], s60 offen offset:2048 lds\n"
    "buffer_load_dwordx4 v95, s[88:91], s60 offen offset:3072 lds\n"
    "s_add_u32 m0, s86, 24576\n"
    "s_nop 0\n"
    "buffer_load_dwordx4 v96, s[88:91], s60 offen offset:0 lds\n"
    "s_mov_b32 s82, 1\n"
    ".Lwoc:\n"
    "s_mul_i32 s60, s84, 82944\n"
    "s_add_u32 s60, s60, s83\n"
    "s_mov_b32 m0, s85\n"
    "s_nop 0\n"
    "buffer_load_dwordx4 v97, s[48:51], s60 offen offset:0 lds\n"
    "buffer_load_dwordx4 v98, s[48:51], s60 offen offset:1024 lds\n"
    "buffer_load_dwordx4 v99, s[48:51], s60 offen offset:2048 lds\n"
    "buffer_load_dwordx4 v100, s[48:51], s60 offen offset:3072 lds\n"
    "s_add_u32 m0, s85, 4096\n"
    "s_nop 0\n"
    "buffer_load_dwordx4 v101, s[48:51], s60 offen offset:0 lds\n"
    "s_add_u32 s84, s84, 2\n"
    "s_and_b32 s84, s84, 7\n"
    "s_add_u32 s85, s85, 20480\n"
    "s_sub_u32 s82, s82, 1\n"
    "s_cmp_lg_u32 s82, 0\n"
    "s_cbranch_scc1 .Lwoc\n"
    "s_cmp_eq_u32 s54, 7\n"
    "s_cbranch_scc1 .Lh_sp\n"
    "s_cmp_eq_u32 s35, 3\n"
    "s_cbranch_scc0 .Lh_e1\n"
    "s_setprio 3\n"
    "s_barrier\n"
    "ds_read_b64 v[26:27], v25 offset:0\n"
    "ds_read_b64 v[34:35], v25 offset:1536\n"
    "ds_read_b64 v[28:29], v25 offset:384\n"
    "ds_read_b64 v[36:37], v25 offset:1920\n"
    "ds_read_b64 v[42:43], v25 offset:3072\n"
    "ds_read_b64 v[44:45], v25 offset:3456\n"
    "ds_read_b64 v[30:31], v25 offset:768\n"
    "ds_read_b64 v[38:39], v25 offset:2304\n"
    "ds_read_b64 v[50:51], v25 offset:4608\n"
    "ds_read_b64 v[52:53], v25 offset:4992\n"
    "ds_read_b64 v[46:47], v25 offset:3840\n"
    "ds_read_b64 v[32:33], v25 offset:1152\n"
    "ds_read_b64 v[40:41], v25 offset:2688\n"
    "ds_read_b64 v[54:55], v25 offset:5376\n"
    "ds_read_b64 v[48:49], v25 offset:4224\n"
    "ds_read_b64 v[56:57], v25 offset:5760\n"
    "s_waitcnt lgkmcnt(13)\n"
    "v_pk_fma_f32 v[216:217], v[58:59], v[26:27], v[28:29] op_sel:[1,1,0] op_sel_hi:[1,0,1] neg_lo:[1,0,0]\n"
    "v_pk_fma_f32 v[78:79], v[58:59], v[26:27], v[216:217] op_sel_hi:[0,1,1]\n"
    "s_waitcnt lgkmcnt(12)\n"
    "v_pk_fma_f32 v[218:219], v[58:59], v[34:35], v[36:37] op_sel:[1,1,0] op_sel_hi:[1,0,1] neg_lo:[1,0,0]\n"
    "v_pk_fma_f32 v[80:81], v[58:59], v[34:35], v[218:219] op_sel_hi:[0,1,1]\n"
    "s_waitcnt lgkmcnt(10)\n"
    "v_pk_fma_f32 v[220:221], v[58:59], v[42:43], v[44:45] op_sel:[1,1,0] op_sel_hi:[1,0,1] neg_lo:[1,0,0]\n"
    "v_pk_fma_f32 v[82:83], v[58:59], v[42:43], v[220:221] op_sel_hi:[0,1,1]\n"
    "s_waitcnt lgkmcnt(9)\n"
    "v_pk_fma_f32 v[216:217], v[58:59], v[78:79], v[30:31] op_sel:[1,1,0] op_sel_hi:[1,0,1] neg_lo:[1,0,0]\n"
    "s_waitcnt lgkmcnt(8)\n"
    "v_pk_fma_f32 v[218:219], v[58:59], v[80:81], v[38:39] op_sel:[1,1,0] op_sel_hi:[1,0,1] neg_lo:[1,0,0]\n"
    "v_pk_fma_f32 v[78:79], v[58:59], v[78:79], v[216:217] op_sel_hi:[0,1,1]\n"
    "v_pk_fma_f32 v[80:81], v[58:59], v[80:81], v[218:219] op_sel_hi:[0,1,1]\n"
    "s_waitcnt lgkmcnt(6)\n"
    "v_pk_fma_f32 v[222:223], v[58:59], v[50:51], v[52:53] op_sel:[1,1,0] op_sel_hi:[1,0,1] neg_lo:[1,0,0]\n"
    "s_waitcnt lgkmcnt(5)\n"
    "v_pk_fma_f32 v[220:221], v[58:59], v[82:83], v[46:47] op_sel:[1,1,0] op_sel_hi:[1,0,1] neg_lo:[1,0,0]\n"
    "v_pk_fma_f32 v[84:85], v[58:59], v[50:51], v[222:223] op_sel_hi:[0,1,1]\n"
    "s_waitcnt lgkmcnt(4)\n"
    "v_pk_fma_f32 v[216:217], v[58:59], v[78:79], v[32:33] op_sel:[1,1,0] op_sel_hi:[1,0,1] neg_lo:[1,0,0]\n"
    "s_waitcnt lgkmcnt(3)\n"
    "v_pk_fma_f32 v[218:219], v[58:59], v[80:81], v[40:41] op_sel:[1,1,0] op_sel_hi:[1,0,1] neg_lo:[1,0,0]\n"
    "v_pk_fma_f32 v[78:79], v[58:59], v[78:79], v[216:217] op_sel_hi:[0,1,1]\n"
    "v_pk_fma_f32 v[80:81], v[58:59], v[80:81], v[218:219] op_sel_hi:[0,1,1]\n"
    "v_pk_fma_f32 v[82:83], v[58:59], v[82:83], v[220:221] op_sel_hi:[0,1,1]\n"
    "s_waitcnt lgkmcnt(2)\n"
    "v_pk_fma_f32 v[222:223], v[58:59], v[84:85], v[54:55] op_sel:[1,1,0] op_sel_hi:[1,0,1] neg_lo:[1,0,0]\n"
    "s_waitcnt lgkmcnt(1)\n"
    "v_pk_fma_f32 v[220:221], v[58:59], v[82:83], v[48:49] op_sel:[1,1,0] op_sel_hi:[1,0,1] neg_lo:[1,0,0]\n"
    "v_pk_fma_f32 v[64:65], v[86:87], v[78:79], v[80:81] op_sel:[1,1,0] op_sel_hi:[1,0,1] neg_lo:[1,0,0]\n"
    "v_pk_fma_f32 v[82:83], v[58:59], v[82:83], v[220:221] op_sel_hi:[0,1,1]\n"
    "v_pk_fma_f32 v[84:85], v[58:59], v[84:85], v[222:223] op_sel_hi:[0,1,1]\n"
    "v_pk_fma_f32 v[62:63], v[86:87], v[78:79], v[64:65] op_sel_hi:[0,1,1]\n"
    "s_waitcnt lgkmcnt(0)\n"
    "v_pk_fma_f32 v[222:223], v[58:59], v[84:85], v[56:57] op_sel:[1,1,0] op_sel_hi:[1,0,1] neg_lo:[1,0,0]\n"
    "v_pk_fma_f32 v[64:65], v[86:87], v[62:63], v[82:83] op_sel:[1,1,0] op_sel_hi:[1,0,1] neg_lo:[1,0,0]\n"
    "v_pk_fma_f32 v[84:85], v[58:59], v[84:85], v[222:223] op_sel_hi:[0,1,1]\n"
    "v_pk_fma_f32 v[62:63], v[86:87], v[62:63], v[64:65] op_sel_hi:[0,1,1]\n"
    "v_pk_fma_f32 v[64:65], v[86:87], v[62:63], v[84:85] op_sel:[1,1,0] op_sel_hi:[1,0,1] neg_lo:[1,0,0]\n"
    "v_pk_fma_f32 v[62:63], v[86:87], v[62:63], v[64:65] op_sel_hi:[0,1,1]\n"
    "v_mov_b32 v66, v62\n"
    "v_mov_b32 v68, v63\n"
    "buffer_store_dwordx4 v[66:69], v24, s[36:39], s68 offen sc1\n"
    "s_mov_b32 s82, 1\n"
    ".Lwsh3:\n"
    "s_mul_i32 s60, s84, 82944\n"
    "s_add_u32 s60, s60, s83\n"
    "s_mov_b32 m0, s85\n"
    "s_nop 0\n"
    "buffer_load_dwordx4 v97, s[48:51], s60 offen offset:0 lds\n"
    "buffer_load_dwordx4 v98, s[48:51], s60 offen offset:1024 lds\n"
    "buffer_load_dwordx4 v99, s[48:51], s60 offen offset:2048 lds\n"
    "buffer_load_dwordx4 v100, s[48:51], s60 offen offset:3072 lds\n"
    "s_add_u32 m0, s85, 4096\n"
    "s_nop 0\n"
    "buffer_load_dwordx4 v101, s[48:51], s60 offen offset:0 lds\n"
    "s_add_u32 s84, s84, 2\n"
    "s_and_b32 s84, s84, 7\n"
    "s_add_u32 s85, s85, 20480\n"
    "s_sub_u32 s82, s82, 1\n"
    "s_cmp_lg_u32 s82, 0\n"
    "s_cbranch_scc1 .Lwsh3\n"
    "v_mov_b32 v62, 0\n"
    "v_mov_b32 v63, 0\n"
    "ds_write_b64 v25, v[62:63] offset:6144\n"
    "ds_write_b64 v25, v[26:27] offset:6528\n"
    "v_pk_fma_f32 v[64:65], v[58:59], v[26:27], v[28:29] op_sel:[1,1,0] op_sel_hi:[1,0,1] neg_lo:[1,0,0]\n"
    "v_pk_fma_f32 v[28:29], v[58:59], v[26:27], v[64:65] op_sel_hi:[0,1,1]\n"
    "ds_write_b64 v25, v[28:29] offset:6912\n"
    "v_pk_fma_f32 v[64:65], v[58:59], v[28:29], v[30:31] op_sel:[1,1,0] op_sel_hi:[1,0,1] neg_lo:[1,0,0]\n"
    "v_pk_fma_f32 v[30:31], v[58:59], v[28:29], v[64:65] op_sel_hi:[0,1,1]\n"
    "ds_write_b64 v25, v[30:31] offset:7296\n"
    "v_pk_fma_f32 v[64:65], v[58:59], v[30:31], v[32:33] op_sel:[1,1,0] op_sel_hi:[1,0,1] neg_lo:[1,0,0]\n"
    "v_pk_fma_f32 v[32:33], v[58:59], v[30:31], v[64:65] op_sel_hi:[0,1,1]\n"
    "ds_write_b64 v25, v[32:33] offset:7680\n"
    "v_pk_fma_f32 v[64:65], v[58:59], v[32:33], v[34:35] op_sel:[1,1,0] op_sel_hi:[1,0,1] neg_lo:[1,0,0]\n"
    "v_pk_fma_f32 v[34:35], v[58:59], v[32:33], v[64:65] op_sel_hi:[0,1,1]\n"
    "ds_write_b64 v25, v[34:35] offset:8064\n"
    "v_pk_fma_f32 v[64:65], v[58:59], v[34:35], v[36:37] op_sel:[1,1,0] op_sel_hi:[1,0,1] neg_lo:[1,0,0]\n"
    "v_pk_fma_f32 v[36:37], v[58:59], v[34:35], v[64:65] op_sel_hi:[0,1,1]\n"
    "ds_write_b64 v25, v[36:37] offset:8448\n"
    "v_pk_fma_f32 v[64:65], v[58:59], v[36:37], v[38:39] op_sel:[1,1,0] op_sel_hi:[1,0,1] neg_lo:[1,0,0]\n"
    "v_pk_fma_f32 v[38:39], v[58:59], v[36:37], v[64:65] op_sel_hi:[0,1,1]\n"
    "ds_write_b64 v25, v[38:39] offset:8832\n"
    "v_pk_fma_f32 v[64:65], v[58:59], v[38:39], v[40:41] op_sel:[1,1,0] op_sel_hi:[1,0,1] neg_lo:[1,0,0]\n"
    "v_pk_fma_f32 v[40:41], v[58:59], v[38:39], v[64:65] op_sel_hi:[0,1,1]\n"
    "ds_write_b64 v25, v[40:41] offset:9216\n"
    "v_pk_fma_f32 v[64:65], v[58:59], v[40:41], v[42:43] op_sel:[1,1,0] op_sel_hi:[1,0,1] neg_lo:[1,0,0]\n"
    "v_pk_fma_f32 v[42:43], v[58:59], v[40:41], v[64:65] op_sel_hi:[0,1,1]\n"
    "ds_write_b64 v25, v[42:43] offset:9600\n"
    "v_pk_fma_f32 v[64:65], v[58:59], v[42:43], v[44:45] op_sel:[1,1,0] op_sel_hi:[1,0,1] neg_lo:[1,0,0]\n"
    "v_pk_fma_f32 v[44:45], v[58:59], v[42:43], v[64:65] op_sel_hi:[0,1,1]\n"
    "ds_write_b64 v25, v[44:45] offset:9984\n"
    "v_pk_fma_f32 v[64:65], v[58:59], v[44:45], v[46:47] op_sel:[1,1,0] op_sel_hi:[1,0,1] neg_lo:[1,0,0]\n"
    "v_pk_fma_f32 v[46:47], v[58:59], v[44:45], v[64:65] op_sel_hi:[0,1,1]\n"
    "ds_write_b64 v25, v[46:47] offset:10368\n"
    "v_pk_fma_f32 v[64:65], v[58:59], v[46:47], v[48:49] op_sel:[1,1,0] op_sel_hi:[1,0,1] neg_lo:[1,0,0]\n"
    "v_pk_fma_f32 v[48:49], v[58:59], v[46:47], v[64:65] op_sel_hi:[0,1,1]\n"
    "ds_write_b64 v25, v[48:49] offset:10752\n"
    "v_pk_fma_f32 v[64:65], v[58:59], v[48:49], v[50:51] op_sel:[1,1,0] op_sel_hi:[1,0,1] neg_lo:[1,0,0]\n"
    "v_pk_fma_f32 v[50:51], v[58:59], v[48:49], v[64:65] op_sel_hi:[0,1,1]\n"
    "ds_write_b64 v25, v[50:51] offset:11136\n"
    "v_pk_fma_f32 v[64:65], v[58:59], v[50:51], v[52:53] op_sel:[1,1,0] op_sel_hi:[1,0,1] neg_lo:[1,0,0]\n"
    "v_pk_fma_f32 v[52:53], v[58:59], v[50:51], v[64:65] op_sel_hi:[0,1,1]\n"
    "ds_write_b64 v25, v[52:53] offset:11520\n"
    "v_pk_fma_f32 v[64:65], v[58:59], v[52:53], v[54:55] op_sel:[1,1,0] op_sel_hi:[1,0,1] neg_lo:[1,0,0]\n"
    "v_pk_fma_f32 v[54:55], v[58:59], v[52:53], v[64:65] op_sel_hi:[0,1,1]\n"
    "ds_write_b64 v25, v[54:55] offset:11904\n"
    "s_mov_b32 s77, 262144\n"
    ".Lspin4:\n"
    "buffer_load_dwordx4 v[188:191], v71, s[36:39], 0 offen sc1\n"
    "buffer_load_dwordx4 v[192:195], v72, s[36:39], 0 offen sc1\n"
    "buffer_load_dwordx4 v[196:199], v73, s[36:39], 0 offen sc1\n"
    "buffer_load_dwordx4 v[200:203], v74, s[36:39], 0 offen sc1\n"
    "buffer_load_dwordx4 v[204:207], v75, s[36:39], 0 offen sc1\n"
    "buffer_load_dwordx4 v[208:211], v76, s[36:39], 0 offen sc1\n"
    "buffer_load_dwordx4 v[212:215], v77, s[36:39], 0 offen sc1\n"
    "s_waitcnt vmcnt(6)\n"
    "v_xor_b32 v189, s69, v189\n"
    "v_xor_b32 v191, s69, v191\n"
    "v_or_b32 v70, v189, v191\n"
    "v_mov_b32 v189, v190\n"
    "s_waitcnt vmcnt(5)\n"
    "v_xor_b32 v193, s70, v193\n"
    "v_xor_b32 v195, s70, v195\n"
    "v_or3_b32 v70, v193, v195, v70\n"
    "v_mov_b32 v193, v194\n"
    "s_waitcnt vmcnt(4)\n"
    "v_xor_b32 v197, s71, v197\n"
    "v_xor_b32 v199, s71, v199\n"
    "v_or3_b32 v70, v197, v199, v70\n"
    "v_mov_b32 v197, v198\n"
    "s_waitcnt vmcnt(3)\n"
    "v_xor_b32 v201, s72, v201\n"
    "v_xor_b32 v203, s72, v203\n"
    "v_or3_b32 v70, v201, v203, v70\n"
    "v_mov_b32 v201, v202\n"
    "s_waitcnt vmcnt(2)\n"
    "v_xor_b32 v205, s73, v205\n"
    "v_xor_b32 v207, s73, v207\n"
    "v_or3_b32 v70, v205, v207, v70\n"
    "v_mov_b32 v205, v206\n"
    "s_waitcnt vmcnt(1)\n"
    "v_xor_b32 v209, s74, v209\n"
    "v_xor_b32 v211, s74, v211\n"
    "v_or3_b32 v70, v209, v211, v70\n"
    "v_mov_b32 v209, v210\n"
    "s_waitcnt vmcnt(0)\n"
    "v_xor_b32 v213, s75, v213\n"
    "v_xor_b32 v215, s75, v215\n"
    "v_or3_b32 v70, v213, v215, v70\n"
    "v_mov_b32 v213, v214\n"
    "v_cmp_ne_u32 vcc, 0, v70\n"
    "s_nop 0\n"
    "s_cbranch_vccz .Lspun4\n"
    "s_sleep 1\n"
    "s_sub_u32 s77, s77, 1\n"
    "s_cmp_lg_u32 s77, 0\n"
    "s_cbranch_scc1 .Lspin4\n"
    ".Lspun4:\n"
    "v_pk_mul_f32 v[218:219], v[228:229], v[200:201] op_sel:[1,1] op_sel_hi:[1,0] neg_lo:[1,0]\n"
    "v_pk_mul_f32 v[220:221], v[232:233], v[192:193] op_sel:[1,1] op_sel_hi:[1,0] neg_lo:[1,0]\n"
    "v_pk_fma_f32 v[216:217], v[224:225], v[208:209], v[212:213] op_sel:[1,1,0] op_sel_hi:[1,0,1] neg_lo:[1,0,0]\n"
    "v_pk_fma_f32 v[80:81], v[228:229], v[200:201], v[218:219] op_sel_hi:[0,1,1]\n"
    "v_pk_fma_f32 v[82:83], v[232:233], v[192:193], v[220:221] op_sel_hi:[0,1,1]\n"
    "v_pk_fma_f32 v[78:79], v[224:225], v[208:209], v[216:217] op_sel_hi:[0,1,1]\n"
    "v_pk_fma_f32 v[218:219], v[230:231], v[196:197], v[80:81] op_sel:[1,1,0] op_sel_hi:[1,0,1] neg_lo:[1,0,0]\n"
    "v_pk_fma_f32 v[220:221], v[234:235], v[188:189], v[82:83] op_sel:[1,1,0] op_sel_hi:[1,0,1] neg_lo:[1,0,0]\n"
    "v_pk_fma_f32 v[216:217], v[226:227], v[204:205], v[78:79] op_sel:[1,1,0] op_sel_hi:[1,0,1] neg_lo:[1,0,0]\n"
    "v_pk_fma_f32 v[80:81], v[230:231], v[196:197], v[218:219] op_sel_hi:[0,1,1]\n"
    "v_pk_fma_f32 v[82:83], v[234:235], v[188:189], v[220:221] op_sel_hi:[0,1,1]\n"
    "v_pk_fma_f32 v[78:79], v[226:227], v[204:205], v[216:217] op_sel_hi:[0,1,1]\n"
    "v_pk_add_f32 v[80:81], v[80:81], v[82:83]\n"
    "v_pk_add_f32 v[78:79], v[78:79], v[80:81]\n"
    "ds_write_b64 v25, v[78:79]\n"
    "s_setprio 0\n"
    "s_branch .Lh_e2\n"
    "s_nop 0\n"
    ".Lh_e1:\n"
    "s_barrier\n"
    "s_sleep 9\n"
    "s_mov_b32 s82, 1\n"
    ".Lwsh:\n"
    "s_mul_i32 s60, s84, 82944\n"
    "s_add_u32 s60, s60, s83\n"
    "s_mov_b32 m0, s85\n"
    "s_nop 0\n"
    "buffer_load_dwordx4 v97, s[48:51], s60 offen offset:0 lds\n"
    "buffer_load_dwordx4 v98, s[48:51], s60 offen offset:1024 lds\n"
    "buffer_load_dwordx4 v99, s[48:51], s60 offen offset:2048 lds\n"
    "buffer_load_dwordx4 v100, s[48:51], s60 offen offset:3072 lds\n"
    "s_add_u32 m0, s85, 4096\n"
    "s_nop 0\n"
    "buffer_load_dwordx4 v101, s[48:51], s60 offen offset:0 lds\n"
    "s_add_u32 s84, s84, 2\n"
    "s_and_b32 s84, s84, 7\n"
    "s_add_u32 s85, s85, 20480\n"
    "s_sub_u32 s82, s82, 1\n"
    "s_cmp_lg_u32 s82, 0\n"
    "s_cbranch_scc1 .Lwsh\n"
    ".Lh_e2:\n"
    "s_waitcnt lgkmcnt(0)\n"
    "s_barrier\n"
    "s_barrier\n"
    ".Lh_sp:\n"
    "s_waitcnt vmcnt(0)\n"
    "s_barrier\n"
    "s_add_u32 s60, s56, 6\n"
    "s_sub_u32 s61, s60, 9\n"
    "s_cselect_b32 s60, s60, s61\n"
    "s_lshl_b32 s60, s60, 7\n"
    "buffer_load_dwordx4 v[188:191], v92, s[88:91], s60 offen offset:0\n"
    "buffer_load_dwordx4 v[192:195], v93, s[88:91], s60 offen offset:1024\n"
    "buffer_load_dwordx4 v[196:199], v94, s[88:91], s60 offen offset:2048\n"
    "buffer_load_dwordx4 v[200:203], v95, s[88:91], s60 offen offset:3072\n"
    "buffer_load_dwordx4 v[204:207], v96, s[88:91], s60 offen offset:0\n"
    "s_add_u32 s60, s56, 7\n"
    "s_sub_u32 s61, s60, 9\n"
    "s_cselect_b32 s60, s60, s61\n"
    "s_lshl_b32 s60, s60, 7\n"
    "buffer_load_dwordx4 v[208:211], v92, s[88:91], s60 offen offset:0\n"
    "buffer_load_dwordx4 v[212:215], v93, s[88:91], s60 offen offset:1024\n"
    "buffer_load_dwordx4 v[216:219], v94, s[88:91], s60 offen offset:2048\n"
    "buffer_load_dwordx4 v[220:223], v95, s[88:91], s60 offen offset:3072\n"
    "buffer_load_dwordx4 v[224:227], v96, s[88:91], s60 offen offset:0\n"
    "s_add_u32 s60, s56, 8\n"
    "s_sub_u32 s61, s60, 9\n"
    "s_cselect_b32 s60, s60, s61\n"
    "s_lshl_b32 s60, s60, 7\n"
    "buffer_load_dwordx4 v[228:231], v92, s[88:91], s60 offen offset:0\n"
    "buffer_load_dwordx4 v[232:235], v93, s[88:91], s60 offen offset:1024\n"
    "buffer_load_dwordx4 v[236:239], v94, s[88:91], s60 offen offset:2048\n"
    "buffer_load_dwordx4 v[240:243], v95, s[88:91], s60 offen offset:3072\n"
    "buffer_load_dwordx4 v[244:247], v96, s[88:91], s60 offen offset:0\n"
    "s_cmp_eq_u32 s54, 7\n"
    "s_cbranch_scc0 .Lwl_a\n"
    "s_mov_b32 s82, 3\n"
    ".Lwod:\n"
    "s_mul_i32 s60, s84, 82944\n"
    "s_add_u32 s60, s60, s83\n"
    "s_mov_b32 m0, s85\n"
    "s_nop 0\n"
    "buffer_load_dwordx4 v97, s[48:51], s60 offen offset:0 lds\n"
    "buffer_load_dwordx4 v98, s[48:51], s60 offen offset:1024 lds\n"
    "buffer_load_dwordx4 v99, s[48:51], s60 offen offset:2048 lds\n"
    "buffer_load_dwordx4 v100, s[48:51], s60 offen offset:3072 lds\n"
    "s_add_u32 m0, s85, 4096\n"
    "s_nop 0\n"
    "buffer_load_dwordx4 v101, s[48:51], s60 offen offset:0 lds\n"
    "s_add_u32 s84, s84, 2\n"
    "s_and_b32 s84, s84, 7\n"
    "s_add_u32 s85, s85, 20480\n"
    "s_sub_u32 s82, s82, 1\n"
    "s_cmp_lg_u32 s82, 0\n"
    "s_cbranch_scc1 .Lwod\n"
    "s_cmp_lt_u32 s35, 2\n"
    "s_cbranch_scc0 .Lwom\n"
    "s_mov_b32 m0, s85\n"
    "s_nop 0\n"
    "buffer_load_dwordx4 v102, s[48:51], s83 offen offset:0 lds\n"
    "buffer_load_dwordx4 v103, s[48:51], s83 offen offset:1024 lds\n"
    "buffer_load_dwordx4 v104, s[48:51], s83 offen offset:2048 lds\n"
    "buffer_load_dwordx4 v105, s[48:51], s83 offen offset:3072 lds\n"
    "s_add_u32 m0, s85, 4096\n"
    "s_nop 0\n"
    "buffer_load_dwordx4 v106, s[48:51], s83 offen offset:0 lds\n"
    ".Lwom:\n"
    ".Lwl_a:\n"
    "s_cmp_eq_u32 s35, 3\n"
    "s_cbranch_scc0 .Lh_nopoll5\n"
    "s_cmpk_eq_u32 s2, 255\n"
    "s_cbranch_scc0 .Lh_nog\n"
    "v_and_b32 v5, 31, v0\n"
    "v_lshlrev_b32_e64 v5, 10, v5\n"
    "s_mov_b32 s60, 90112\n"
    "s_mov_b32 s77, 262144\n"
    ".Lspin5:\n"
    "buffer_load_dwordx4 v[8:11], v5, s[36:39], s60 offen sc1\n"
    "s_waitcnt vmcnt(0)\n"
    "v_xor_b32 v9, s34, v9\n"
    "v_xor_b32 v11, s34, v11\n"
    "v_or_b32 v9, v9, v11\n"
    "v_cmp_ne_u32 vcc, 0, v9\n"
    "s_nop 0\n"
    "s_cbranch_vccz .Lspun5\n"
    "s_sleep 1\n"
    "s_sub_u32 s77, s77, 1\n"
    "s_cmp_lg_u32 s77, 0\n"
    "s_cbranch_scc1 .Lspin5\n"
    ".Lspun5:\n"
    ".Lh_nog:\n"
    "v_lshlrev_b32 v12, 7, v4\n"
    "v_lshlrev_b32 v6, 4, v0\n"
    "v_add_u32 v6, 154624, v6\n"
    "v_mov_b32 v8, 0\n"
    "v_mov_b32_e64 v9, 1\n"
    "ds_write_b64 v6, v[8:9]\n"
    "s_mov_b32 m0, 154624\n"
    "s_waitcnt lgkmcnt(0)\n"
    "v_min_u32 v5, 11, v0\n"
    "v_lshlrev_b32 v5, 7, v5\n"
    "s_lshl_b32 s76, s58, 11\n"
    "s_add_u32 s76, s76, 1900544\n"
    "s_mov_b32 s77, 262144\n"
    ".Lspin6:\n"
    "buffer_load_dwordx2 v[6:7], v5, s[36:39], s76 offen sc1\n"
    "s_waitcnt vmcnt(0)\n"
    "v_xor_b32 v6, s34, v6\n"
    "v_xor_b32 v7, s34, v7\n"
    "v_or_b32 v6, v6, v7\n"
    "v_cmp_ne_u32 vcc, 0, v6\n"
    "s_nop 0\n"
    "s_cbranch_vccz .Lspun6\n"
    "s_sleep 1\n"
    "s_sub_u32 s77, s77, 1\n"
    "s_cmp_lg_u32 s77, 0\n"
    "s_cbranch_scc1 .Lspin6\n"
    ".Lspun6:\n"
    "s_add_u32 s76, s76, 65536\n"
    ".Lh_nopoll5:\n"
    "v_lshlrev_b32 v5, 4, v0\n"
    "v_add_u32_e64 v5, s86, v5\n"
    "s_waitcnt vmcnt(0)\n"
    "s_barrier\n"
    "ds_write_b128 v5, v[108:111] offset:0\n"
    "ds_write_b128 v5, v[112:115] offset:1024\n"
    "ds_write_b128 v5, v[116:119] offset:2048\n"
    "ds_write_b128 v5, v[120:123] offset:3072\n"
    "ds_write_b128 v5, v[124:127] offset:4096\n"
    "ds_write_b128 v5, v[128:131] offset:20480\n"
    "ds_write_b128 v5, v[132:135] offset:21504\n"
    "ds_write_b128 v5, v[136:139] offset:22528\n"
    "ds_write_b128 v5, v[140:143] offset:23552\n"
    "ds_write_b128 v5, v[144:147] offset:24576\n"
    "ds_write_b128 v5, v[148:151] offset:40960\n"
    "ds_write_b128 v5, v[152:155] offset:41984\n"
    "ds_write_b128 v5, v[156:159] offset:43008\n"
    "ds_write_b128 v5, v[160:163] offset:44032\n"
    "ds_write_b128 v5, v[164:167] offset:45056\n"
    "s_waitcnt lgkmcnt(10)\n"
    "s_barrier ; relaxed\n"
    "s_waitcnt lgkmcnt(5)\n"
    "s_barrier ; relaxed\n"
    "ds_write_b128 v5, v[168:171] offset:0\n"
    "ds_write_b128 v5, v[172:175] offset:1024\n"
    "ds_write_b128 v5, v[176:179] offset:2048\n"
    "ds_write_b128 v5, v[180:183] offset:3072\n"
    "ds_write_b128 v5, v[184:187] offset:4096\n"
    "s_cmp_eq_u32 s54, 7\n"
    "s_cbranch_scc1 .Lwl_3\n"
    "s_mov_b32 s82, 1\n"
    ".Lwe3:\n"
    "s_mul_i32 s60, s84, 82944\n"
    "s_add_u32 s60, s60, s83\n"
    "s_mov_b32 m0, s85\n"
    "s_nop 0\n"
    "buffer_load_dwordx4 v97, s[48:51], s60 offen offset:0 lds\n"
    "buffer_load_dwordx4 v98, s[48:51], s60 offen offset:1024 lds\n"
    "buffer_load_dwordx4 v99, s[48:51], s60 offen offset:2048 lds\n"
    "buffer_load_dwordx4 v100, s[48:51], s60 offen offset:3072 lds\n"
    "s_add_u32 m0, s85, 4096\n"
    "s_nop 0\n"
    "buffer_load_dwordx4 v101, s[48:51], s60 offen offset:0 lds\n"
    "s_add_u32 s84, s84, 2\n"
    "s_and_b32 s84, s84, 7\n"
    "s_add_u32 s85, s85, 20480\n"
    "s_sub_u32 s82, s82, 1\n"
    "s_cmp_lg_u32 s82, 0\n"
    "s_cbranch_scc1 .Lwe3\n"
    "s_mov_b32 m0, 154624\n"
    ".Lwl_3:\n"
    "s_waitcnt lgkmcnt(5)\n"
    "s_barrier ; relaxed\n"
    "s_cmp_eq_u32 s35, 3\n"
    "s_cbranch_scc0 .Ldp54\n"
    "s_mov_b64 exec, 0xffff\n"
    "buffer_load_dwordx4 v12, s[36:39], s76 offen sc1 lds ; poll\n"
    "s_mov_b64 exec, -1\n"
    ".Ldp54:\n"
    "ds_write_b128 v5, v[188:191] offset:20480\n"
    "ds_write_b128 v5, v[192:195] offset:21504\n"
    "ds_write_b128 v5, v[196:199] offset:22528\n"
    "ds_write_b128 v5, v[200:203] offset:23552\n"
    "ds_write_b128 v5, v[204:207] offset:24576\n"
    "s_cmp_eq_u32 s54, 7\n"
    "s_cbranch_scc1 .Lwl_4\n"
    "s_mov_b32 s82, 1\n"
    ".Lwe4:\n"
    "s_mul_i32 s60, s84, 82944\n"
    "s_add_u32 s60, s60, s83\n"
    "s_mov_b32 m0, s85\n"
    "s_nop 0\n"
    "buffer_load_dwordx4 v97, s[48:51], s60 offen offset:0 lds\n"
    "buffer_load_dwordx4 v98, s[48:51], s60 offen offset:1024 lds\n"
    "buffer_load_dwordx4 v99, s[48:51], s60 offen offset:2048 lds\n"
    "buffer_load_dwordx4 v100, s[48:51], s60 offen offset:3072 lds\n"
    "s_add_u32 m0, s85, 4096\n"
    "s_nop 0\n"
    "buffer_load_dwordx4 v101, s[48:51], s60 offen offset:0 lds\n"
    "s_add_u32 s84, s84, 2\n"
    "s_and_b32 s84, s84, 7\n"
    "s_add_u32 s85, s85, 20480\n"
    "s_sub_u32 s82, s82, 1\n"
    "s_cmp_lg_u32 s82, 0\n"
    "s_cbranch_scc1 .Lwe4\n"
    "s_mov_b32 m0, 154624\n"
    ".Lwl_4:\n"
    "s_waitcnt lgkmcnt(5)\n"
    "s_barrier ; relaxed\n"
    "s_cmp_eq_u32 s35, 3\n"
    "s_cbranch_scc0 .Ldp55\n"
    "s_mov_b64 exec, 0xffff\n"
    "buffer_load_dwordx4 v12, s[36:39], s76 offen sc1 lds ; poll\n"
    "s_mov_b64 exec, -1\n"
    ".Ldp55:\n"
    "ds_write_b128 v5, v[208:211] offset:40960\n"
    "ds_write_b128 v5, v[212:215] offset:41984\n"
    "ds_write_b128 v5, v[216:219] offset:43008\n"
    "ds_write_b128 v5, v[220:223] offset:44032\n"
    "ds_write_b128 v5, v[224:227] offset:45056\n"
    "s_cmp_eq_u32 s54, 7\n"
    "s_cbranch_scc1 .Lwl_5\n"
    "s_mov_b32 m0, 154624\n"
    ".Lwl_5:\n"
    "s_waitcnt lgkmcnt(5)\n"
    "s_barrier ; relaxed\n"
    "s_cmp_eq_u32 s35, 3\n"
    "s_cbranch_scc0 .Ldp56\n"
    "s_mov_b64 exec, 0xffff\n"
    "buffer_load_dwordx4 v12, s[36:39], s76 offen sc1 lds ; poll\n"
    "s_mov_b64 exec, -1\n"
    ".Ldp56:\n"
    "ds_write_b128 v5, v[228:231] offset:0\n"
    "ds_write_b128 v5, v[232:235] offset:1024\n"
    "ds_write_b128 v5, v[236:239] offset:2048\n"
    "ds_write_b128 v5, v[240:243] offset:3072\n"
    "ds_write_b128 v5, v[244:247] offset:4096\n"
    "s_cmp_eq_u32 s54, 7\n"
    "s_cbranch_scc1 .Lwl_6\n"
    "s_cmp_lt_u32 s35, 2\n"
    "s_cbranch_scc0 .Lwf\n"
    "s_mov_b32 m0, s85\n"
    "s_nop 0\n"
    "buffer_load_dwordx4 v102, s[48:51], s83 offen offset:0 lds\n"
    "buffer_load_dwordx4 v103, s[48:51], s83 offen offset:1024 lds\n"
    "buffer_load_dwordx4 v104, s[48:51], s83 offen offset:2048 lds\n"
    "buffer_load_dwordx4 v105, s[48:51], s83 offen offset:3072 lds\n"
    "s_add_u32 m0, s85, 4096\n"
    "s_nop 0\n"
    "buffer_load_dwordx4 v106, s[48:51], s83 offen offset:0 lds\n"
    ".Lwf:\n"
    "s_mov_b32 m0, 154624\n"
    ".Lwl_6:\n"
    "s_waitcnt lgkmcnt(5)\n"
    "s_barrier ; relaxed\n"
    "s_waitcnt lgkmcnt(0)\n"
    "s_barrier\n"
    "s_cmpk_eq_u32 s2, 255\n"
    "s_cbranch_scc0 .Lh_noe\n"
    "s_cmp_eq_u32 s35, 3\n"
    "s_cbranch_scc0 .Lh_noe\n"
    "v_mov_b32 v6, s34\n"
    "s_mov_b64 exec, 1\n"
    "buffer_store_dword v6, off, s[36:39], 0 sc1\n"
    "s_mov_b64 exec, -1\n"
    ".Lh_noe:\n"
    "s_waitcnt vmcnt(0)\n"
    "s_endpgm\n"
    "s_nop 0\n"
    ".Lspare:\n"
    "s_waitcnt lgkmcnt(0)\n"
    "s_mov_b32 s36, s12\n"
    "s_and_b32 s37, s13, 0xffff\n"
    "s_mov_b32 s38, 9175040\n"
    "s_mov_b32 s39, 0x20000\n"
    "buffer_load_dword v253, off, s[36:39], 0 sc1\n"
    "s_cmp_eq_u32 s3, 1\n"
    "s_cbranch_scc0 .Lnofw\n"
    "v_and_b32 v5, 1, v3\n"
    "v_lshlrev_b32 v5, 16, v5\n"
    "v_lshl_add_u32 v5, v4, 7, v5\n"
    "s_lshl_b32 s60, s58, 11\n"
    "s_add_u32 s60, s60, 1900544\n"
    "buffer_load_dword v246, v5, s[36:39], s60 offen sc1\n"
    ".Lnofw:\n"
    "s_lshr_b32 s65, s3, 2\n"
    "s_add_u32 s65, s65, 6\n"
    "s_cmp_lt_u32 s65, 9\n"
    "s_cbranch_scc0 .Lls0\n"
    "s_lshl2_add_u32 s62, s53, s35\n"
    "s_and_b32 s64, s52, 1\n"
    "s_lshl4_add_u32 s64, s64, s62\n"
    "s_mul_i32 s64, s64, 73728\n"
    "s_lshl_b32 s66, s65, 7\n"
    "s_add_u32 s64, s64, s66\n"
    "v_mul_u32_u24 v100, 1152, v0\n"
    "v_add_u32 v100, s64, v100\n"
    "v_min_u32 v101, 35, v0\n"
    "v_mul_u32_u24 v102, 86, v101\n"
    "v_lshrrev_b32 v102, 8, v102\n"
    "v_mad_u32_u24 v101, v102, 6, v101\n"
    "s_lshl4_add_u32 s65, s65, s62\n"
    "s_mul_i32 s65, s65, 13824\n"
    "s_lshr_b32 s66, s52, 1\n"
    "s_lshl_b32 s66, s66, 8\n"
    "s_add_u32 s65, s65, s66\n"
    "v_lshl_add_u32 v101, v101, 7, s65\n"
    "global_load_dword v110, v100, s[4:5]\n"
    "global_load_dword v111, v101, s[6:7]\n"
    ".Lls0:\n"
    "s_barrier\n"
    "s_lshr_b32 s65, s3, 2\n"
    "s_add_u32 s65, s65, 8\n"
    "s_cmp_lt_u32 s65, 9\n"
    "s_cbranch_scc0 .Lls1\n"
    "s_lshl2_add_u32 s62, s53, s35\n"
    "s_and_b32 s64, s52, 1\n"
    "s_lshl4_add_u32 s64, s64, s62\n"
    "s_mul_i32 s64, s64, 73728\n"
    "s_lshl_b32 s66, s65, 7\n"
    "s_add_u32 s64, s64, s66\n"
    "v_mul_u32_u24 v100, 1152, v0\n"
    "v_add_u32 v100, s64, v100\n"
    "v_min_u32 v101, 35, v0\n"
    "v_mul_u32_u24 v102, 86, v101\n"
    "v_lshrrev_b32 v102, 8, v102\n"
    "v_mad_u32_u24 v101, v102, 6, v101\n"
    "s_lshl4_add_u32 s65, s65, s62\n"
    "s_mul_i32 s65, s65, 13824\n"
    "s_lshr_b32 s66, s52, 1\n"
    "s_lshl_b32 s66, s66, 8\n"
    "s_add_u32 s65, s65, s66\n"
    "v_lshl_add_u32 v101, v101, 7, s65\n"
    "global_load_dword v112, v100, s[4:5]\n"
    "global_load_dword v113, v101, s[6:7]\n"
    ".Lls1:\n"
    "s_barrier\n"
    "s_cmp_lt_u32 s3, 3\n"
    "s_cbranch_scc0 .Lwu3\n"
    "s_mul_i32 s64, s58, 162\n"
    "s_mul_i32 s65, s3, 54\n"
    "s_add_u32 s64, s64, s65\n"
    "v_min_u32 v100, 53, v0\n"
    "v_add_lshl_u32 v100, v100, s64, 7\n"
    "global_load_dword v120, v100, s[22:23]\n"
    "global_load_dword v121, v100, s[24:25]\n"
    "global_load_dword v122, v100, s[26:27]\n"
    ".Lwu3:\n"
    "s_barrier\n"
    "s_waitcnt vmcnt(0)\n"
    "v_readfirstlane_b32 s34, v253\n"
    "s_nop 0\n"
    "s_add_u32 s34, s34, 0x9E3779B9\n"
    "s_cmp_lt_u32 s3, 4\n"
    "s_cbranch_scc1 .Lsetup4\n"
    "s_mov_b32 s40, s28\n"
    "s_and_b32 s41, s29, 0xffff\n"
    "s_mov_b32 s42, 1152\n"
    "s_mov_b32 s43, 0x20000\n"
    "s_mul_i32 s63, s35, 1152\n"
    "s_add_u32 s63, s63, 156160\n"
    "v_lshlrev_b32 v5, 4, v0\n"
    "s_mov_b32 m0, s63\n"
    "s_nop 0\n"
    "buffer_load_dwordx4 v5, s[40:43], 0 offen lds\n"
    "s_mov_b64 exec, 0xff\n"
    "buffer_load_dwordx4 v5, s[40:43], 0 offen offset:1024 lds\n"
    "s_mov_b64 exec, -1\n"
    "s_and_b32 s61, s52, 1\n"
    "s_lshr_b32 s60, s52, 1\n"
    "s_lshl2_add_u32 s61, s61, s60\n"
    "s_lshl2_add_u32 s61, s61, s53\n"
    "s_lshl_b32 s61, s61, 6\n"
    "s_lshl1_add_u32 s60, s35, s61\n"
    "v_lshrrev_b32 v5, 2, v1\n"
    "v_lshlrev_b32_e64 v5, 1, v5\n"
    "v_and_or_b32 v5, v1, 1, v5\n"
    "v_bfe_u32 v6, v1, 1, 1\n"
    "v_lshl_add_u32 v5, v5, 3, v6\n"
    "v_add_u32 v5, s60, v5\n"
    "v_mul_u32_u24 v5, 1152, v5\n"
    "v_and_b32 v6, 7, v0\n"
    "v_lshlrev_b32 v6, 4, v6\n"
    "v_add_u32 v5, v5, v6\n"
    "v_add_u32 v7, 36864, v5\n"
    "v_add_u32_e64 v6, s63, v6\n"
    "global_load_dwordx4 v[24:27], v5, s[4:5] offset:0\n"
    "global_load_dwordx4 v[28:31], v5, s[4:5] offset:128\n"
    "global_load_dwordx4 v[32:35], v5, s[4:5] offset:256\n"
    "global_load_dwordx4 v[36:39], v5, s[4:5] offset:384\n"
    "global_load_dwordx4 v[40:43], v5, s[4:5] offset:512\n"
    "global_load_dwordx4 v[44:47], v5, s[4:5] offset:640\n"
    "global_load_dwordx4 v[48:51], v5, s[4:5] offset:768\n"
    "global_load_dwordx4 v[52:55], v5, s[4:5] offset:896\n"
    "global_load_dwordx4 v[56:59], v5, s[4:5] offset:1024\n"
    "global_load_dwordx4 v[60:63], v7, s[4:5] offset:0\n"
    "global_load_dwordx4 v[64:67], v7, s[4:5] offset:128\n"
    "global_load_dwordx4 v[68:71], v7, s[4:5] offset:256\n"
    "global_load_dwordx4 v[72:75], v7, s[4:5] offset:384\n"
    "global_load_dwordx4 v[76:79], v7, s[4:5] offset:512\n"
    "global_load_dwordx4 v[80:83], v7, s[4:5] offset:640\n"
    "global_load_dwordx4 v[84:87], v7, s[4:5] offset:768\n"
    "global_load_dwordx4 v[88:91], v7, s[4:5] offset:896\n"
    "global_load_dwordx4 v[92:95], v7, s[4:5] offset:1024\n"
    "s_nop 0\n"
    "s_waitcnt vmcnt(18)\n"
    "ds_read_b128 v[96:99], v6 offset:0\n"
    "ds_read_b128 v[100:103], v6 offset:128\n"
    "ds_read_b128 v[104:107], v6 offset:256\n"
    "ds_read_b128 v[108:111], v6 offset:384\n"
    "ds_read_b128 v[112:115], v6 offset:512\n"
    "ds_read_b128 v[116:119], v6 offset:640\n"
    "ds_read_b128 v[120:123], v6 offset:768\n"
    "ds_read_b128 v[124:127], v6 offset:896\n"
    "ds_read_b128 v[128:131], v6 offset:1024\n"
    "s_waitcnt lgkmcnt(8)\n"
    "v_lshlrev_b32 v140, 16, v96\n"
    "v_and_b32 v141, 0xffff0000, v96\n"
    "v_lshlrev_b32 v142, 16, v97\n"
    "v_and_b32 v143, 0xffff0000, v97\n"
    "v_lshlrev_b32 v144, 16, v98\n"
    "v_and_b32 v145, 0xffff0000, v98\n"
    "v_lshlrev_b32 v146, 16, v99\n"
    "v_and_b32 v147, 0xffff0000, v99\n"
    "s_waitcnt lgkmcnt(7)\n"
    "v_lshlrev_b32 v148, 16, v100\n"
    "v_and_b32 v149, 0xffff0000, v100\n"
    "v_lshlrev_b32 v150, 16, v101\n"
    "v_and_b32 v151, 0xffff0000, v101\n"
    "v_lshlrev_b32 v152, 16, v102\n"
    "v_and_b32 v153, 0xffff0000, v102\n"
    "v_lshlrev_b32 v154, 16, v103\n"
    "v_and_b32 v155, 0xffff0000, v103\n"
    "s_waitcnt lgkmcnt(6)\n"
    "v_lshlrev_b32 v156, 16, v104\n"
    "v_and_b32 v157, 0xffff0000, v104\n"
    "v_lshlrev_b32 v158, 16, v105\n"
    "v_and_b32 v159, 0xffff0000, v105\n"
    "v_lshlrev_b32 v160, 16, v106\n"
    "v_and_b32 v161, 0xffff0000, v106\n"
    "v_lshlrev_b32 v162, 16, v107\n"
    "v_and_b32 v163, 0xffff0000, v107\n"
    "s_waitcnt lgkmcnt(5)\n"
    "v_lshlrev_b32 v164, 16, v108\n"
    "v_and_b32 v165, 0xffff0000, v108\n"
    "v_lshlrev_b32 v166, 16, v109\n"
    "v_and_b32 v167, 0xffff0000, v109\n"
    "v_lshlrev_b32 v168, 16, v110\n"
    "v_and_b32 v169, 0xffff0000, v110\n"
    "v_lshlrev_b32 v170, 16, v111\n"
    "v_and_b32 v171, 0xffff0000, v111\n"
    "s_waitcnt lgkmcnt(4)\n"
    "v_lshlrev_b32 v172, 16, v112\n"
    "v_and_b32 v173, 0xffff0000, v112\n"
    "v_lshlrev_b32 v174, 16, v113\n"
    "v_and_b32 v175, 0xffff0000, v113\n"
    "v_lshlrev_b32 v176, 16, v114\n"
    "v_and_b32 v177, 0xffff0000, v114\n"
    "v_lshlrev_b32 v178, 16, v115\n"
    "v_and_b32 v179, 0xffff0000, v115\n"
    "s_waitcnt lgkmcnt(3)\n"
    "v_lshlrev_b32 v180, 16, v116\n"
    "v_and_b32 v181, 0xffff0000, v116\n"
    "v_lshlrev_b32 v182, 16, v117\n"
    "v_and_b32 v183, 0xffff0000, v117\n"
    "v_lshlrev_b32 v184, 16, v118\n"
    "v_and_b32 v185, 0xffff0000, v118\n"
    "v_lshlrev_b32 v186, 16, v119\n"
    "v_and_b32 v187, 0xffff0000, v119\n"
    "s_waitcnt lgkmcnt(2)\n"
    "v_lshlrev_b32 v188, 16, v120\n"
    "v_and_b32 v189, 0xffff0000, v120\n"
    "v_lshlrev_b32 v190, 16, v121\n"
    "v_and_b32 v191, 0xffff0000, v121\n"
    "v_lshlrev_b32 v192, 16, v122\n"
    "v_and_b32 v193, 0xffff0000, v122\n"
    "v_lshlrev_b32 v194, 16, v123\n"
    "v_and_b32 v195, 0xffff0000, v123\n"
    "s_waitcnt lgkmcnt(1)\n"
    "v_lshlrev_b32 v196, 16, v124\n"
    "v_and_b32 v197, 0xffff0000, v124\n"
    "v_lshlrev_b32 v198, 16, v125\n"
    "v_and_b32 v199, 0xffff0000, v125\n"
    "v_lshlrev_b32 v200, 16, v126\n"
    "v_and_b32 v201, 0xffff0000, v126\n"
    "v_lshlrev_b32 v202, 16, v127\n"
    "v_and_b32 v203, 0xffff0000, v127\n"
    "s_waitcnt lgkmcnt(0)\n"
    "v_lshlrev_b32 v204, 16, v128\n"
    "v_and_b32 v205, 0xffff0000, v128\n"
    "v_lshlrev_b32 v206, 16, v129\n"
    "v_and_b32 v207, 0xffff0000, v129\n"
    "v_lshlrev_b32 v208, 16, v130\n"
    "v_and_b32 v209, 0xffff0000, v130\n"
    "v_lshlrev_b32 v210, 16, v131\n"
    "v_and_b32 v211, 0xffff0000, v131\n"
    "v_mov_b32 v8, 0\n"
    "v_mov_b32 v9, 0\n"
    "s_waitcnt vmcnt(17)\n"
    "v_lshlrev_b32_e64 v16, 16, v24\n"
    "v_and_b32 v17, 0xffff0000, v24\n"
    "v_pk_fma_f32 v[8:9], v[16:17], v[140:141], v[8:9]\n"
    "v_lshlrev_b32_e64 v18, 16, v25\n"
    "v_and_b32 v19, 0xffff0000, v25\n"
    "v_pk_fma_f32 v[8:9], v[18:19], v[142:143], v[8:9]\n"
    "v_lshlrev_b32_e64 v16, 16, v26\n"
    "v_and_b32 v17, 0xffff0000, v26\n"
    "v_pk_fma_f32 v[8:9], v[16:17], v[144:145], v[8:9]\n"
    "v_lshlrev_b32_e64 v18, 16, v27\n"
    "v_and_b32 v19, 0xffff0000, v27\n"
    "v_pk_fma_f32 v[8:9], v[18:19], v[146:147], v[8:9]\n"
    "s_waitcnt vmcnt(16)\n"
    "v_lshlrev_b32 v16, 16, v28\n"
    "v_and_b32 v17, 0xffff0000, v28\n"
    "v_pk_fma_f32 v[8:9], v[16:17], v[148:149], v[8:9]\n"
    "v_lshlrev_b32_e64 v18, 16, v29\n"
    "v_and_b32 v19, 0xffff0000, v29\n"
    "v_pk_fma_f32 v[8:9], v[18:19], v[150:151], v[8:9]\n"
    "v_lshlrev_b32_e64 v16, 16, v30\n"
    "v_and_b32 v17, 0xffff0000, v30\n"
    "v_pk_fma_f32 v[8:9], v[16:17], v[152:153], v[8:9]\n"
    "v_lshlrev_b32_e64 v18, 16, v31\n"
    "v_and_b32 v19, 0xffff0000, v31\n"
    "v_pk_fma_f32 v[8:9], v[18:19], v[154:155], v[8:9]\n"
    "s_waitcnt vmcnt(15)\n"
    "v_lshlrev_b32 v16, 16, v32\n"
    "v_and_b32 v17, 0xffff0000, v32\n"
    "v_pk_fma_f32 v[8:9], v[16:17], v[156:157], v[8:9]\n"
    "v_lshlrev_b32_e64 v18, 16, v33\n"
    "v_and_b32 v19, 0xffff0000, v33\n"
    "v_pk_fma_f32 v[8:9], v[18:19], v[158:159], v[8:9]\n"
    "v_lshlrev_b32_e64 v16, 16, v34\n"
    "v_and_b32 v17, 0xffff0000, v34\n"
    "v_pk_fma_f32 v[8:9], v[16:17], v[160:161], v[8:9]\n"
    "v_lshlrev_b32_e64 v18, 16, v35\n"
    "v_and_b32 v19, 0xffff0000, v35\n"
    "v_pk_fma_f32 v[8:9], v[18:19], v[162:163], v[8:9]\n"
    "s_waitcnt vmcnt(14)\n"
    "v_lshlrev_b32 v16, 16, v36\n"
    "v_and_b32 v17, 0xffff0000, v36\n"
    "v_pk_fma_f32 v[8:9], v[16:17], v[164:165], v[8:9]\n"
    "v_lshlrev_b32_e64 v18, 16, v37\n"
    "v_and_b32 v19, 0xffff0000, v37\n"
    "v_pk_fma_f32 v[8:9], v[18:19], v[166:167], v[8:9]\n"
    "v_lshlrev_b32_e64 v16, 16, v38\n"
    "v_and_b32 v17, 0xffff0000, v38\n"
    "v_pk_fma_f32 v[8:9], v[16:17], v[168:169], v[8:9]\n"
    "v_lshlrev_b32_e64 v18, 16, v39\n"
    "v_and_b32 v19, 0xffff0000, v39\n"
    "v_pk_fma_f32 v[8:9], v[18:19], v[170:171], v[8:9]\n"
    "s_waitcnt vmcnt(13)\n"
    "v_lshlrev_b32 v16, 16, v40\n"
    "v_and_b32 v17, 0xffff0000, v40\n"
    "v_pk_fma_f32 v[8:9], v[16:17], v[172:173], v[8:9]\n"
    "v_lshlrev_b32_e64 v18, 16, v41\n"
    "v_and_b32 v19, 0xffff0000, v41\n"
    "v_pk_fma_f32 v[8:9], v[18:19], v[174:175], v[8:9]\n"
    "v_lshlrev_b32_e64 v16, 16, v42\n"
    "v_and_b32 v17, 0xffff0000, v42\n"
    "v_pk_fma_f32 v[8:9], v[16:17], v[176:177], v[8:9]\n"
    "v_lshlrev_b32_e64 v18, 16, v43\n"
    "v_and_b32 v19, 0xffff0000, v43\n"
    "v_pk_fma_f32 v[8:9], v[18:19], v[178:179], v[8:9]\n"
    "s_waitcnt vmcnt(12)\n"
    "v_lshlrev_b32 v16, 16, v44\n"
    "v_and_b32 v17, 0xffff0000, v44\n"
    "v_pk_fma_f32 v[8:9], v[16:17], v[180:181], v[8:9]\n"
    "v_lshlrev_b32_e64 v18, 16, v45\n"
    "v_and_b32 v19, 0xffff0000, v45\n"
    "v_pk_fma_f32 v[8:9], v[18:19], v[182:183], v[8:9]\n"
    "v_lshlrev_b32_e64 v16, 16, v46\n"
    "v_and_b32 v17, 0xffff0000, v46\n"
    "v_pk_fma_f32 v[8:9], v[16:17], v[184:185], v[8:9]\n"
    "v_lshlrev_b32_e64 v18, 16, v47\n"
    "v_and_b32 v19, 0xffff0000, v47\n"
    "v_pk_fma_f32 v[8:9], v[18:19], v[186:187], v[8:9]\n"
    "s_waitcnt vmcnt(11)\n"
    "v_lshlrev_b32 v16, 16, v48\n"
    "v_and_b32 v17, 0xffff0000, v48\n"
    "v_pk_fma_f32 v[8:9], v[16:17], v[188:189], v[8:9]\n"
    "v_lshlrev_b32_e64 v18, 16, v49\n"
    "v_and_b32 v19, 0xffff0000, v49\n"
    "v_pk_fma_f32 v[8:9], v[18:19], v[190:191], v[8:9]\n"
    "v_lshlrev_b32_e64 v16, 16, v50\n"
    "v_and_b32 v17, 0xffff0000, v50\n"
    "v_pk_fma_f32 v[8:9], v[16:17], v[192:193], v[8:9]\n"
    "v_lshlrev_b32_e64 v18, 16, v51\n"
    "v_and_b32 v19, 0xffff0000, v51\n"
    "v_pk_fma_f32 v[8:9], v[18:19], v[194:195], v[8:9]\n"
    "s_waitcnt vmcnt(10)\n"
    "v_lshlrev_b32 v16, 16, v52\n"
    "v_and_b32 v17, 0xffff0000, v52\n"
    "v_pk_fma_f32 v[8:9], v[16:17], v[196:197], v[8:9]\n"
    "v_lshlrev_b32_e64 v18, 16, v53\n"
    "v_and_b32 v19, 0xffff0000, v53\n"
    "v_pk_fma_f32 v[8:9], v[18:19], v[198:199], v[8:9]\n"
    "v_lshlrev_b32_e64 v16, 16, v54\n"
    "v_and_b32 v17, 0xffff0000, v54\n"
    "v_pk_fma_f32 v[8:9], v[16:17], v[200:201], v[8:9]\n"
    "v_lshlrev_b32_e64 v18, 16, v55\n"
    "v_and_b32 v19, 0xffff0000, v55\n"
    "v_pk_fma_f32 v[8:9], v[18:19], v[202:203], v[8:9]\n"
    "s_waitcnt vmcnt(9)\n"
    "v_lshlrev_b32 v16, 16, v56\n"
    "v_and_b32 v17, 0xffff0000, v56\n"
    "v_pk_fma_f32 v[8:9], v[16:17], v[204:205], v[8:9]\n"
    "v_lshlrev_b32_e64 v18, 16, v57\n"
    "v_and_b32 v19, 0xffff0000, v57\n"
    "v_pk_fma_f32 v[8:9], v[18:19], v[206:207], v[8:9]\n"
    "v_lshlrev_b32_e64 v16, 16, v58\n"
    "v_and_b32 v17, 0xffff0000, v58\n"
    "v_pk_fma_f32 v[8:9], v[16:17], v[208:209], v[8:9]\n"
    "v_lshlrev_b32_e64 v18, 16, v59\n"
    "v_and_b32 v19, 0xffff0000, v59\n"
    "v_pk_fma_f32 v[8:9], v[18:19], v[210:211], v[8:9]\n"
    "v_add_f32 v8, v8, v9\n"
    "s_nop 1\n"
    "v_add_f32_dpp v8, v8, v8 quad_perm:[1,0,3,2] row_mask:0xf bank_mask:0xf\n"
    "s_nop 1\n"
    "v_add_f32_dpp v8, v8, v8 quad_perm:[2,3,0,1] row_mask:0xf bank_mask:0xf\n"
    "s_nop 1\n"
    "v_add_f32_dpp v8, v8, v8 row_half_mirror row_mask:0xf bank_mask:0xf\n"
    "v_mul_f32 v8, 0xbfb8aa3b, v8\n"
    "v_exp_f32 v8, v8\n"
    "s_nop 0\n"
    "v_add_f32 v8, 1.0, v8\n"
    "v_rcp_f32 v8, v8\n"
    "v_mov_b32 v9, s34\n"
    "v_mov_b32 v11, s34\n"
    "v_mov_b32 v10, v8\n"
    "v_mov_b32 v12, 0\n"
    "v_mov_b32 v13, 0\n"
    "s_waitcnt vmcnt(8)\n"
    "v_lshlrev_b32_e64 v16, 16, v60\n"
    "v_and_b32 v17, 0xffff0000, v60\n"
    "v_pk_fma_f32 v[12:13], v[16:17], v[140:141], v[12:13]\n"
    "v_lshlrev_b32_e64 v18, 16, v61\n"
    "v_and_b32 v19, 0xffff0000, v61\n"
    "v_pk_fma_f32 v[12:13], v[18:19], v[142:143], v[12:13]\n"
    "v_lshlrev_b32_e64 v16, 16, v62\n"
    "v_and_b32 v17, 0xffff0000, v62\n"
    "v_pk_fma_f32 v[12:13], v[16:17], v[144:145], v[12:13]\n"
    "v_lshlrev_b32_e64 v18, 16, v63\n"
    "v_and_b32 v19, 0xffff0000, v63\n"
    "v_pk_fma_f32 v[12:13], v[18:19], v[146:147], v[12:13]\n"
    "s_waitcnt vmcnt(7)\n"
    "v_lshlrev_b32 v16, 16, v64\n"
    "v_and_b32 v17, 0xffff0000, v64\n"
    "v_pk_fma_f32 v[12:13], v[16:17], v[148:149], v[12:13]\n"
    "v_lshlrev_b32_e64 v18, 16, v65\n"
    "v_and_b32 v19, 0xffff0000, v65\n"
    "v_pk_fma_f32 v[12:13], v[18:19], v[150:151], v[12:13]\n"
    "v_lshlrev_b32_e64 v16, 16, v66\n"
    "v_and_b32 v17, 0xffff0000, v66\n"
    "v_pk_fma_f32 v[12:13], v[16:17], v[152:153], v[12:13]\n"
    "v_lshlrev_b32_e64 v18, 16, v67\n"
    "v_and_b32 v19, 0xffff0000, v67\n"
    "v_pk_fma_f32 v[12:13], v[18:19], v[154:155], v[12:13]\n"
    "s_waitcnt vmcnt(6)\n"
    "v_lshlrev_b32 v16, 16, v68\n"
    "v_and_b32 v17, 0xffff0000, v68\n"
    "v_pk_fma_f32 v[12:13], v[16:17], v[156:157], v[12:13]\n"
    "v_lshlrev_b32_e64 v18, 16, v69\n"
    "v_and_b32 v19, 0xffff0000, v69\n"
    "v_pk_fma_f32 v[12:13], v[18:19], v[158:159], v[12:13]\n"
    "v_lshlrev_b32_e64 v16, 16, v70\n"
    "v_and_b32 v17, 0xffff0000, v70\n"
    "v_pk_fma_f32 v[12:13], v[16:17], v[160:161], v[12:13]\n"
    "v_lshlrev_b32_e64 v18, 16, v71\n"
    "v_and_b32 v19, 0xffff0000, v71\n"
    "v_pk_fma_f32 v[12:13], v[18:19], v[162:163], v[12:13]\n"
    "s_waitcnt vmcnt(5)\n"
    "v_lshlrev_b32 v16, 16, v72\n"
    "v_and_b32 v17, 0xffff0000, v72\n"
    "v_pk_fma_f32 v[12:13], v[16:17], v[164:165], v[12:13]\n"
    "v_lshlrev_b32_e64 v18, 16, v73\n"
    "v_and_b32 v19, 0xffff0000, v73\n"
    "v_pk_fma_f32 v[12:13], v[18:19], v[166:167], v[12:13]\n"
    "v_lshlrev_b32_e64 v16, 16, v74\n"
    "v_and_b32 v17, 0xffff0000, v74\n"
    "v_pk_fma_f32 v[12:13], v[16:17], v[168:169], v[12:13]\n"
    "v_lshlrev_b32_e64 v18, 16, v75\n"
    "v_and_b32 v19, 0xffff0000, v75\n"
    "v_pk_fma_f32 v[12:13], v[18:19], v[170:171], v[12:13]\n"
    "s_waitcnt vmcnt(4)\n"
    "v_lshlrev_b32 v16, 16, v76\n"
    "v_and_b32 v17, 0xffff0000, v76\n"
    "v_pk_fma_f32 v[12:13], v[16:17], v[172:173], v[12:13]\n"
    "v_lshlrev_b32_e64 v18, 16, v77\n"
    "v_and_b32 v19, 0xffff0000, v77\n"
    "v_pk_fma_f32 v[12:13], v[18:19], v[174:175], v[12:13]\n"
    "v_lshlrev_b32_e64 v16, 16, v78\n"
    "v_and_b32 v17, 0xffff0000, v78\n"
    "v_pk_fma_f32 v[12:13], v[16:17], v[176:177], v[12:13]\n"
    "v_lshlrev_b32_e64 v18, 16, v79\n"
    "v_and_b32 v19, 0xffff0000, v79\n"
    "v_pk_fma_f32 v[12:13], v[18:19], v[178:179], v[12:13]\n"
    "s_waitcnt vmcnt(3)\n"
    "v_lshlrev_b32 v16, 16, v80\n"
    "v_and_b32 v17, 0xffff0000, v80\n"
    "v_pk_fma_f32 v[12:13], v[16:17], v[180:181], v[12:13]\n"
    "v_lshlrev_b32_e64 v18, 16, v81\n"
    "v_and_b32 v19, 0xffff0000, v81\n"
    "v_pk_fma_f32 v[12:13], v[18:19], v[182:183], v[12:13]\n"
    "v_lshlrev_b32_e64 v16, 16, v82\n"
    "v_and_b32 v17, 0xffff0000, v82\n"
    "v_pk_fma_f32 v[12:13], v[16:17], v[184:185], v[12:13]\n"
    "v_lshlrev_b32_e64 v18, 16, v83\n"
    "v_and_b32 v19, 0xffff0000, v83\n"
    "v_pk_fma_f32 v[12:13], v[18:19], v[186:187], v[12:13]\n"
    "s_waitcnt vmcnt(2)\n"
    "v_lshlrev_b32 v16, 16, v84\n"
    "v_and_b32 v17, 0xffff0000, v84\n"
    "v_pk_fma_f32 v[12:13], v[16:17], v[188:189], v[12:13]\n"
    "v_lshlrev_b32_e64 v18, 16, v85\n"
    "v_and_b32 v19, 0xffff0000, v85\n"
    "v_pk_fma_f32 v[12:13], v[18:19], v[190:191], v[12:13]\n"
    "v_lshlrev_b32_e64 v16, 16, v86\n"
    "v_and_b32 v17, 0xffff0000, v86\n"
    "v_pk_fma_f32 v[12:13], v[16:17], v[192:193], v[12:13]\n"
    "v_lshlrev_b32_e64 v18, 16, v87\n"
    "v_and_b32 v19, 0xffff0000, v87\n"
    "v_pk_fma_f32 v[12:13], v[18:19], v[194:195], v[12:13]\n"
    "s_waitcnt vmcnt(1)\n"
    "v_lshlrev_b32 v16, 16, v88\n"
    "v_and_b32 v17, 0xffff0000, v88\n"
    "v_pk_fma_f32 v[12:13], v[16:17], v[196:197], v[12:13]\n"
    "v_lshlrev_b32_e64 v18, 16, v89\n"
    "v_and_b32 v19, 0xffff0000, v89\n"
    "v_pk_fma_f32 v[12:13], v[18:19], v[198:199], v[12:13]\n"
    "v_lshlrev_b32_e64 v16, 16, v90\n"
    "v_and_b32 v17, 0xffff0000, v90\n"
    "v_pk_fma_f32 v[12:13], v[16:17], v[200:201], v[12:13]\n"
    "v_lshlrev_b32_e64 v18, 16, v91\n"
    "v_and_b32 v19, 0xffff0000, v91\n"
    "v_pk_fma_f32 v[12:13], v[18:19], v[202:203], v[12:13]\n"
    "s_waitcnt vmcnt(0)\n"
    "v_lshlrev_b32 v16, 16, v92\n"
    "v_and_b32 v17, 0xffff0000, v92\n"
    "v_pk_fma_f32 v[12:13], v[16:17], v[204:205], v[12:13]\n"
    "v_lshlrev_b32_e64 v18, 16, v93\n"
    "v_and_b32 v19, 0xffff0000, v93\n"
    "v_pk_fma_f32 v[12:13], v[18:19], v[206:207], v[12:13]\n"
    "v_lshlrev_b32_e64 v16, 16, v94\n"
    "v_and_b32 v17, 0xffff0000, v94\n"
    "v_pk_fma_f32 v[12:13], v[16:17], v[208:209], v[12:13]\n"
    "v_lshlrev_b32_e64 v18, 16, v95\n"
    "v_and_b32 v19, 0xffff0000, v95\n"
    "v_pk_fma_f32 v[12:13], v[18:19], v[210:211], v[12:13]\n"
    "v_add_f32 v12, v12, v13\n"
    "s_nop 1\n"
    "v_add_f32_dpp v12, v12, v12 quad_perm:[1,0,3,2] row_mask:0xf bank_mask:0xf\n"
    "s_nop 1\n"
    "v_add_f32_dpp v12, v12, v12 quad_perm:[2,3,0,1] row_mask:0xf bank_mask:0xf\n"
    "s_nop 1\n"
    "v_add_f32_dpp v12, v12, v12 row_half_mirror row_mask:0xf bank_mask:0xf\n"
    "v_mul_f32 v12, 0xbfb8aa3b, v12\n"
    "v_exp_f32 v12, v12\n"
    "s_nop 0\n"
    "v_add_f32 v12, 1.0, v12\n"
    "v_rcp_f32 v12, v12\n"
    "v_mov_b32 v13, s34\n"
    "v_mov_b32 v15, s34\n"
    "v_mov_b32 v14, v12\n"
    "s_lshl4_add_u32 s60, s35, s61\n"
    "s_lshl_b32 s60, s60, 4\n"
    "s_add_u32 s60, s60, 90112\n"
    "v_lshlrev_b32_e64 v5, 4, v1\n"
    "s_barrier\n"
    "s_mov_b32 exec_lo, 0x01010101\n"
    "s_mov_b32 exec_hi, 0x01010101\n"
    "buffer_store_dwordx4 v[8:11], v5, s[36:39], s60 offen sc1\n"
    "buffer_store_dwordx4 v[12:15], v5, s[36:39], s60 offen offset:128 sc1\n"
    "s_mov_b64 exec, -1\n"
    "s_getpc_b64 s[60:61]\n"
    ".Lpc2:\n"
    "s_lshl2_add_u32 s62, s35, s53\n"
    "v_and_b32 v5, 31, v0\n"
    "v_lshl_add_u32 v5, v5, 4, s62\n"
    "v_lshlrev_b32_e64 v5, 7, v5\n"
    "v_min_u32 v5, .Lcode_end-.Lpc2-4, v5\n"
    "global_load_dword v5, v5, s[60:61]\n"
    "s_waitcnt vmcnt(0)\n"
    "s_setprio 2\n"
    "s_branch .Lp2_4\n"
    ".Lcompute:\n"
    "s_cmp_eq_u32 s54, 7\n"
    "s_cbranch_scc1 .Lspare\n"
    "s_setprio 2\n"
    "v_bfe_u32 v5, v0, 2, 2\n"
    "v_and_b32 v6, 3, v0\n"
    "v_lshrrev_b32 v20, 1, v4\n"
    "v_xor_b32 v20, v20, v3\n"
    "v_lshlrev_b32 v20, 4, v20\n"
    "v_lshl_add_u32 v9, v4, 7, v20\n"
    "s_and_b32 s61, s35, 1\n"
    "s_cmp_lt_u32 s54, 4\n"
    "s_cselect_b32 s60, s61, s35\n"
    "s_cselect_b32 s61, 33792, 0\n"
    "s_mul_i32 s60, s60, 24576\n"
    "s_add_u32 s60, s60, s61\n"
    "s_add_u32 s60, s60, 49152\n"
    "v_add_u32 v7, s60, v9\n"
    "v_xor_b32 v8, 64, v7\n"
    "v_xor_b32_e64 v10, 64, v9\n"
    "s_waitcnt lgkmcnt(0)\n"
    "s_mov_b32 s36, s12\n"
    "s_and_b32 s37, s13, 0xffff\n"
    "s_mov_b32 s38, 9175040\n"
    "s_mov_b32 s39, 0x20000\n"
    "buffer_load_dword v253, off, s[36:39], 0 sc1\n"
    "s_cmp_lt_u32 s54, 4\n"
    "s_cbranch_scc0 .Lc_ssm\n"
    "s_lshr_b32 s62, s35, 1\n"
    "s_lshl2_add_u32 s60, s57, s62\n"
    "s_mul_i32 s60, s60, 648\n"
    "s_add_u32 s60, s14, s60\n"
    "s_addc_u32 s61, s15, 0\n"
    "v_add_u32 v128, 0, v0\n"
    "v_add_u32 v129, 64, v0\n"
    "v_add_u32 v130, 128, v0\n"
    "v_min_u32 v130, 161, v130\n"
    "v_lshlrev_b32_e64 v132, 2, v128\n"
    "global_load_dword v136, v132, s[60:61]\n"
    "global_load_dword v180, v132, s[60:61] offset:1296\n"
    "v_lshlrev_b32_e64 v133, 2, v129\n"
    "global_load_dword v137, v133, s[60:61]\n"
    "global_load_dword v181, v133, s[60:61] offset:1296\n"
    "v_lshlrev_b32_e64 v134, 2, v130\n"
    "global_load_dword v138, v134, s[60:61]\n"
    "global_load_dword v182, v134, s[60:61] offset:1296\n"
    "v_mov_b64 v[16:17], 0\n"
    "v_mov_b64 v[18:19], 0\n"
    "v_mov_b64 v[20:21], 0\n"
    "v_mov_b64 v[22:23], 0\n"
    "v_mov_b64 v[24:25], 0\n"
    "v_mov_b64 v[26:27], 0\n"
    "v_mov_b64 v[28:29], 0\n"
    "v_mov_b64 v[30:31], 0\n"
    "v_mov_b64 v[32:33], 0\n"
    "v_mov_b64 v[34:35], 0\n"
    "v_mov_b64 v[36:37], 0\n"
    "v_mov_b64 v[38:39], 0\n"
    "v_mov_b64 v[40:41], 0\n"
    "v_mov_b64 v[42:43], 0\n"
    "v_mov_b64 v[44:45], 0\n"
    "v_mov_b64 v[46:47], 0\n"
    "v_mov_b64 v[48:49], 0\n"
    "v_mov_b64 v[50:51], 0\n"
    "v_mov_b64 v[52:53], 0\n"
    "v_mov_b64 v[54:55], 0\n"
    "v_mov_b64 v[56:57], 0\n"
    "v_mov_b64 v[58:59], 0\n"
    "v_mov_b64 v[60:61], 0\n"
    "v_mov_b64 v[62:63], 0\n"
    "v_mov_b64 v[64:65], 0\n"
    "v_mov_b64 v[66:67], 0\n"
    "v_mov_b64 v[68:69], 0\n"
    "v_mov_b64 v[70:71], 0\n"
    "v_mov_b64 v[72:73], 0\n"
    "v_mov_b64 v[74:75], 0\n"
    "v_mov_b64 v[76:77], 0\n"
    "v_mov_b64 v[78:79], 0\n"
    "v_mov_b64 v[80:81], 0\n"
    "v_mov_b64 v[82:83], 0\n"
    "v_mov_b64 v[84:85], 0\n"
    "v_mov_b64 v[86:87], 0\n"
    "v_mov_b64 v[88:89], 0\n"
    "v_mov_b64 v[90:91], 0\n"
    "v_mov_b64 v[92:93], 0\n"
    "v_mov_b64 v[94:95], 0\n"
    "v_mov_b64 v[96:97], 0\n"
    "v_mov_b64 v[98:99], 0\n"
    "v_mov_b64 v[100:101], 0\n"
    "v_mov_b64 v[102:103], 0\n"
    "v_mov_b64 v[104:105], 0\n"
    "v_mov_b64 v[106:107], 0\n"
    "v_mov_b64 v[108:109], 0\n"
    "v_mov_b64 v[110:111], 0\n"
    "v_mov_b64 v[112:113], 0\n"
    "v_mov_b64 v[114:115], 0\n"
    "v_mov_b64 v[116:117], 0\n"
    "v_mov_b64 v[118:119], 0\n"
    "v_mov_b64 v[120:121], 0\n"
    "v_mov_b64 v[122:123], 0\n"
    "v_mov_b64 v[124:125], 0\n"
    "v_mov_b64 v[126:127], 0\n"
    "s_lshl_b32 s63, s35, 6\n"
    "s_add_u32 s63, s63, 135552\n"
    "v_lshrrev_b32 v140, 2, v0\n"
    "v_lshlrev_b32 v140, 8, v140\n"
    "v_lshl_add_u32 v140, v6, 4, v140\n"
    "v_add_u32 v140, s63, v140\n"
    "ds_write_b128 v140, v[16:19] offset:0\n"
    "ds_write_b128 v140, v[16:19] offset:4096\n"
    "ds_write_b128 v140, v[16:19] offset:8192\n"
    "ds_write_b128 v140, v[16:19] offset:12288\n"
    "ds_write_b128 v140, v[16:19] offset:16384\n"
    "v_lshlrev_b32_e64 v140, 4, v6\n"
    "v_add_u32 v140, 163712, v140\n"
    "ds_write_b128 v140, v[16:19]\n"
    "s_mul_i32 s62, s62, 18\n"
    "s_and_b32 s64, s35, 1\n"
    "s_mul_i32 s64, s64, 0x80008000\n"
    "v_and_b32 v160, 1, v3\n"
    "v_lshrrev_b32 v175, 1, v3\n"
    "v_lshl_add_u32 v170, v160, 2, v5\n"
    "s_lshr_b32 s68, s35, 1\n"
    "v_lshl_add_u32 v170, v175, 3, v170\n"
    "s_mul_i32 s65, s68, 72\n"
    "v_mul_u32_u24 v160, 864, v170\n"
    "v_and_b32 v172, 7, v170\n"
    "v_add_u32_e64 v160, s65, v160\n"
    "v_lshl_add_u32 v172, v172, 1, v175\n"
    "s_mul_i32 s65, s68, 10240\n"
    "s_mul_i32 s69, s68, 29696\n"
    "v_mul_u32_u24 v172, 464, v172\n"
    "s_add_u32 s65, s65, 135552\n"
    "v_add_u32 v172, s69, v172\n"
    "s_and_b32 s70, s35, 1\n"
    "v_lshl_add_u32 v173, v6, 3, v172\n"
    "s_lshl_b32 s71, s70, 6\n"
    "v_mul_u32_u24 v142, 7282, v128\n"
    "v_add_u32_e64 v221, s71, v173\n"
    "s_mul_i32 s71, s70, 40\n"
    "v_mul_u32_u24 v148, 7282, v129\n"
    "s_add_u32 s71, s71, 160\n"
    "v_mul_u32_u24 v154, 7282, v130\n"
    "v_add_u32 v222, s71, v173\n"
    "s_lshl_b32 s71, s70, 1\n"
    "v_lshrrev_b32 v142, 16, v142\n"
    "v_subrev_u32 v173, s71, v4\n"
    "v_lshrrev_b32 v148, 16, v148\n"
    "v_cmp_gt_u32 s[74:75], 2, v173\n"
    "v_lshrrev_b32 v154, 16, v154\n"
    "v_min_u32 v174, 1, v6\n"
    "v_mul_u32_u24 v143, 9, v142\n"
    "v_cndmask_b32_e64 v173, -1, v173, s[74:75]\n"
    "v_cmp_eq_u32 s[74:75], 6, v4\n"
    "v_mul_u32_u24 v149, 9, v148\n"
    "v_mul_u32_u24 v155, 9, v154\n"
    "v_mul_u32_u24 v174, 120, v174\n"
    "v_cndmask_b32_e64 v173, v173, 2, s[74:75]\n"
    "v_cmp_eq_u32 s[74:75], 10, v4\n"
    "v_sub_u32 v143, v128, v143\n"
    "v_sub_u32 v149, v129, v149\n"
    "v_sub_u32 v155, v130, v155\n"
    "v_sub_u32 v172, v172, v174\n"
    "v_lshrrev_b32 v146, 1, v143\n"
    "v_lshrrev_b32 v152, 1, v149\n"
    "v_lshrrev_b32_e64 v158, 1, v155\n"
    "v_cndmask_b32_e64 v173, v173, 3, s[74:75]\n"
    "v_add_u32 v231, 192, v172\n"
    "v_lshlrev_b32 v146, 11, v146\n"
    "v_and_b32 v145, 1, v143\n"
    "v_lshlrev_b32 v152, 11, v152\n"
    "v_and_b32 v151, 1, v149\n"
    "v_lshlrev_b32 v158, 11, v158\n"
    "v_and_b32 v157, 1, v155\n"
    "v_and_b32 v172, 0x40000000, v173\n"
    "v_add_u32 v144, s62, v142\n"
    "v_add_u32 v150, s62, v148\n"
    "v_add_u32 v156, s62, v154\n"
    "v_and_b32_e64 v173, 3, v173\n"
    "v_lshl_add_u32 v146, v145, 5, v146\n"
    "v_lshl_add_u32 v152, v151, 5, v152\n"
    "v_lshl_add_u32 v158, v157, 5, v158\n"
    "v_bfe_u32 v145, v144, 2, 2\n"
    "v_bfe_u32 v151, v150, 2, 2\n"
    "v_bfe_u32 v157, v156, 2, 2\n"
    "v_lshl_add_u32 v173, v3, 2, v173\n"
    "s_lshl_b32 s71, s70, 9\n"
    "v_lshl_add_u32 v146, v145, 8, v146\n"
    "v_lshl_add_u32 v152, v151, 8, v152\n"
    "v_lshl_add_u32 v158, v157, 8, v158\n"
    "v_lshl_add_u32 v173, v173, 4, v172\n"
    "s_add_u32 s71, s71, 5120\n"
    "v_and_b32 v145, 3, v144\n"
    "v_and_b32 v151, 3, v150\n"
    "v_and_b32 v157, 3, v156\n"
    "v_add_u32_e64 v14, s71, v173\n"
    "v_lshl_add_u32 v146, v145, 1, v146\n"
    "v_lshl_add_u32 v152, v151, 1, v152\n"
    "v_lshl_add_u32 v158, v157, 1, v158\n"
    "v_lshlrev_b32 v230, 4, v0\n"
    "s_lshl_b32 s71, s70, 11\n"
    "v_bfe_u32 v145, v144, 4, 1\n"
    "v_bfe_u32 v151, v150, 4, 1\n"
    "v_bfe_u32 v157, v156, 4, 1\n"
    "s_cmp_eq_u32 s70, 0\n"
    "v_add_u32 v224, s71, v230\n"
    "v_lshl_add_u32 v146, v145, 3, v146\n"
    "v_lshl_add_u32 v152, v151, 3, v152\n"
    "v_lshl_add_u32 v158, v157, 3, v158\n"
    "s_mul_i32 s66, s35, 14848\n"
    "v_mul_u32_u24 v218, 232, v4\n"
    "s_cselect_b32 s71, 0, -1\n"
    "s_cselect_b32 s74, -1, 0\n"
    "v_lshrrev_b32 v145, 5, v144\n"
    "v_lshrrev_b32 v151, 5, v150\n"
    "v_lshrrev_b32 v157, 5, v156\n"
    "v_min_u32 v161, 2, v6\n"
    "s_lshl4_add_u32 s88, s57, s55\n"
    "v_add_u32 v218, s66, v218\n"
    "v_lshl_add_u32 v146, v145, 10, v146\n"
    "v_lshl_add_u32 v152, v151, 10, v152\n"
    "v_lshl_add_u32 v158, v157, 10, v158\n"
    "v_mul_u32_u24 v161, 144, v161\n"
    "v_xor_b32 v173, s70, v175\n"
    "s_lshl1_add_u32 s88, s88, s68\n"
    "v_mad_u32_u24 v216, v3, 40, v218\n"
    "v_lshl_add_u32 v217, v3, 3, v218\n"
    "v_add_u32 v146, s63, v146\n"
    "v_mul_u32_u24 v145, 264, v145\n"
    "v_add_u32 v152, s63, v152\n"
    "v_mul_u32_u24 v151, 264, v151\n"
    "v_add_u32_e64 v158, s63, v158\n"
    "v_mul_u32_u24 v157, 264, v157\n"
    "v_lshl_add_u32 v9, v6, 3, v160\n"
    "v_add3_u32 v10, v160, v161, 64\n"
    "s_mov_b32 s84, 0\n"
    "s_mov_b32 s85, -1\n"
    "v_add_u32 v218, 224, v218\n"
    "v_lshlrev_b32 v173, 30, v173\n"
    "s_mul_i32 s88, s88, 6400\n"
    "v_add_u32 v147, v145, v146\n"
    "v_add_u32 v153, v151, v152\n"
    "v_add_u32 v159, v157, v158\n"
    "v_add_u32 v11, 55296, v9\n"
    "v_add_u32 v12, 55296, v10\n"
    "s_mov_b32 s86, 0x3fb8aa3b\n"
    "s_mov_b32 s87, 0x3fb8aa3b\n"
    "v_add_u32_e64 v220, s65, v230\n"
    "v_cndmask_b32_e64 v218, v216, v218, s[84:85]\n"
    "v_and_or_b32 v223, v173, s71, v224\n"
    "v_and_or_b32 v225, v173, s74, v224\n"
    "v_and_or_b32 v13, v173, s71, v14\n"
    "v_and_or_b32 v15, v173, s74, v14\n"
    "s_add_u32 s88, s88, 262144\n"
    "s_waitcnt vmcnt(5)\n"
    "v_xor_b32 v136, s64, v136\n"
    "ds_write_b16 v146, v136 offset:0\n"
    "ds_write_b16_d16_hi v146, v136 offset:16\n"
    "ds_write_b16 v147, v136 offset:0\n"
    "ds_write_b16_d16_hi v147, v136 offset:16\n"
    "s_waitcnt vmcnt(4)\n"
    "v_xor_b32 v180, s64, v180\n"
    "ds_write_b16 v146, v180 offset:10240\n"
    "ds_write_b16_d16_hi v146, v180 offset:10256\n"
    "ds_write_b16 v147, v180 offset:10240\n"
    "ds_write_b16_d16_hi v147, v180 offset:10256\n"
    "s_waitcnt vmcnt(3)\n"
    "v_xor_b32 v137, s64, v137\n"
    "ds_write_b16 v152, v137 offset:0\n"
    "ds_write_b16_d16_hi v152, v137 offset:16\n"
    "ds_write_b16 v153, v137 offset:0\n"
    "ds_write_b16_d16_hi v153, v137 offset:16\n"
    "s_waitcnt vmcnt(2)\n"
    "v_xor_b32 v181, s64, v181\n"
    "ds_write_b16 v152, v181 offset:10240\n"
    "ds_write_b16_d16_hi v152, v181 offset:10256\n"
    "ds_write_b16 v153, v181 offset:10240\n"
    "ds_write_b16_d16_hi v153, v181 offset:10256\n"
    "s_waitcnt vmcnt(1)\n"
    "v_xor_b32 v138, s64, v138\n"
    "ds_write_b16 v158, v138 offset:0\n"
    "ds_write_b16_d16_hi v158, v138 offset:16\n"
    "ds_write_b16 v159, v138 offset:0\n"
    "ds_write_b16_d16_hi v159, v138 offset:16\n"
    "s_waitcnt vmcnt(0)\n"
    "v_xor_b32 v182, s64, v182\n"
    "ds_write_b16 v158, v182 offset:10240\n"
    "ds_write_b16_d16_hi v158, v182 offset:10256\n"
    "ds_write_b16 v159, v182 offset:10240\n"
    "ds_write_b16_d16_hi v159, v182 offset:10256\n"
    "s_mov_b32 s60, 3\n"
    "s_nop 0\n"
    "s_waitcnt lgkmcnt(0)\n"
    "s_barrier\n"
    "ds_read_b128 v[184:187], v7 offset:0\n"
    "ds_read_b128 v[188:191], v7 offset:2048\n"
    "ds_read_b128 v[192:195], v7 offset:4096\n"
    "ds_read_b128 v[196:199], v7 offset:6144\n"
    "ds_read_b64_tr_b16 v[128:129], v9 offset:144\n"
    "ds_read_b64_tr_b16 v[130:131], v9 offset:576\n"
    "ds_read_b64_tr_b16 v[132:133], v9 offset:176\n"
    "ds_read_b64_tr_b16 v[134:135], v9 offset:608\n"
    "ds_read_b64_tr_b16 v[136:137], v9 offset:0\n"
    "ds_read_b64_tr_b16 v[138:139], v9 offset:432\n"
    "ds_read_b64_tr_b16 v[140:141], v9 offset:32\n"
    "ds_read_b64_tr_b16 v[142:143], v9 offset:464\n"
    "ds_read_b64_tr_b16 v[144:145], v9 offset:288\n"
    "ds_read_b64_tr_b16 v[146:147], v9 offset:720\n"
    "ds_read_b64_tr_b16 v[148:149], v9 offset:320\n"
    "ds_read_b64_tr_b16 v[150:151], v9 offset:752\n"
    "ds_read_b64_tr_b16 v[152:153], v10 offset:0\n"
    "ds_read_b64_tr_b16 v[154:155], v10 offset:432\n"
    "s_waitcnt lgkmcnt(0)\n"
    ".Lat_loop:\n"
    "v_mfma_f32_16x16x32_bf16 v[16:19], v[128:131], v[184:187], v[16:19]\n"
    "ds_read_b128 v[200:203], v8 offset:0\n"
    "v_mfma_f32_16x16x32_bf16 v[20:23], v[128:131], v[188:191], v[20:23]\n"
    "ds_read_b128 v[204:207], v8 offset:2048\n"
    "v_mfma_f32_16x16x32_bf16 v[24:27], v[128:131], v[192:195], v[24:27]\n"
    "ds_read_b128 v[208:211], v8 offset:4096\n"
    "v_mfma_f32_16x16x32_bf16 v[28:31], v[128:131], v[196:199], v[28:31]\n"
    "ds_read_b128 v[212:215], v8 offset:6144\n"
    "v_mfma_f32_16x16x32_bf16 v[32:35], v[132:135], v[184:187], v[32:35]\n"
    "ds_read_b64_tr_b16 v[156:157], v9 offset:13968\n"
    "v_mfma_f32_16x16x32_bf16 v[36:39], v[132:135], v[188:191], v[36:39]\n"
    "ds_read_b64_tr_b16 v[158:159], v9 offset:14400\n"
    "v_mfma_f32_16x16x32_bf16 v[40:43], v[132:135], v[192:195], v[40:43]\n"
    "ds_read_b64_tr_b16 v[160:161], v9 offset:14000\n"
    "v_mfma_f32_16x16x32_bf16 v[44:47], v[132:135], v[196:199], v[44:47]\n"
    "ds_read_b64_tr_b16 v[162:163], v9 offset:14432\n"
    "v_mfma_f32_16x16x32_bf16 v[48:51], v[136:139], v[184:187], v[48:51]\n"
    "ds_read_b64_tr_b16 v[164:165], v9 offset:13824\n"
    "v_mfma_f32_16x16x32_bf16 v[52:55], v[136:139], v[188:191], v[52:55]\n"
    "ds_read_b64_tr_b16 v[166:167], v9 offset:14256\n"
    "v_mfma_f32_16x16x32_bf16 v[56:59], v[136:139], v[192:195], v[56:59]\n"
    "ds_read_b64_tr_b16 v[168:169], v9 offset:13856\n"
    "v_mfma_f32_16x16x32_bf16 v[60:63], v[136:139], v[196:199], v[60:63]\n"
    "ds_read_b64_tr_b16 v[170:171], v9 offset:14288\n"
    "v_mfma_f32_16x16x32_bf16 v[64:67], v[140:143], v[184:187], v[64:67]\n"
    "ds_read_b64_tr_b16 v[172:173], v9 offset:14112\n"
    "v_mfma_f32_16x16x32_bf16 v[68:71], v[140:143], v[188:191], v[68:71]\n"
    "ds_read_b64_tr_b16 v[174:175], v9 offset:14544\n"
    "v_mfma_f32_16x16x32_bf16 v[72:75], v[140:143], v[192:195], v[72:75]\n"
    "ds_read_b64_tr_b16 v[176:177], v9 offset:14144\n"
    "v_mfma_f32_16x16x32_bf16 v[76:79], v[140:143], v[196:199], v[76:79]\n"
    "ds_read_b64_tr_b16 v[178:179], v9 offset:14576\n"
    "v_mfma_f32_16x16x32_bf16 v[80:83], v[144:147], v[184:187], v[80:83]\n"
    "ds_read_b64_tr_b16 v[180:181], v10 offset:13824\n"
    "v_mfma_f32_16x16x32_bf16 v[84:87], v[144:147], v[188:191], v[84:87]\n"
    "ds_read_b64_tr_b16 v[182:183], v10 offset:14256\n"
    "v_mfma_f32_16x16x32_bf16 v[88:91], v[144:147], v[192:195], v[88:91]\n"
    "v_mfma_f32_16x16x32_bf16 v[92:95], v[144:147], v[196:199], v[92:95]\n"
    "v_mfma_f32_16x16x32_bf16 v[96:99], v[148:151], v[184:187], v[96:99]\n"
    "v_mfma_f32_16x16x32_bf16 v[100:103], v[148:151], v[188:191], v[100:103]\n"
    "v_mfma_f32_16x16x32_bf16 v[104:107], v[148:151], v[192:195], v[104:107]\n"
    "v_mfma_f32_16x16x32_bf16 v[108:111], v[148:151], v[196:199], v[108:111]\n"
    "v_mfma_f32_16x16x32_bf16 v[112:115], v[152:155], v[184:187], v[112:115]\n"
    "v_mfma_f32_16x16x32_bf16 v[116:119], v[152:155], v[188:191], v[116:119]\n"
    "v_mfma_f32_16x16x32_bf16 v[120:123], v[152:155], v[192:195], v[120:123]\n"
    "v_mfma_f32_16x16x32_bf16 v[124:127], v[152:155], v[196:199], v[124:127]\n"
    "s_waitcnt lgkmcnt(0)\n"
    "s_barrier\n"
    "v_mfma_f32_16x16x32_bf16 v[16:19], v[156:159], v[200:203], v[16:19]\n"
    "ds_read_b128 v[184:187], v7 offset:8192\n"
    "v_mfma_f32_16x16x32_bf16 v[20:23], v[156:159], v[204:207], v[20:23]\n"
    "ds_read_b128 v[188:191], v7 offset:10240\n"
    "v_mfma_f32_16x16x32_bf16 v[24:27], v[156:159], v[208:211], v[24:27]\n"
    "ds_read_b128 v[192:195], v7 offset:12288\n"
    "v_mfma_f32_16x16x32_bf16 v[28:31], v[156:159], v[212:215], v[28:31]\n"
    "ds_read_b128 v[196:199], v7 offset:14336\n"
    "v_mfma_f32_16x16x32_bf16 v[32:35], v[160:163], v[200:203], v[32:35]\n"
    "ds_read_b64_tr_b16 v[128:129], v9 offset:27792\n"
    "v_mfma_f32_16x16x32_bf16 v[36:39], v[160:163], v[204:207], v[36:39]\n"
    "ds_read_b64_tr_b16 v[130:131], v9 offset:28224\n"
    "v_mfma_f32_16x16x32_bf16 v[40:43], v[160:163], v[208:211], v[40:43]\n"
    "ds_read_b64_tr_b16 v[132:133], v9 offset:27824\n"
    "v_mfma_f32_16x16x32_bf16 v[44:47], v[160:163], v[212:215], v[44:47]\n"
    "ds_read_b64_tr_b16 v[134:135], v9 offset:28256\n"
    "v_mfma_f32_16x16x32_bf16 v[48:51], v[164:167], v[200:203], v[48:51]\n"
    "ds_read_b64_tr_b16 v[136:137], v9 offset:27648\n"
    "v_mfma_f32_16x16x32_bf16 v[52:55], v[164:167], v[204:207], v[52:55]\n"
    "ds_read_b64_tr_b16 v[138:139], v9 offset:28080\n"
    "v_mfma_f32_16x16x32_bf16 v[56:59], v[164:167], v[208:211], v[56:59]\n"
    "ds_read_b64_tr_b16 v[140:141], v9 offset:27680\n"
    "v_mfma_f32_16x16x32_bf16 v[60:63], v[164:167], v[212:215], v[60:63]\n"
    "ds_read_b64_tr_b16 v[142:143], v9 offset:28112\n"
    "v_mfma_f32_16x16x32_bf16 v[64:67], v[168:171], v[200:203], v[64:67]\n"
    "ds_read_b64_tr_b16 v[144:145], v9 offset:27936\n"
    "v_mfma_f32_16x16x32_bf16 v[68:71], v[168:171], v[204:207], v[68:71]\n"
    "ds_read_b64_tr_b16 v[146:147], v9 offset:28368\n"
    "v_mfma_f32_16x16x32_bf16 v[72:75], v[168:171], v[208:211], v[72:75]\n"
    "ds_read_b64_tr_b16 v[148:149], v9 offset:27968\n"
    "v_mfma_f32_16x16x32_bf16 v[76:79], v[168:171], v[212:215], v[76:79]\n"
    "ds_read_b64_tr_b16 v[150:151], v9 offset:28400\n"
    "v_mfma_f32_16x16x32_bf16 v[80:83], v[172:175], v[200:203], v[80:83]\n"
    "ds_read_b64_tr_b16 v[152:153], v10 offset:27648\n"
    "v_mfma_f32_16x16x32_bf16 v[84:87], v[172:175], v[204:207], v[84:87]\n"
    "ds_read_b64_tr_b16 v[154:155], v10 offset:28080\n"
    "v_mfma_f32_16x16x32_bf16 v[88:91], v[172:175], v[208:211], v[88:91]\n"
    "v_mfma_f32_16x16x32_bf16 v[92:95], v[172:175], v[212:215], v[92:95]\n"
    "v_mfma_f32_16x16x32_bf16 v[96:99], v[176:179], v[200:203], v[96:99]\n"
    "v_mfma_f32_16x16x32_bf16 v[100:103], v[176:179], v[204:207], v[100:103]\n"
    "v_mfma_f32_16x16x32_bf16 v[104:107], v[176:179], v[208:211], v[104:107]\n"
    "v_mfma_f32_16x16x32_bf16 v[108:111], v[176:179], v[212:215], v[108:111]\n"
    "v_mfma_f32_16x16x32_bf16 v[112:115], v[180:183], v[200:203], v[112:115]\n"
    "v_mfma_f32_16x16x32_bf16 v[116:119], v[180:183], v[204:207], v[116:119]\n"
    "v_mfma_f32_16x16x32_bf16 v[120:123], v[180:183], v[208:211], v[120:123]\n"
    "v_mfma_f32_16x16x32_bf16 v[124:127], v[180:183], v[212:215], v[124:127]\n"
    "s_waitcnt lgkmcnt(0)\n"
    "v_mfma_f32_16x16x32_bf16 v[16:19], v[128:131], v[184:187], v[16:19]\n"
    "ds_read_b128 v[200:203], v8 offset:8192\n"
    "v_mfma_f32_16x16x32_bf16 v[20:23], v[128:131], v[188:191], v[20:23]\n"
    "ds_read_b128 v[204:207], v8 offset:10240\n"
    "v_mfma_f32_16x16x32_bf16 v[24:27], v[128:131], v[192:195], v[24:27]\n"
    "ds_read_b128 v[208:211], v8 offset:12288\n"
    "v_mfma_f32_16x16x32_bf16 v[28:31], v[128:131], v[196:199], v[28:31]\n"
    "ds_read_b128 v[212:215], v8 offset:14336\n"
    "v_mfma_f32_16x16x32_bf16 v[32:35], v[132:135], v[184:187], v[32:35]\n"
    "ds_read_b64_tr_b16 v[156:157], v9 offset:41616\n"
    "v_mfma_f32_16x16x32_bf16 v[36:39], v[132:135], v[188:191], v[36:39]\n"
    "ds_read_b64_tr_b16 v[158:159], v9 offset:42048\n"
    "v_mfma_f32_16x16x32_bf16 v[40:43], v[132:135], v[192:195], v[40:43]\n"
    "ds_read_b64_tr_b16 v[160:161], v9 offset:41648\n"
    "v_mfma_f32_16x16x32_bf16 v[44:47], v[132:135], v[196:199], v[44:47]\n"
    "ds_read_b64_tr_b16 v[162:163], v9 offset:42080\n"
    "v_mfma_f32_16x16x32_bf16 v[48:51], v[136:139], v[184:187], v[48:51]\n"
    "ds_read_b64_tr_b16 v[164:165], v9 offset:41472\n"
    "v_mfma_f32_16x16x32_bf16 v[52:55], v[136:139], v[188:191], v[52:55]\n"
    "ds_read_b64_tr_b16 v[166:167], v9 offset:41904\n"
    "v_mfma_f32_16x16x32_bf16 v[56:59], v[136:139], v[192:195], v[56:59]\n"
    "ds_read_b64_tr_b16 v[168:169], v9 offset:41504\n"
    "v_mfma_f32_16x16x32_bf16 v[60:63], v[136:139], v[196:199], v[60:63]\n"
    "ds_read_b64_tr_b16 v[170:171], v9 offset:41936\n"
    "v_mfma_f32_16x16x32_bf16 v[64:67], v[140:143], v[184:187], v[64:67]\n"
    "ds_read_b64_tr_b16 v[172:173], v9 offset:41760\n"
    "v_mfma_f32_16x16x32_bf16 v[68:71], v[140:143], v[188:191], v[68:71]\n"
    "ds_read_b64_tr_b16 v[174:175], v9 offset:42192\n"
    "v_mfma_f32_16x16x32_bf16 v[72:75], v[140:143], v[192:195], v[72:75]\n"
    "ds_read_b64_tr_b16 v[176:177], v9 offset:41792\n"
    "v_mfma_f32_16x16x32_bf16 v[76:79], v[140:143], v[196:199], v[76:79]\n"
    "ds_read_b64_tr_b16 v[178:179], v9 offset:42224\n"
    "v_mfma_f32_16x16x32_bf16 v[80:83], v[144:147], v[184:187], v[80:83]\n"
    "ds_read_b64_tr_b16 v[180:181], v10 offset:41472\n"
    "v_mfma_f32_16x16x32_bf16 v[84:87], v[144:147], v[188:191], v[84:87]\n"
    "ds_read_b64_tr_b16 v[182:183], v10 offset:41904\n"
    "v_mfma_f32_16x16x32_bf16 v[88:91], v[144:147], v[192:195], v[88:91]\n"
    "v_mfma_f32_16x16x32_bf16 v[92:95], v[144:147], v[196:199], v[92:95]\n"
    "v_mfma_f32_16x16x32_bf16 v[96:99], v[148:151], v[184:187], v[96:99]\n"
    "v_mfma_f32_16x16x32_bf16 v[100:103], v[148:151], v[188:191], v[100:103]\n"
    "v_mfma_f32_16x16x32_bf16 v[104:107], v[148:151], v[192:195], v[104:107]\n"
    "v_mfma_f32_16x16x32_bf16 v[108:111], v[148:151], v[196:199], v[108:111]\n"
    "v_mfma_f32_16x16x32_bf16 v[112:115], v[152:155], v[184:187], v[112:115]\n"
    "v_mfma_f32_16x16x32_bf16 v[116:119], v[152:155], v[188:191], v[116:119]\n"
    "v_mfma_f32_16x16x32_bf16 v[120:123], v[152:155], v[192:195], v[120:123]\n"
    "v_mfma_f32_16x16x32_bf16 v[124:127], v[152:155], v[196:199], v[124:127]\n"
    "s_waitcnt lgkmcnt(0)\n"
    "s_barrier\n"
    "v_mfma_f32_16x16x32_bf16 v[16:19], v[156:159], v[200:203], v[16:19]\n"
    "ds_read_b128 v[184:187], v7 offset:16384\n"
    "v_mfma_f32_16x16x32_bf16 v[20:23], v[156:159], v[204:207], v[20:23]\n"
    "ds_read_b128 v[188:191], v7 offset:18432\n"
    "v_mfma_f32_16x16x32_bf16 v[24:27], v[156:159], v[208:211], v[24:27]\n"
    "ds_read_b128 v[192:195], v7 offset:20480\n"
    "v_mfma_f32_16x16x32_bf16 v[28:31], v[156:159], v[212:215], v[28:31]\n"
    "ds_read_b128 v[196:199], v7 offset:22528\n"
    "v_mfma_f32_16x16x32_bf16 v[32:35], v[160:163], v[200:203], v[32:35]\n"
    "ds_read_b64_tr_b16 v[128:129], v11 offset:144\n"
    "v_mfma_f32_16x16x32_bf16 v[36:39], v[160:163], v[204:207], v[36:39]\n"
    "ds_read_b64_tr_b16 v[130:131], v11 offset:576\n"
    "v_mfma_f32_16x16x32_bf16 v[40:43], v[160:163], v[208:211], v[40:43]\n"
    "ds_read_b64_tr_b16 v[132:133], v11 offset:176\n"
    "v_mfma_f32_16x16x32_bf16 v[44:47], v[160:163], v[212:215], v[44:47]\n"
    "ds_read_b64_tr_b16 v[134:135], v11 offset:608\n"
    "v_mfma_f32_16x16x32_bf16 v[48:51], v[164:167], v[200:203], v[48:51]\n"
    "ds_read_b64_tr_b16 v[136:137], v11 offset:0\n"
    "v_mfma_f32_16x16x32_bf16 v[52:55], v[164:167], v[204:207], v[52:55]\n"
    "ds_read_b64_tr_b16 v[138:139], v11 offset:432\n"
    "v_mfma_f32_16x16x32_bf16 v[56:59], v[164:167], v[208:211], v[56:59]\n"
    "ds_read_b64_tr_b16 v[140:141], v11 offset:32\n"
    "v_mfma_f32_16x16x32_bf16 v[60:63], v[164:167], v[212:215], v[60:63]\n"
    "ds_read_b64_tr_b16 v[142:143], v11 offset:464\n"
    "v_mfma_f32_16x16x32_bf16 v[64:67], v[168:171], v[200:203], v[64:67]\n"
    "ds_read_b64_tr_b16 v[144:145], v11 offset:288\n"
    "v_mfma_f32_16x16x32_bf16 v[68:71], v[168:171], v[204:207], v[68:71]\n"
    "ds_read_b64_tr_b16 v[146:147], v11 offset:720\n"
    "v_mfma_f32_16x16x32_bf16 v[72:75], v[168:171], v[208:211], v[72:75]\n"
    "ds_read_b64_tr_b16 v[148:149], v11 offset:320\n"
    "v_mfma_f32_16x16x32_bf16 v[76:79], v[168:171], v[212:215], v[76:79]\n"
    "ds_read_b64_tr_b16 v[150:151], v11 offset:752\n"
    "v_mfma_f32_16x16x32_bf16 v[80:83], v[172:175], v[200:203], v[80:83]\n"
    "ds_read_b64_tr_b16 v[152:153], v12 offset:0\n"
    "v_mfma_f32_16x16x32_bf16 v[84:87], v[172:175], v[204:207], v[84:87]\n"
    "ds_read_b64_tr_b16 v[154:155], v12 offset:432\n"
    "v_mfma_f32_16x16x32_bf16 v[88:91], v[172:175], v[208:211], v[88:91]\n"
    "v_mfma_f32_16x16x32_bf16 v[92:95], v[172:175], v[212:215], v[92:95]\n"
    "v_mfma_f32_16x16x32_bf16 v[96:99], v[176:179], v[200:203], v[96:99]\n"
    "v_mfma_f32_16x16x32_bf16 v[100:103], v[176:179], v[204:207], v[100:103]\n"
    "v_mfma_f32_16x16x32_bf16 v[104:107], v[176:179], v[208:211], v[104:107]\n"
    "v_mfma_f32_16x16x32_bf16 v[108:111], v[176:179], v[212:215], v[108:111]\n"
    "v_mfma_f32_16x16x32_bf16 v[112:115], v[180:183], v[200:203], v[112:115]\n"
    "v_mfma_f32_16x16x32_bf16 v[116:119], v[180:183], v[204:207], v[116:119]\n"
    "v_mfma_f32_16x16x32_bf16 v[120:123], v[180:183], v[208:211], v[120:123]\n"
    "v_mfma_f32_16x16x32_bf16 v[124:127], v[180:183], v[212:215], v[124:127]\n"
    "s_waitcnt lgkmcnt(0)\n"
    "v_mfma_f32_16x16x32_bf16 v[16:19], v[128:131], v[184:187], v[16:19]\n"
    "ds_read_b128 v[200:203], v8 offset:16384\n"
    "v_mfma_f32_16x16x32_bf16 v[20:23], v[128:131], v[188:191], v[20:23]\n"
    "ds_read_b128 v[204:207], v8 offset:18432\n"
    "v_mfma_f32_16x16x32_bf16 v[24:27], v[128:131], v[192:195], v[24:27]\n"
    "ds_read_b128 v[208:211], v8 offset:20480\n"
    "v_mfma_f32_16x16x32_bf16 v[28:31], v[128:131], v[196:199], v[28:31]\n"
    "ds_read_b128 v[212:215], v8 offset:22528\n"
    "v_mfma_f32_16x16x32_bf16 v[32:35], v[132:135], v[184:187], v[32:35]\n"
    "ds_read_b64_tr_b16 v[156:157], v11 offset:13968\n"
    "v_mfma_f32_16x16x32_bf16 v[36:39], v[132:135], v[188:191], v[36:39]\n"
    "ds_read_b64_tr_b16 v[158:159], v11 offset:14400\n"
    "v_mfma_f32_16x16x32_bf16 v[40:43], v[132:135], v[192:195], v[40:43]\n"
    "ds_read_b64_tr_b16 v[160:161], v11 offset:14000\n"
    "v_mfma_f32_16x16x32_bf16 v[44:47], v[132:135], v[196:199], v[44:47]\n"
    "ds_read_b64_tr_b16 v[162:163], v11 offset:14432\n"
    "v_mfma_f32_16x16x32_bf16 v[48:51], v[136:139], v[184:187], v[48:51]\n"
    "ds_read_b64_tr_b16 v[164:165], v11 offset:13824\n"
    "v_mfma_f32_16x16x32_bf16 v[52:55], v[136:139], v[188:191], v[52:55]\n"
    "ds_read_b64_tr_b16 v[166:167], v11 offset:14256\n"
    "v_mfma_f32_16x16x32_bf16 v[56:59], v[136:139], v[192:195], v[56:59]\n"
    "ds_read_b64_tr_b16 v[168:169], v11 offset:13856\n"
    "v_mfma_f32_16x16x32_bf16 v[60:63], v[136:139], v[196:199], v[60:63]\n"
    "ds_read_b64_tr_b16 v[170:171], v11 offset:14288\n"
    "v_mfma_f32_16x16x32_bf16 v[64:67], v[140:143], v[184:187], v[64:67]\n"
    "ds_read_b64_tr_b16 v[172:173], v11 offset:14112\n"
    "v_mfma_f32_16x16x32_bf16 v[68:71], v[140:143], v[188:191], v[68:71]\n"
    "ds_read_b64_tr_b16 v[174:175], v11 offset:14544\n"
    "v_mfma_f32_16x16x32_bf16 v[72:75], v[140:143], v[192:195], v[72:75]\n"
    "ds_read_b64_tr_b16 v[176:177], v11 offset:14144\n"
    "v_mfma_f32_16x16x32_bf16 v[76:79], v[140:143], v[196:199], v[76:79]\n"
    "ds_read_b64_tr_b16 v[178:179], v11 offset:14576\n"
    "v_mfma_f32_16x16x32_bf16 v[80:83], v[144:147], v[184:187], v[80:83]\n"
    "ds_read_b64_tr_b16 v[180:181], v12 offset:13824\n"
    "v_mfma_f32_16x16x32_bf16 v[84:87], v[144:147], v[188:191], v[84:87]\n"
    "ds_read_b64_tr_b16 v[182:183], v12 offset:14256\n"
    "v_mfma_f32_16x16x32_bf16 v[88:91], v[144:147], v[192:195], v[88:91]\n"
    "v_mfma_f32_16x16x32_bf16 v[92:95], v[144:147], v[196:199], v[92:95]\n"
    "v_mfma_f32_16x16x32_bf16 v[96:99], v[148:151], v[184:187], v[96:99]\n"
    "v_mfma_f32_16x16x32_bf16 v[100:103], v[148:151], v[188:191], v[100:103]\n"
    "v_mfma_f32_16x16x32_bf16 v[104:107], v[148:151], v[192:195], v[104:107]\n"
    "v_mfma_f32_16x16x32_bf16 v[108:111], v[148:151], v[196:199], v[108:111]\n"
    "v_mfma_f32_16x16x32_bf16 v[112:115], v[152:155], v[184:187], v[112:115]\n"
    "v_mfma_f32_16x16x32_bf16 v[116:119], v[152:155], v[188:191], v[116:119]\n"
    "v_mfma_f32_16x16x32_bf16 v[120:123], v[152:155], v[192:195], v[120:123]\n"
    "v_mfma_f32_16x16x32_bf16 v[124:127], v[152:155], v[196:199], v[124:127]\n"
    "s_sub_u32 s60, s60, 1\n"
    "s_cmp_eq_u32 s60, 0\n"
    "s_waitcnt lgkmcnt(0)\n"
    "s_barrier\n"
    "s_cbranch_scc1 .Lat_loopx\n"
    "v_mfma_f32_16x16x32_bf16 v[16:19], v[156:159], v[200:203], v[16:19]\n"
    "ds_read_b128 v[184:187], v7 offset:0\n"
    "v_mfma_f32_16x16x32_bf16 v[20:23], v[156:159], v[204:207], v[20:23]\n"
    "ds_read_b128 v[188:191], v7 offset:2048\n"
    "v_mfma_f32_16x16x32_bf16 v[24:27], v[156:159], v[208:211], v[24:27]\n"
    "ds_read_b128 v[192:195], v7 offset:4096\n"
    "v_mfma_f32_16x16x32_bf16 v[28:31], v[156:159], v[212:215], v[28:31]\n"
    "ds_read_b128 v[196:199], v7 offset:6144\n"
    "v_mfma_f32_16x16x32_bf16 v[32:35], v[160:163], v[200:203], v[32:35]\n"
    "ds_read_b64_tr_b16 v[128:129], v9 offset:144\n"
    "v_mfma_f32_16x16x32_bf16 v[36:39], v[160:163], v[204:207], v[36:39]\n"
    "ds_read_b64_tr_b16 v[130:131], v9 offset:576\n"
    "v_mfma_f32_16x16x32_bf16 v[40:43], v[160:163], v[208:211], v[40:43]\n"
    "ds_read_b64_tr_b16 v[132:133], v9 offset:176\n"
    "v_mfma_f32_16x16x32_bf16 v[44:47], v[160:163], v[212:215], v[44:47]\n"
    "ds_read_b64_tr_b16 v[134:135], v9 offset:608\n"
    "v_mfma_f32_16x16x32_bf16 v[48:51], v[164:167], v[200:203], v[48:51]\n"
    "ds_read_b64_tr_b16 v[136:137], v9 offset:0\n"
    "v_mfma_f32_16x16x32_bf16 v[52:55], v[164:167], v[204:207], v[52:55]\n"
    "ds_read_b64_tr_b16 v[138:139], v9 offset:432\n"
    "v_mfma_f32_16x16x32_bf16 v[56:59], v[164:167], v[208:211], v[56:59]\n"
    "ds_read_b64_tr_b16 v[140:141], v9 offset:32\n"
    "v_mfma_f32_16x16x32_bf16 v[60:63], v[164:167], v[212:215], v[60:63]\n"
    "ds_read_b64_tr_b16 v[142:143], v9 offset:464\n"
    "v_mfma_f32_16x16x32_bf16 v[64:67], v[168:171], v[200:203], v[64:67]\n"
    "ds_read_b64_tr_b16 v[144:145], v9 offset:288\n"
    "v_mfma_f32_16x16x32_bf16 v[68:71], v[168:171], v[204:207], v[68:71]\n"
    "ds_read_b64_tr_b16 v[146:147], v9 offset:720\n"
    "v_mfma_f32_16x16x32_bf16 v[72:75], v[168:171], v[208:211], v[72:75]\n"
    "ds_read_b64_tr_b16 v[148:149], v9 offset:320\n"
    "v_mfma_f32_16x16x32_bf16 v[76:79], v[168:171], v[212:215], v[76:79]\n"
    "ds_read_b64_tr_b16 v[150:151], v9 offset:752\n"
    "v_mfma_f32_16x16x32_bf16 v[80:83], v[172:175], v[200:203], v[80:83]\n"
    "ds_read_b64_tr_b16 v[152:153], v10 offset:0\n"
    "v_mfma_f32_16x16x32_bf16 v[84:87], v[172:175], v[204:207], v[84:87]\n"
    "ds_read_b64_tr_b16 v[154:155], v10 offset:432\n"
    "v_mfma_f32_16x16x32_bf16 v[88:91], v[172:175], v[208:211], v[88:91]\n"
    "v_mfma_f32_16x16x32_bf16 v[92:95], v[172:175], v[212:215], v[92:95]\n"
    "v_mfma_f32_16x16x32_bf16 v[96:99], v[176:179], v[200:203], v[96:99]\n"
    "v_mfma_f32_16x16x32_bf16 v[100:103], v[176:179], v[204:207], v[100:103]\n"
    "v_mfma_f32_16x16x32_bf16 v[104:107], v[176:179], v[208:211], v[104:107]\n"
    "v_mfma_f32_16x16x32_bf16 v[108:111], v[176:179], v[212:215], v[108:111]\n"
    "v_mfma_f32_16x16x32_bf16 v[112:115], v[180:183], v[200:203], v[112:115]\n"
    "v_mfma_f32_16x16x32_bf16 v[116:119], v[180:183], v[204:207], v[116:119]\n"
    "v_mfma_f32_16x16x32_bf16 v[120:123], v[180:183], v[208:211], v[120:123]\n"
    "v_mfma_f32_16x16x32_bf16 v[124:127], v[180:183], v[212:215], v[124:127]\n"
    "s_waitcnt lgkmcnt(0)\n"
    "s_branch .Lat_loop\n"
    "s_nop 0\n"
    ".Lat_loopx:\n"
    "s_waitcnt vmcnt(0)\n"
    "v_readfirstlane_b32 s34, v253\n"
    "s_nop 0\n"
    "s_add_u32 s34, s34, 0x9E3779B9\n"
    "v_mfma_f32_16x16x32_bf16 v[16:19], v[156:159], v[200:203], v[16:19]\n"
    "ds_read_b128 v[128:131], v220 offset:0\n"
    "v_mfma_f32_16x16x32_bf16 v[32:35], v[160:163], v[200:203], v[32:35]\n"
    "ds_read_b128 v[132:135], v220 offset:2048\n"
    "v_mfma_f32_16x16x32_bf16 v[112:115], v[180:183], v[200:203], v[112:115]\n"
    "ds_read_b128 v[136:139], v220 offset:4096\n"
    "v_mfma_f32_16x16x32_bf16 v[20:23], v[156:159], v[204:207], v[20:23]\n"
    "ds_read_b128 v[140:143], v220 offset:6144\n"
    "v_mfma_f32_16x16x32_bf16 v[24:27], v[156:159], v[208:211], v[24:27]\n"
    "ds_read_b128 v[148:151], v220 offset:1024\n"
    "v_mfma_f32_16x16x32_bf16 v[28:31], v[156:159], v[212:215], v[28:31]\n"
    "ds_read_b128 v[152:155], v220 offset:3072\n"
    "v_mfma_f32_16x16x32_bf16 v[36:39], v[160:163], v[204:207], v[36:39]\n"
    "ds_read_b128 v[144:147], v220 offset:8192\n"
    "v_mfma_f32_16x16x32_bf16 v[40:43], v[160:163], v[208:211], v[40:43]\n"
    "ds_read_b128 v[184:187], v220 offset:5120\n"
    "v_mfma_f32_16x16x32_bf16 v[44:47], v[160:163], v[212:215], v[44:47]\n"
    "ds_read_b128 v[188:191], v220 offset:7168\n"
    "v_mfma_f32_16x16x32_bf16 v[116:119], v[180:183], v[204:207], v[116:119]\n"
    "ds_read_b128 v[192:195], v220 offset:9216\n"
    "v_mfma_f32_16x16x32_bf16 v[80:83], v[172:175], v[200:203], v[80:83]\n"
    "v_cvt_pk_bf16_f32 v16, v16, v17\n"
    "v_cvt_pk_bf16_f32 v17, v18, v19\n"
    "v_mfma_f32_16x16x32_bf16 v[84:87], v[172:175], v[204:207], v[84:87]\n"
    "v_cvt_pk_bf16_f32 v18, v32, v33\n"
    "v_cvt_pk_bf16_f32 v19, v34, v35\n"
    "v_mfma_f32_16x16x32_bf16 v[88:91], v[172:175], v[208:211], v[88:91]\n"
    "v_cvt_pk_bf16_f32 v112, v112, v113\n"
    "v_cvt_pk_bf16_f32 v113, v114, v115\n"
    "s_waitcnt lgkmcnt(9)\n"
    "v_mfma_f32_16x16x32_bf16 v[232:235], v[128:131], v[16:19], 0\n"
    "ds_write_b64 v218, v[112:113] offset:0\n"
    "s_waitcnt lgkmcnt(9)\n"
    "v_mfma_f32_16x16x32_bf16 v[236:239], v[132:135], v[16:19], 0\n"
    "v_cvt_pk_bf16_f32 v20, v20, v21\n"
    "v_cvt_pk_bf16_f32 v24, v24, v25\n"
    "s_waitcnt lgkmcnt(8)\n"
    "v_mfma_f32_16x16x32_bf16 v[240:243], v[136:139], v[16:19], 0\n"
    "v_cvt_pk_bf16_f32 v28, v28, v29\n"
    "v_cvt_pk_bf16_f32 v21, v22, v23\n"
    "s_waitcnt lgkmcnt(7)\n"
    "v_mfma_f32_16x16x32_bf16 v[244:247], v[140:143], v[16:19], 0\n"
    "v_cvt_pk_bf16_f32 v25, v26, v27\n"
    "v_cvt_pk_bf16_f32 v29, v30, v31\n"
    "s_waitcnt lgkmcnt(6)\n"
    "v_mfma_f32_16x16x16_bf16 v[232:235], v[150:151], v[112:113], v[232:235]\n"
    "v_cvt_pk_bf16_f32 v22, v36, v37\n"
    "v_cvt_pk_bf16_f32 v23, v38, v39\n"
    "s_waitcnt lgkmcnt(5)\n"
    "v_mfma_f32_16x16x16_bf16 v[236:239], v[154:155], v[112:113], v[236:239]\n"
    "v_cvt_pk_bf16_f32 v26, v40, v41\n"
    "v_cvt_pk_bf16_f32 v27, v42, v43\n"
    "s_waitcnt lgkmcnt(4)\n"
    "v_mfma_f32_16x16x32_bf16 v[248:251], v[144:147], v[16:19], 0\n"
    "v_cvt_pk_bf16_f32 v30, v44, v45\n"
    "v_cvt_pk_bf16_f32 v31, v46, v47\n"
    "s_waitcnt lgkmcnt(3)\n"
    "v_mfma_f32_16x16x16_bf16 v[240:243], v[186:187], v[112:113], v[240:243]\n"
    "v_max3_f32 v114, v232, v233, v234\n"
    "v_max3_f32 v115, v235, v236, v237\n"
    "s_waitcnt lgkmcnt(2)\n"
    "v_mfma_f32_16x16x16_bf16 v[244:247], v[190:191], v[112:113], v[244:247]\n"
    "v_cvt_pk_bf16_f32 v116, v116, v117\n"
    "v_max3_f32 v114, v114, v238, v239\n"
    "s_waitcnt lgkmcnt(1)\n"
    "v_mfma_f32_16x16x16_bf16 v[248:251], v[194:195], v[112:113], v[248:251]\n"
    "v_cvt_pk_bf16_f32 v117, v118, v119\n"
    "v_max3_f32 v115, v115, v240, v241\n"
    "v_mfma_f32_16x16x32_bf16 v[32:35], v[128:131], v[20:23], 0\n"
    "ds_write_b64 v218, v[116:117] offset:3712\n"
    "v_mfma_f32_16x16x32_bf16 v[36:39], v[132:135], v[20:23], 0\n"
    "v_max3_f32 v114, v114, v242, v243\n"
    "v_max3_f32 v115, v115, v244, v245\n"
    "v_mfma_f32_16x16x32_bf16 v[40:43], v[136:139], v[20:23], 0\n"
    "v_max3_f32 v114, v114, v246, v247\n"
    "v_max3_f32 v115, v115, v248, v249\n"
    "v_mfma_f32_16x16x32_bf16 v[92:95], v[172:175], v[212:215], v[92:95]\n"
    "v_max_f32 v114, v114, v115\n"
    "v_cvt_pk_bf16_f32 v80, v80, v81\n"
    "v_mfma_f32_16x16x32_bf16 v[44:47], v[140:143], v[20:23], 0\n"
    "v_mul_f32 v114, 0xbfb8aa3b, v114\n"
    "v_cvt_pk_bf16_f32 v84, v84, v85\n"
    "v_mfma_f32_16x16x32_bf16 v[96:99], v[176:179], v[200:203], v[96:99]\n"
    "v_cvt_pk_bf16_f32 v81, v82, v83\n"
    "v_cvt_pk_bf16_f32 v85, v86, v87\n"
    "v_mfma_f32_16x16x16_bf16 v[32:35], v[150:151], v[116:117], v[32:35]\n"
    "ds_write_b64 v217, v[80:81] offset:160\n"
    "v_mfma_f32_16x16x16_bf16 v[36:39], v[154:155], v[116:117], v[36:39]\n"
    "v_cvt_pk_bf16_f32 v88, v88, v89\n"
    "ds_write_b64 v217, v[84:85] offset:3872\n"
    "v_mfma_f32_16x16x32_bf16 v[196:199], v[144:147], v[20:23], 0\n"
    "s_nop 2\n"
    "v_max3_f32 v118, v32, v33, v34\n"
    "v_cvt_pk_bf16_f32 v89, v90, v91\n"
    "v_mfma_f32_16x16x32_bf16 v[120:123], v[180:183], v[208:211], v[120:123]\n"
    "ds_write_b64 v217, v[88:89] offset:7584\n"
    "v_mfma_f32_16x16x32_bf16 v[124:127], v[180:183], v[212:215], v[124:127]\n"
    "v_max3_f32 v119, v35, v36, v37\n"
    "v_max3_f32 v118, v118, v38, v39\n"
    "v_mfma_f32_16x16x16_bf16 v[40:43], v[186:187], v[116:117], v[40:43]\n"
    "s_nop 2\n"
    "v_cvt_pk_bf16_f32 v120, v120, v121\n"
    "v_cvt_pk_bf16_f32 v121, v122, v123\n"
    "v_mfma_f32_16x16x16_bf16 v[44:47], v[190:191], v[116:117], v[44:47]\n"
    "ds_write_b64 v218, v[120:121] offset:7424\n"
    "v_mfma_f32_16x16x16_bf16 v[196:199], v[194:195], v[116:117], v[196:199]\n"
    "v_max3_f32 v119, v119, v40, v41\n"
    "v_max3_f32 v118, v118, v42, v43\n"
    "v_mfma_f32_16x16x32_bf16 v[156:159], v[128:131], v[24:27], 0\n"
    "s_nop 2\n"
    "v_max3_f32 v119, v119, v44, v45\n"
    "v_max3_f32 v118, v118, v46, v47\n"
    "v_mfma_f32_16x16x32_bf16 v[160:163], v[132:135], v[24:27], 0\n"
    "v_cvt_pk_bf16_f32 v92, v92, v93\n"
    "v_max3_f32 v119, v119, v196, v197\n"
    "v_mfma_f32_16x16x32_bf16 v[180:183], v[136:139], v[24:27], 0\n"
    "v_max_f32 v118, v118, v119\n"
    "v_cvt_pk_bf16_f32 v93, v94, v95\n"
    "v_mfma_f32_16x16x32_bf16 v[100:103], v[176:179], v[204:207], v[100:103]\n"
    "ds_write_b64 v217, v[92:93] offset:11296\n"
    "v_mfma_f32_16x16x32_bf16 v[104:107], v[176:179], v[208:211], v[104:107]\n"
    "v_mul_f32 v118, 0xbfb8aa3b, v118\n"
    "v_cvt_pk_bf16_f32 v96, v96, v97\n"
    "v_mfma_f32_16x16x32_bf16 v[108:111], v[176:179], v[212:215], v[108:111]\n"
    "v_cvt_pk_bf16_f32 v97, v98, v99\n"
    "v_cvt_pk_bf16_f32 v124, v124, v125\n"
    "v_mfma_f32_16x16x32_bf16 v[172:175], v[140:143], v[24:27], 0\n"
    "ds_write_b64 v217, v[96:97] offset:192\n"
    "v_mfma_f32_16x16x16_bf16 v[156:159], v[150:151], v[120:121], v[156:159]\n"
    "v_cvt_pk_bf16_f32 v125, v126, v127\n"
    "v_cvt_pk_bf16_f32 v100, v100, v101\n"
    "v_mfma_f32_16x16x16_bf16 v[160:163], v[154:155], v[120:121], v[160:163]\n"
    "ds_write_b64 v218, v[124:125] offset:11136\n"
    "v_mfma_f32_16x16x32_bf16 v[176:179], v[144:147], v[24:27], 0\n"
    "s_nop 2\n"
    "v_max3_f32 v122, v156, v157, v158\n"
    "v_cvt_pk_bf16_f32 v104, v104, v105\n"
    "v_mfma_f32_16x16x16_bf16 v[180:183], v[186:187], v[120:121], v[180:183]\n"
    "v_max3_f32 v123, v159, v160, v161\n"
    "v_max3_f32 v122, v122, v162, v163\n"
    "v_mfma_f32_16x16x16_bf16 v[172:175], v[190:191], v[120:121], v[172:175]\n"
    "v_cvt_pk_bf16_f32 v108, v108, v109\n"
    "v_cvt_pk_bf16_f32 v101, v102, v103\n"
    "v_mfma_f32_16x16x16_bf16 v[176:179], v[194:195], v[120:121], v[176:179]\n"
    "ds_write_b64 v217, v[100:101] offset:3904\n"
    "v_mfma_f32_16x16x32_bf16 v[80:83], v[128:131], v[28:31], 0\n"
    "v_max3_f32 v123, v123, v180, v181\n"
    "v_max3_f32 v122, v122, v182, v183\n"
    "v_mfma_f32_16x16x32_bf16 v[84:87], v[132:135], v[28:31], 0\n"
    "v_max3_f32 v123, v123, v172, v173\n"
    "v_max3_f32 v122, v122, v174, v175\n"
    "v_mfma_f32_16x16x32_bf16 v[88:91], v[136:139], v[28:31], 0\n"
    "v_max3_f32 v123, v123, v176, v177\n"
    "v_cvt_pk_bf16_f32 v105, v106, v107\n"
    "v_mfma_f32_16x16x32_bf16 v[92:95], v[140:143], v[28:31], 0\n"
    "ds_write_b64 v217, v[104:105] offset:7616\n"
    "v_mfma_f32_16x16x16_bf16 v[80:83], v[150:151], v[124:125], v[80:83]\n"
    "v_max_f32 v122, v122, v123\n"
    "v_cvt_pk_bf16_f32 v109, v110, v111\n"
    "v_mfma_f32_16x16x16_bf16 v[84:87], v[154:155], v[124:125], v[84:87]\n"
    "ds_write_b64 v217, v[108:109] offset:11328\n"
    "v_mfma_f32_16x16x32_bf16 v[96:99], v[144:147], v[28:31], 0\n"
    "v_mul_f32 v122, 0xbfb8aa3b, v122\n"
    "s_nop 1\n"
    "v_max3_f32 v126, v80, v81, v82\n"
    "v_mfma_f32_16x16x16_bf16 v[88:91], v[186:187], v[124:125], v[88:91]\n"
    "s_nop 0\n"
    "v_max3_f32 v127, v83, v84, v85\n"
    "v_max3_f32 v126, v126, v86, v87\n"
    "v_mfma_f32_16x16x16_bf16 v[92:95], v[190:191], v[124:125], v[92:95]\n"
    "v_mfma_f32_16x16x16_bf16 v[96:99], v[194:195], v[124:125], v[96:99]\n"
    "s_nop 2\n"
    "v_max3_f32 v127, v127, v88, v89\n"
    "v_max3_f32 v126, v126, v90, v91\n"
    "v_pk_fma_f32 v[232:233], v[232:233], s[86:87], v[114:115] op_sel_hi:[1,1,0]\n"
    "v_pk_fma_f32 v[234:235], v[234:235], s[86:87], v[114:115] op_sel_hi:[1,1,0]\n"
    "v_exp_f32 v232, v232\n"
    "v_pk_fma_f32 v[236:237], v[236:237], s[86:87], v[114:115] op_sel_hi:[1,1,0]\n"
    "v_exp_f32 v233, v233\n"
    "v_pk_fma_f32 v[238:239], v[238:239], s[86:87], v[114:115] op_sel_hi:[1,1,0]\n"
    "v_exp_f32 v234, v234\n"
    "v_pk_fma_f32 v[240:241], v[240:241], s[86:87], v[114:115] op_sel_hi:[1,1,0]\n"
    "v_exp_f32 v235, v235\n"
    "v_pk_fma_f32 v[242:243], v[242:243], s[86:87], v[114:115] op_sel_hi:[1,1,0]\n"
    "v_exp_f32 v236, v236\n"
    "v_pk_add_f32 v[250:251], v[232:233], v[234:235]\n"
    "v_exp_f32 v237, v237\n"
    "v_pk_fma_f32 v[244:245], v[244:245], s[86:87], v[114:115] op_sel_hi:[1,1,0]\n"
    "v_exp_f32 v238, v238\n"
    "v_pk_add_f32 v[250:251], v[250:251], v[236:237]\n"
    "v_exp_f32 v239, v239\n"
    "v_pk_fma_f32 v[246:247], v[246:247], s[86:87], v[114:115] op_sel_hi:[1,1,0]\n"
    "v_exp_f32 v240, v240\n"
    "v_pk_add_f32 v[250:251], v[250:251], v[238:239]\n"
    "v_exp_f32 v241, v241\n"
    "v_pk_fma_f32 v[248:249], v[248:249], s[86:87], v[114:115] op_sel_hi:[1,1,0]\n"
    "v_exp_f32 v242, v242\n"
    "v_pk_add_f32 v[250:251], v[250:251], v[240:241]\n"
    "v_exp_f32 v243, v243\n"
    "v_pk_fma_f32 v[32:33], v[32:33], s[86:87], v[118:119] op_sel_hi:[1,1,0]\n"
    "v_exp_f32 v244, v244\n"
    "v_pk_add_f32 v[250:251], v[250:251], v[242:243]\n"
    "v_exp_f32 v245, v245\n"
    "v_pk_fma_f32 v[34:35], v[34:35], s[86:87], v[118:119] op_sel_hi:[1,1,0]\n"
    "v_exp_f32 v246, v246\n"
    "v_pk_add_f32 v[250:251], v[250:251], v[244:245]\n"
    "v_exp_f32 v247, v247\n"
    "v_pk_fma_f32 v[36:37], v[36:37], s[86:87], v[118:119] op_sel_hi:[1,1,0]\n"
    "v_exp_f32 v248, v248\n"
    "v_pk_add_f32 v[250:251], v[250:251], v[246:247]\n"
    "v_exp_f32 v249, v249\n"
    "v_pk_fma_f32 v[38:39], v[38:39], s[86:87], v[118:119] op_sel_hi:[1,1,0]\n"
    "v_exp_f32 v32, v32\n"
    "v_pk_add_f32 v[250:251], v[250:251], v[248:249]\n"
    "v_exp_f32 v33, v33\n"
    "v_add_f32 v114, v250, v251\n"
    "v_exp_f32 v34, v34\n"
    "v_pk_fma_f32 v[40:41], v[40:41], s[86:87], v[118:119] op_sel_hi:[1,1,0]\n"
    "v_exp_f32 v35, v35\n"
    "v_pk_fma_f32 v[42:43], v[42:43], s[86:87], v[118:119] op_sel_hi:[1,1,0]\n"
    "v_exp_f32 v36, v36\n"
    "v_pk_add_f32 v[198:199], v[32:33], v[34:35]\n"
    "v_exp_f32 v37, v37\n"
    "v_pk_fma_f32 v[44:45], v[44:45], s[86:87], v[118:119] op_sel_hi:[1,1,0]\n"
    "v_exp_f32 v38, v38\n"
    "v_pk_add_f32 v[198:199], v[198:199], v[36:37]\n"
    "v_exp_f32 v39, v39\n"
    "v_pk_fma_f32 v[46:47], v[46:47], s[86:87], v[118:119] op_sel_hi:[1,1,0]\n"
    "v_rcp_f32 v114, v114\n"
    "v_pk_add_f32 v[198:199], v[198:199], v[38:39]\n"
    "v_exp_f32 v40, v40\n"
    "v_pk_mul_f32 v[232:233], v[232:233], v[114:115] op_sel_hi:[1,0]\n"
    "v_exp_f32 v41, v41\n"
    "v_pk_mul_f32 v[234:235], v[234:235], v[114:115] op_sel_hi:[1,0]\n"
    "v_exp_f32 v42, v42\n"
    "v_pk_add_f32 v[198:199], v[198:199], v[40:41]\n"
    "v_exp_f32 v43, v43\n"
    "v_cvt_pk_bf16_f32 v232, v232, v233\n"
    "v_exp_f32 v44, v44\n"
    "v_pk_mul_f32 v[236:237], v[236:237], v[114:115] op_sel_hi:[1,0]\n"
    "v_exp_f32 v45, v45\n"
    "v_pk_fma_f32 v[196:197], v[196:197], s[86:87], v[118:119] op_sel_hi:[1,1,0]\n"
    "v_exp_f32 v46, v46\n"
    "v_cvt_pk_bf16_f32 v233, v234, v235\n"
    "v_exp_f32 v47, v47\n"
    "ds_write_b64 v216, v[232:233] offset:0\n"
    "v_exp_f32 v196, v196\n"
    "v_pk_add_f32 v[198:199], v[198:199], v[42:43]\n"
    "v_exp_f32 v197, v197\n"
    "v_pk_mul_f32 v[238:239], v[238:239], v[114:115] op_sel_hi:[1,0]\n"
    "v_pk_mul_f32 v[240:241], v[240:241], v[114:115] op_sel_hi:[1,0]\n"
    "v_cvt_pk_bf16_f32 v234, v236, v237\n"
    "v_pk_mul_f32 v[242:243], v[242:243], v[114:115] op_sel_hi:[1,0]\n"
    "v_pk_mul_f32 v[244:245], v[244:245], v[114:115] op_sel_hi:[1,0]\n"
    "v_pk_mul_f32 v[246:247], v[246:247], v[114:115] op_sel_hi:[1,0]\n"
    "v_pk_mul_f32 v[248:249], v[248:249], v[114:115] op_sel_hi:[1,0]\n"
    "v_pk_add_f32 v[198:199], v[198:199], v[44:45]\n"
    "v_cvt_pk_bf16_f32 v235, v238, v239\n"
    "v_cvt_pk_bf16_f32 v236, v240, v241\n"
    "ds_write_b64 v216, v[234:235] offset:8\n"
    "v_pk_fma_f32 v[156:157], v[156:157], s[86:87], v[122:123] op_sel_hi:[1,1,0]\n"
    "v_pk_fma_f32 v[158:159], v[158:159], s[86:87], v[122:123] op_sel_hi:[1,1,0]\n"
    "v_exp_f32 v156, v156\n"
    "v_cvt_pk_bf16_f32 v237, v242, v243\n"
    "v_exp_f32 v157, v157\n"
    "ds_write_b64 v216, v[236:237] offset:16\n"
    "v_exp_f32 v158, v158\n"
    "v_cvt_pk_bf16_f32 v238, v244, v245\n"
    "v_exp_f32 v159, v159\n"
    "v_cvt_pk_bf16_f32 v239, v246, v247\n"
    "v_cvt_pk_bf16_f32 v240, v248, v249\n"
    "ds_write_b64 v216, v[238:239] offset:24\n"
    "v_mov_b32 v241, 0x3f80\n"
    "v_pk_add_f32 v[198:199], v[198:199], v[46:47]\n"
    "v_pk_fma_f32 v[160:161], v[160:161], s[86:87], v[122:123] op_sel_hi:[1,1,0]\n"
    "ds_write_b64 v216, v[240:241] offset:32\n"
    "v_exp_f32 v160, v160\n"
    "v_pk_add_f32 v[198:199], v[198:199], v[196:197]\n"
    "v_exp_f32 v161, v161\n"
    "v_pk_fma_f32 v[162:163], v[162:163], s[86:87], v[122:123] op_sel_hi:[1,1,0]\n"
    "v_add_f32 v118, v198, v199\n"
    "v_exp_f32 v162, v162\n"
    "v_pk_fma_f32 v[180:181], v[180:181], s[86:87], v[122:123] op_sel_hi:[1,1,0]\n"
    "v_exp_f32 v163, v163\n"
    "v_pk_add_f32 v[178:179], v[156:157], v[158:159]\n"
    "v_rcp_f32 v118, v118\n"
    "v_pk_fma_f32 v[182:183], v[182:183], s[86:87], v[122:123] op_sel_hi:[1,1,0]\n"
    "v_exp_f32 v180, v180\n"
    "v_pk_add_f32 v[178:179], v[178:179], v[160:161]\n"
    "v_exp_f32 v181, v181\n"
    "v_max3_f32 v127, v127, v92, v93\n"
    "v_exp_f32 v182, v182\n"
    "v_pk_fma_f32 v[172:173], v[172:173], s[86:87], v[122:123] op_sel_hi:[1,1,0]\n"
    "v_exp_f32 v183, v183\n"
    "v_pk_add_f32 v[178:179], v[178:179], v[162:163]\n"
    "v_exp_f32 v172, v172\n"
    "v_pk_mul_f32 v[32:33], v[32:33], v[118:119] op_sel_hi:[1,0]\n"
    "v_exp_f32 v173, v173\n"
    "v_max3_f32 v126, v126, v94, v95\n"
    "v_max3_f32 v127, v127, v96, v97\n"
    "v_pk_fma_f32 v[174:175], v[174:175], s[86:87], v[122:123] op_sel_hi:[1,1,0]\n"
    "v_pk_mul_f32 v[34:35], v[34:35], v[118:119] op_sel_hi:[1,0]\n"
    "v_exp_f32 v174, v174\n"
    "v_pk_add_f32 v[178:179], v[178:179], v[180:181]\n"
    "v_exp_f32 v175, v175\n"
    "v_cvt_pk_bf16_f32 v32, v32, v33\n"
    "v_pk_mul_f32 v[36:37], v[36:37], v[118:119] op_sel_hi:[1,0]\n"
    "v_max_f32_e64 v126, v126, v127\n"
    "v_pk_fma_f32 v[176:177], v[176:177], s[86:87], v[122:123] op_sel_hi:[1,1,0]\n"
    "v_cvt_pk_bf16_f32 v33, v34, v35\n"
    "v_exp_f32 v176, v176\n"
    "ds_write_b64 v216, v[32:33] offset:3712\n"
    "v_exp_f32 v177, v177\n"
    "v_pk_add_f32 v[178:179], v[178:179], v[182:183]\n"
    "v_pk_mul_f32 v[38:39], v[38:39], v[118:119] op_sel_hi:[1,0]\n"
    "v_pk_mul_f32 v[40:41], v[40:41], v[118:119] op_sel_hi:[1,0]\n"
    "v_cvt_pk_bf16_f32 v34, v36, v37\n"
    "v_mul_f32 v126, 0xbfb8aa3b, v126\n"
    "v_pk_mul_f32 v[42:43], v[42:43], v[118:119] op_sel_hi:[1,0]\n"
    "v_pk_mul_f32 v[44:45], v[44:45], v[118:119] op_sel_hi:[1,0]\n"
    "v_pk_mul_f32 v[46:47], v[46:47], v[118:119] op_sel_hi:[1,0]\n"
    "v_pk_mul_f32 v[196:197], v[196:197], v[118:119] op_sel_hi:[1,0]\n"
    "v_pk_add_f32 v[178:179], v[178:179], v[172:173]\n"
    "v_cvt_pk_bf16_f32 v35, v38, v39\n"
    "v_cvt_pk_bf16_f32 v36, v40, v41\n"
    "ds_write_b64 v216, v[34:35] offset:3720\n"
    "v_pk_fma_f32 v[80:81], v[80:81], s[86:87], v[126:127] op_sel_hi:[1,1,0]\n"
    "v_pk_fma_f32 v[82:83], v[82:83], s[86:87], v[126:127] op_sel_hi:[1,1,0]\n"
    "v_exp_f32 v80, v80\n"
    "v_cvt_pk_bf16_f32 v37, v42, v43\n"
    "v_exp_f32 v81, v81\n"
    "ds_write_b64 v216, v[36:37] offset:3728\n"
    "v_exp_f32 v82, v82\n"
    "v_cvt_pk_bf16_f32 v38, v44, v45\n"
    "v_exp_f32 v83, v83\n"
    "v_cvt_pk_bf16_f32 v39, v46, v47\n"
    "v_cvt_pk_bf16_f32 v40, v196, v197\n"
    "ds_write_b64 v216, v[38:39] offset:3736\n"
    "v_mov_b32 v41, 0x3f80\n"
    "v_pk_add_f32 v[178:179], v[178:179], v[174:175]\n"
    "v_pk_fma_f32 v[84:85], v[84:85], s[86:87], v[126:127] op_sel_hi:[1,1,0]\n"
    "ds_write_b64 v216, v[40:41] offset:3744\n"
    "v_exp_f32 v84, v84\n"
    "v_pk_add_f32 v[178:179], v[178:179], v[176:177]\n"
    "v_exp_f32 v85, v85\n"
    "v_pk_fma_f32 v[86:87], v[86:87], s[86:87], v[126:127] op_sel_hi:[1,1,0]\n"
    "v_add_f32 v122, v178, v179\n"
    "v_exp_f32 v86, v86\n"
    "v_pk_fma_f32 v[88:89], v[88:89], s[86:87], v[126:127] op_sel_hi:[1,1,0]\n"
    "v_exp_f32 v87, v87\n"
    "v_pk_add_f32 v[98:99], v[80:81], v[82:83]\n"
    "v_rcp_f32 v122, v122\n"
    "v_pk_fma_f32 v[90:91], v[90:91], s[86:87], v[126:127] op_sel_hi:[1,1,0]\n"
    "v_exp_f32 v88, v88\n"
    "v_pk_add_f32 v[98:99], v[98:99], v[84:85]\n"
    "v_exp_f32 v89, v89\n"
    "v_pk_fma_f32 v[92:93], v[92:93], s[86:87], v[126:127] op_sel_hi:[1,1,0]\n"
    "v_exp_f32 v90, v90\n"
    "v_pk_add_f32 v[98:99], v[98:99], v[86:87]\n"
    "v_exp_f32 v91, v91\n"
    "v_pk_mul_f32 v[156:157], v[156:157], v[122:123] op_sel_hi:[1,0]\n"
    "v_exp_f32 v92, v92\n"
    "v_pk_fma_f32 v[94:95], v[94:95], s[86:87], v[126:127] op_sel_hi:[1,1,0]\n"
    "v_exp_f32 v93, v93\n"
    "v_pk_mul_f32 v[158:159], v[158:159], v[122:123] op_sel_hi:[1,0]\n"
    "v_exp_f32 v94, v94\n"
    "v_pk_add_f32 v[98:99], v[98:99], v[88:89]\n"
    "v_exp_f32 v95, v95\n"
    "v_cvt_pk_bf16_f32 v156, v156, v157\n"
    "v_pk_mul_f32 v[160:161], v[160:161], v[122:123] op_sel_hi:[1,0]\n"
    "v_pk_fma_f32 v[96:97], v[96:97], s[86:87], v[126:127] op_sel_hi:[1,1,0]\n"
    "v_cvt_pk_bf16_f32 v157, v158, v159\n"
    "v_exp_f32 v96, v96\n"
    "ds_write_b64 v216, v[156:157] offset:7424\n"
    "v_exp_f32 v97, v97\n"
    "v_pk_add_f32 v[98:99], v[98:99], v[90:91]\n"
    "v_pk_mul_f32 v[162:163], v[162:163], v[122:123] op_sel_hi:[1,0]\n"
    "v_pk_mul_f32 v[180:181], v[180:181], v[122:123] op_sel_hi:[1,0]\n"
    "v_cvt_pk_bf16_f32 v158, v160, v161\n"
    "v_pk_mul_f32 v[182:183], v[182:183], v[122:123] op_sel_hi:[1,0]\n"
    "v_pk_mul_f32 v[172:173], v[172:173], v[122:123] op_sel_hi:[1,0]\n"
    "v_pk_mul_f32 v[174:175], v[174:175], v[122:123] op_sel_hi:[1,0]\n"
    "v_pk_mul_f32 v[176:177], v[176:177], v[122:123] op_sel_hi:[1,0]\n"
    "v_pk_add_f32 v[98:99], v[98:99], v[92:93]\n"
    "v_cvt_pk_bf16_f32 v159, v162, v163\n"
    "v_cvt_pk_bf16_f32 v160, v180, v181\n"
    "ds_write_b64 v216, v[158:159] offset:7432\n"
    "v_cvt_pk_bf16_f32 v161, v182, v183\n"
    "v_cvt_pk_bf16_f32 v162, v172, v173\n"
    "v_cvt_pk_bf16_f32 v163, v174, v175\n"
    "ds_write_b64 v216, v[160:161] offset:7440\n"
    "v_cvt_pk_bf16_f32 v180, v176, v177\n"
    "v_mov_b32 v181, 0x3f80\n"
    "v_pk_add_f32 v[98:99], v[98:99], v[94:95]\n"
    "ds_write_b64 v216, v[162:163] offset:7448\n"
    "v_pk_add_f32 v[98:99], v[98:99], v[96:97]\n"
    "v_add_f32 v126, v98, v99\n"
    "ds_write_b64 v216, v[180:181] offset:7456\n"
    "v_rcp_f32_e64 v126, v126\n"
    "s_nop 0\n"
    "v_pk_mul_f32 v[80:81], v[80:81], v[126:127] op_sel_hi:[1,0]\n"
    "v_pk_mul_f32 v[82:83], v[82:83], v[126:127] op_sel_hi:[1,0]\n"
    "v_cvt_pk_bf16_f32 v80, v80, v81\n"
    "v_pk_mul_f32 v[84:85], v[84:85], v[126:127] op_sel_hi:[1,0]\n"
    "v_cvt_pk_bf16_f32 v81, v82, v83\n"
    "v_pk_mul_f32 v[86:87], v[86:87], v[126:127] op_sel_hi:[1,0]\n"
    "ds_write_b64 v216, v[80:81] offset:11136\n"
    "v_pk_mul_f32 v[88:89], v[88:89], v[126:127] op_sel_hi:[1,0]\n"
    "v_cvt_pk_bf16_f32 v82, v84, v85\n"
    "v_pk_mul_f32 v[90:91], v[90:91], v[126:127] op_sel_hi:[1,0]\n"
    "v_pk_mul_f32 v[92:93], v[92:93], v[126:127] op_sel_hi:[1,0]\n"
    "v_pk_mul_f32 v[94:95], v[94:95], v[126:127] op_sel_hi:[1,0]\n"
    "v_pk_mul_f32 v[96:97], v[96:97], v[126:127] op_sel_hi:[1,0]\n"
    "v_cvt_pk_bf16_f32 v83, v86, v87\n"
    "v_cvt_pk_bf16_f32 v84, v88, v89\n"
    "ds_write_b64 v216, v[82:83] offset:11144\n"
    "v_cvt_pk_bf16_f32 v85, v90, v91\n"
    "v_cvt_pk_bf16_f32 v86, v92, v93\n"
    "v_cvt_pk_bf16_f32 v87, v94, v95\n"
    "ds_write_b64 v216, v[84:85] offset:11152\n"
    "v_cvt_pk_bf16_f32 v88, v96, v97\n"
    "v_mov_b32 v89, 0x3f80\n"
    "ds_write_b64 v216, v[86:87] offset:11160\n"
    "ds_write_b64 v216, v[88:89] offset:11168\n"
    "s_waitcnt lgkmcnt(0)\n"
    "s_barrier\n"
    "ds_read_b64_tr_b16 v[16:17], v222 offset:0\n"
    "ds_read_b64_tr_b16 v[18:19], v222 offset:232\n"
    "ds_read_b64_tr_b16 v[20:21], v221 offset:0\n"
    "ds_read_b64_tr_b16 v[22:23], v221 offset:232\n"
    "ds_read_b64_tr_b16 v[24:25], v231 offset:0\n"
    "ds_read_b64_tr_b16 v[26:27], v231 offset:232\n"
    "ds_read_b64_tr_b16 v[28:29], v221 offset:32\n"
    "ds_read_b64_tr_b16 v[30:31], v221 offset:264\n"
    "ds_read_b64_tr_b16 v[32:33], v221 offset:64\n"
    "ds_read_b64_tr_b16 v[34:35], v221 offset:296\n"
    "ds_read_b64_tr_b16 v[80:81], v222 offset:7424\n"
    "ds_read_b64_tr_b16 v[82:83], v222 offset:7656\n"
    "ds_read_b64_tr_b16 v[84:85], v221 offset:7424\n"
    "ds_read_b64_tr_b16 v[86:87], v221 offset:7656\n"
    "ds_read_b64_tr_b16 v[88:89], v231 offset:7424\n"
    "ds_read_b64_tr_b16 v[90:91], v231 offset:7656\n"
    "ds_read_b64_tr_b16 v[92:93], v221 offset:7456\n"
    "ds_read_b64_tr_b16 v[94:95], v221 offset:7688\n"
    "ds_read_b64_tr_b16 v[232:233], v222 offset:14848\n"
    "ds_read_b64_tr_b16 v[234:235], v222 offset:15080\n"
    "ds_read_b64_tr_b16 v[236:237], v221 offset:14848\n"
    "ds_read_b64_tr_b16 v[238:239], v221 offset:15080\n"
    "ds_read_b64_tr_b16 v[240:241], v231 offset:14848\n"
    "ds_read_b64_tr_b16 v[242:243], v231 offset:15080\n"
    "ds_read_b64_tr_b16 v[96:97], v221 offset:7488\n"
    "s_waitcnt lgkmcnt(15)\n"
    "v_mfma_f32_16x16x32_bf16 v[36:39], v[20:23], v[16:19], 0\n"
    "ds_read_b64_tr_b16 v[98:99], v221 offset:7720\n"
    "ds_read_b64_tr_b16 v[244:245], v221 offset:14880\n"
    "v_mfma_f32_16x16x32_bf16 v[40:43], v[20:23], v[24:27], 0\n"
    "ds_read_b64_tr_b16 v[246:247], v221 offset:15112\n"
    "ds_read_b64_tr_b16 v[20:21], v221 offset:22272\n"
    "v_mfma_f32_16x16x32_bf16 v[44:47], v[28:31], v[16:19], 0\n"
    "ds_read_b64_tr_b16 v[22:23], v221 offset:22504\n"
    "ds_read_b64_tr_b16 v[248:249], v221 offset:14912\n"
    "v_mfma_f32_16x16x32_bf16 v[104:107], v[32:35], v[16:19], 0\n"
    "ds_read_b64_tr_b16 v[16:17], v222 offset:22272\n"
    "ds_read_b64_tr_b16 v[18:19], v222 offset:22504\n"
    "v_mfma_f32_16x16x32_bf16 v[100:103], v[28:31], v[24:27], 0\n"
    "ds_read_b64_tr_b16 v[250:251], v221 offset:15144\n"
    "ds_read_b64_tr_b16 v[28:29], v221 offset:22304\n"
    "v_mfma_f32_16x16x32_bf16 v[108:111], v[32:35], v[24:27], 0\n"
    "ds_read_b64_tr_b16 v[24:25], v231 offset:22272\n"
    "ds_read_b64_tr_b16 v[26:27], v231 offset:22504\n"
    "s_waitcnt lgkmcnt(15)\n"
    "v_mfma_f32_16x16x32_bf16 v[36:39], v[84:87], v[80:83], v[36:39]\n"
    "ds_read_b64_tr_b16 v[30:31], v221 offset:22536\n"
    "ds_read_b64_tr_b16 v[32:33], v221 offset:22336\n"
    "v_mfma_f32_16x16x32_bf16 v[40:43], v[84:87], v[88:91], v[40:43]\n"
    "ds_read_b64_tr_b16 v[34:35], v221 offset:22568\n"
    "v_mov_b32 v157, s34\n"
    "v_mfma_f32_16x16x32_bf16 v[44:47], v[92:95], v[80:83], v[44:47]\n"
    "v_mov_b32 v159, s34\n"
    "v_mov_b32 v161, s34\n"
    "v_mfma_f32_16x16x32_bf16 v[36:39], v[236:239], v[232:235], v[36:39]\n"
    "v_mov_b32 v163, s34\n"
    "v_mov_b32 v173, s34\n"
    "v_mfma_f32_16x16x32_bf16 v[100:103], v[92:95], v[88:91], v[100:103]\n"
    "v_mov_b32 v175, s34\n"
    "v_mov_b32 v177, s34\n"
    "s_waitcnt lgkmcnt(15)\n"
    "v_mfma_f32_16x16x32_bf16 v[40:43], v[236:239], v[240:243], v[40:43]\n"
    "v_mov_b32 v179, s34\n"
    "v_mov_b32 v181, s34\n"
    "s_waitcnt lgkmcnt(14)\n"
    "v_mfma_f32_16x16x32_bf16 v[104:107], v[96:99], v[80:83], v[104:107]\n"
    "v_mov_b32 v183, s34\n"
    "v_mov_b32 v197, s34\n"
    "s_waitcnt lgkmcnt(12)\n"
    "v_mfma_f32_16x16x32_bf16 v[44:47], v[244:247], v[232:235], v[44:47]\n"
    "v_mov_b32 v199, s34\n"
    "v_mfma_f32_16x16x32_bf16 v[108:111], v[96:99], v[88:91], v[108:111]\n"
    "s_waitcnt lgkmcnt(7)\n"
    "v_mfma_f32_16x16x32_bf16 v[36:39], v[20:23], v[16:19], v[36:39]\n"
    "v_mfma_f32_16x16x32_bf16 v[100:103], v[244:247], v[240:243], v[100:103]\n"
    "s_waitcnt lgkmcnt(3)\n"
    "v_mfma_f32_16x16x32_bf16 v[40:43], v[20:23], v[24:27], v[40:43]\n"
    "s_nop 4\n"
    "v_cvt_pk_bf16_f32 v156, v36, v37\n"
    "v_mfma_f32_16x16x32_bf16 v[104:107], v[248:251], v[232:235], v[104:107]\n"
    "v_cvt_pk_bf16_f32 v158, v38, v39\n"
    "buffer_store_dwordx4 v[156:159], v223, s[36:39], s88 offen offset:0 sc1\n"
    "s_waitcnt lgkmcnt(2)\n"
    "v_mfma_f32_16x16x32_bf16 v[44:47], v[28:31], v[16:19], v[44:47]\n"
    "v_cvt_pk_bf16_f32 v160, v40, v41\n"
    "v_cvt_pk_bf16_f32 v162, v42, v43\n"
    "v_mfma_f32_16x16x32_bf16 v[108:111], v[248:251], v[240:243], v[108:111]\n"
    "buffer_store_dwordx4 v[160:163], v13, s[36:39], s88 offen offset:0 sc1\n"
    "v_mfma_f32_16x16x32_bf16 v[100:103], v[28:31], v[24:27], v[100:103]\n"
    "s_nop 2\n"
    "v_cvt_pk_bf16_f32 v172, v44, v45\n"
    "s_waitcnt lgkmcnt(0)\n"
    "v_mfma_f32_16x16x32_bf16 v[104:107], v[32:35], v[16:19], v[104:107]\n"
    "v_cvt_pk_bf16_f32 v174, v46, v47\n"
    "buffer_store_dwordx4 v[172:175], v224, s[36:39], s88 offen offset:1024 sc1\n"
    "v_mfma_f32_16x16x32_bf16 v[108:111], v[32:35], v[24:27], v[108:111]\n"
    "v_cvt_pk_bf16_f32 v176, v100, v101\n"
    "v_cvt_pk_bf16_f32 v178, v102, v103\n"
    "buffer_store_dwordx4 v[176:179], v14, s[36:39], s88 offen offset:256 sc1\n"
    "s_nop 1\n"
    "v_cvt_pk_bf16_f32 v180, v104, v105\n"
    "v_cvt_pk_bf16_f32 v182, v106, v107\n"
    "buffer_store_dwordx4 v[180:183], v225, s[36:39], s88 offen offset:2048 sc1\n"
    "v_cvt_pk_bf16_f32 v196, v108, v109\n"
    "v_cvt_pk_bf16_f32 v198, v110, v111\n"
    "buffer_store_dwordx4 v[196:199], v15, s[36:39], s88 offen offset:512 sc1\n"
    "s_barrier\n"
    "s_setprio 0\n"
    "v_mfma_f32_16x16x32_bf16 v[48:51], v[164:167], v[200:203], v[48:51]\n"
    "v_mov_b32 v47, 0\n"
    "v_mov_b32 v109, 0\n"
    "v_mfma_f32_16x16x32_bf16 v[64:67], v[168:171], v[200:203], v[64:67]\n"
    "v_mfma_f32_16x16x32_bf16 v[52:55], v[164:167], v[204:207], v[52:55]\n"
    "s_nop 3\n"
    "v_cvt_pk_bf16_f32 v48, v48, v49\n"
    "v_mfma_f32_16x16x32_bf16 v[68:71], v[168:171], v[204:207], v[68:71]\n"
    "v_cvt_pk_bf16_f32 v49, v50, v51\n"
    "v_cvt_pk_bf16_f32 v50, v64, v65\n"
    "v_mfma_f32_16x16x32_bf16 v[56:59], v[164:167], v[208:211], v[56:59]\n"
    "v_cvt_pk_bf16_f32 v51, v66, v67\n"
    "v_cvt_pk_bf16_f32 v52, v52, v53\n"
    "v_mfma_f32_16x16x32_bf16 v[72:75], v[168:171], v[208:211], v[72:75]\n"
    "v_cvt_pk_bf16_f32 v53, v54, v55\n"
    "s_nop 0\n"
    "v_cvt_pk_bf16_f32 v54, v68, v69\n"
    "v_mfma_f32_16x16x32_bf16 v[232:235], v[128:131], v[48:51], 0\n"
    "v_cvt_pk_bf16_f32 v55, v70, v71\n"
    "v_cvt_pk_bf16_f32 v56, v56, v57\n"
    "v_mfma_f32_16x16x32_bf16 v[236:239], v[132:135], v[48:51], 0\n"
    "v_cvt_pk_bf16_f32 v57, v58, v59\n"
    "v_cvt_pk_bf16_f32 v58, v72, v73\n"
    "v_mfma_f32_16x16x32_bf16 v[240:243], v[136:139], v[48:51], 0\n"
    "v_cvt_pk_bf16_f32 v59, v74, v75\n"
    "v_mfma_f32_16x16x32_bf16 v[244:247], v[140:143], v[48:51], 0\n"
    "v_mfma_f32_16x16x16_bf16 v[232:235], v[148:149], v[112:113], v[232:235]\n"
    "v_mfma_f32_16x16x16_bf16 v[236:239], v[152:153], v[112:113], v[236:239]\n"
    "v_mfma_f32_16x16x32_bf16 v[248:251], v[144:147], v[48:51], 0\n"
    "s_nop 5\n"
    "v_max3_f32 v114, v232, v233, v234\n"
    "v_mfma_f32_16x16x16_bf16 v[240:243], v[184:185], v[112:113], v[240:243]\n"
    "v_max3_f32 v115, v235, v236, v237\n"
    "v_mfma_f32_16x16x16_bf16 v[244:247], v[188:189], v[112:113], v[244:247]\n"
    "v_max3_f32 v114, v114, v238, v239\n"
    "v_mfma_f32_16x16x16_bf16 v[248:251], v[192:193], v[112:113], v[248:251]\n"
    "s_nop 3\n"
    "v_max3_f32 v115, v115, v240, v241\n"
    "v_mfma_f32_16x16x32_bf16 v[16:19], v[128:131], v[52:55], 0\n"
    "v_max3_f32 v114, v114, v242, v243\n"
    "v_max3_f32 v115, v115, v244, v245\n"
    "v_mfma_f32_16x16x32_bf16 v[20:23], v[132:135], v[52:55], 0\n"
    "v_max3_f32 v114, v114, v246, v247\n"
    "v_max3_f32 v115, v115, v248, v249\n"
    "v_mfma_f32_16x16x32_bf16 v[24:27], v[136:139], v[52:55], 0\n"
    "v_max_f32 v114, v114, v115\n"
    "v_mul_f32 v114, 0xbfb8aa3b, v114\n"
    "v_mfma_f32_16x16x32_bf16 v[28:31], v[140:143], v[52:55], 0\n"
    "v_mfma_f32_16x16x16_bf16 v[16:19], v[148:149], v[116:117], v[16:19]\n"
    "v_mfma_f32_16x16x16_bf16 v[20:23], v[152:153], v[116:117], v[20:23]\n"
    "v_mfma_f32_16x16x32_bf16 v[32:35], v[144:147], v[52:55], 0\n"
    "s_nop 5\n"
    "v_max3_f32 v118, v16, v17, v18\n"
    "v_mfma_f32_16x16x16_bf16 v[24:27], v[184:185], v[116:117], v[24:27]\n"
    "v_max3_f32 v119, v19, v20, v21\n"
    "v_mfma_f32_16x16x16_bf16 v[28:31], v[188:189], v[116:117], v[28:31]\n"
    "v_max3_f32 v118, v118, v22, v23\n"
    "v_mfma_f32_16x16x16_bf16 v[32:35], v[192:193], v[116:117], v[32:35]\n"
    "s_nop 3\n"
    "v_max3_f32 v119, v119, v24, v25\n"
    "v_mfma_f32_16x16x32_bf16 v[80:83], v[128:131], v[56:59], 0\n"
    "v_max3_f32 v118, v118, v26, v27\n"
    "v_max3_f32 v119, v119, v28, v29\n"
    "v_mfma_f32_16x16x32_bf16 v[84:87], v[132:135], v[56:59], 0\n"
    "v_max3_f32 v118, v118, v30, v31\n"
    "v_max3_f32 v119, v119, v32, v33\n"
    "v_mfma_f32_16x16x32_bf16 v[60:63], v[164:167], v[212:215], v[60:63]\n"
    "v_max_f32 v118, v118, v119\n"
    "v_mul_f32 v118, 0xbfb8aa3b, v118\n"
    "v_mfma_f32_16x16x32_bf16 v[88:91], v[136:139], v[56:59], 0\n"
    "v_mfma_f32_16x16x32_bf16 v[76:79], v[168:171], v[212:215], v[76:79]\n"
    "s_nop 3\n"
    "v_cvt_pk_bf16_f32 v60, v60, v61\n"
    "v_cvt_pk_bf16_f32 v61, v62, v63\n"
    "v_mfma_f32_16x16x32_bf16 v[92:95], v[140:143], v[56:59], 0\n"
    "v_mfma_f32_16x16x16_bf16 v[80:83], v[148:149], v[120:121], v[80:83]\n"
    "v_cvt_pk_bf16_f32 v62, v76, v77\n"
    "v_cvt_pk_bf16_f32 v63, v78, v79\n"
    "v_mfma_f32_16x16x16_bf16 v[84:87], v[152:153], v[120:121], v[84:87]\n"
    "v_mfma_f32_16x16x32_bf16 v[96:99], v[144:147], v[56:59], 0\n"
    "s_nop 3\n"
    "v_max3_f32 v122, v80, v81, v82\n"
    "v_mfma_f32_16x16x16_bf16 v[88:91], v[184:185], v[120:121], v[88:91]\n"
    "s_nop 0\n"
    "v_max3_f32 v123, v83, v84, v85\n"
    "v_mfma_f32_16x16x16_bf16 v[92:95], v[188:189], v[120:121], v[92:95]\n"
    "v_max3_f32 v122, v122, v86, v87\n"
    "v_mfma_f32_16x16x16_bf16 v[96:99], v[192:193], v[120:121], v[96:99]\n"
    "s_nop 2\n"
    "v_max3_f32 v123, v123, v88, v89\n"
    "v_mfma_f32_16x16x32_bf16 v[164:167], v[128:131], v[60:63], 0\n"
    "v_max3_f32 v122, v122, v90, v91\n"
    "v_max3_f32 v123, v123, v92, v93\n"
    "v_mfma_f32_16x16x32_bf16 v[168:171], v[132:135], v[60:63], 0\n"
    "v_max3_f32 v122, v122, v94, v95\n"
    "v_max3_f32 v123, v123, v96, v97\n"
    "v_mfma_f32_16x16x32_bf16 v[172:175], v[136:139], v[60:63], 0\n"
    "v_max_f32 v122, v122, v123\n"
    "v_mul_f32 v122, 0xbfb8aa3b, v122\n"
    "v_mfma_f32_16x16x32_bf16 v[176:179], v[140:143], v[60:63], 0\n"
    "v_mfma_f32_16x16x16_bf16 v[164:167], v[148:149], v[124:125], v[164:167]\n"
    "v_mfma_f32_16x16x16_bf16 v[168:171], v[152:153], v[124:125], v[168:171]\n"
    "v_mfma_f32_16x16x32_bf16 v[180:183], v[144:147], v[60:63], 0\n"
    "s_nop 5\n"
    "v_max3_f32 v126, v164, v165, v166\n"
    "v_mfma_f32_16x16x16_bf16 v[172:175], v[184:185], v[124:125], v[172:175]\n"
    "v_max3_f32 v127, v167, v168, v169\n"
    "v_mfma_f32_16x16x16_bf16 v[176:179], v[188:189], v[124:125], v[176:179]\n"
    "v_max3_f32 v126, v126, v170, v171\n"
    "v_mfma_f32_16x16x16_bf16 v[180:183], v[192:193], v[124:125], v[180:183]\n"
    "v_mov_b32 v193, 0\n"
    "s_nop 2\n"
    "v_max3_f32 v127, v127, v172, v173\n"
    "v_max3_f32 v126, v126, v174, v175\n"
    "v_pk_fma_f32 v[232:233], v[232:233], s[86:87], v[114:115] op_sel_hi:[1,1,0]\n"
    "v_pk_fma_f32 v[234:235], v[234:235], s[86:87], v[114:115] op_sel_hi:[1,1,0]\n"
    "v_exp_f32 v232, v232\n"
    "v_pk_fma_f32 v[236:237], v[236:237], s[86:87], v[114:115] op_sel_hi:[1,1,0]\n"
    "v_exp_f32 v233, v233\n"
    "v_pk_fma_f32 v[238:239], v[238:239], s[86:87], v[114:115] op_sel_hi:[1,1,0]\n"
    "v_exp_f32 v234, v234\n"
    "v_pk_fma_f32 v[240:241], v[240:241], s[86:87], v[114:115] op_sel_hi:[1,1,0]\n"
    "v_exp_f32 v235, v235\n"
    "v_pk_fma_f32 v[242:243], v[242:243], s[86:87], v[114:115] op_sel_hi:[1,1,0]\n"
    "v_exp_f32 v236, v236\n"
    "v_pk_add_f32 v[250:251], v[232:233], v[234:235]\n"
    "v_exp_f32 v237, v237\n"
    "v_pk_fma_f32 v[244:245], v[244:245], s[86:87], v[114:115] op_sel_hi:[1,1,0]\n"
    "v_exp_f32 v238, v238\n"
    "v_pk_add_f32 v[250:251], v[250:251], v[236:237]\n"
    "v_exp_f32 v239, v239\n"
    "v_pk_fma_f32 v[246:247], v[246:247], s[86:87], v[114:115] op_sel_hi:[1,1,0]\n"
    "v_exp_f32 v240, v240\n"
    "v_pk_add_f32 v[250:251], v[250:251], v[238:239]\n"
    "v_exp_f32 v241, v241\n"
    "v_pk_fma_f32 v[248:249], v[248:249], s[86:87], v[114:115] op_sel_hi:[1,1,0]\n"
    "v_exp_f32 v242, v242\n"
    "v_pk_add_f32 v[250:251], v[250:251], v[240:241]\n"
    "v_exp_f32 v243, v243\n"
    "v_pk_fma_f32 v[16:17], v[16:17], s[86:87], v[118:119] op_sel_hi:[1,1,0]\n"
    "v_exp_f32 v244, v244\n"
    "v_pk_add_f32 v[250:251], v[250:251], v[242:243]\n"
    "v_exp_f32 v245, v245\n"
    "v_pk_fma_f32 v[18:19], v[18:19], s[86:87], v[118:119] op_sel_hi:[1,1,0]\n"
    "v_exp_f32 v246, v246\n"
    "v_pk_add_f32 v[250:251], v[250:251], v[244:245]\n"
    "v_exp_f32 v247, v247\n"
    "v_pk_fma_f32 v[20:21], v[20:21], s[86:87], v[118:119] op_sel_hi:[1,1,0]\n"
    "v_exp_f32 v248, v248\n"
    "v_pk_add_f32 v[250:251], v[250:251], v[246:247]\n"
    "v_exp_f32 v249, v249\n"
    "v_pk_fma_f32 v[22:23], v[22:23], s[86:87], v[118:119] op_sel_hi:[1,1,0]\n"
    "v_exp_f32 v16, v16\n"
    "v_pk_add_f32 v[250:251], v[250:251], v[248:249]\n"
    "v_exp_f32 v17, v17\n"
    "v_add_f32 v114, v250, v251\n"
    "v_exp_f32 v18, v18\n"
    "v_pk_fma_f32 v[24:25], v[24:25], s[86:87], v[118:119] op_sel_hi:[1,1,0]\n"
    "v_exp_f32 v19, v19\n"
    "v_pk_fma_f32 v[26:27], v[26:27], s[86:87], v[118:119] op_sel_hi:[1,1,0]\n"
    "v_rcp_f32 v114, v114\n"
    "v_pk_add_f32 v[34:35], v[16:17], v[18:19]\n"
    "v_exp_f32 v20, v20\n"
    "v_pk_mul_f32 v[232:233], v[232:233], v[114:115] op_sel_hi:[1,0]\n"
    "v_exp_f32 v21, v21\n"
    "v_pk_mul_f32 v[234:235], v[234:235], v[114:115] op_sel_hi:[1,0]\n"
    "v_exp_f32 v22, v22\n"
    "v_pk_add_f32 v[34:35], v[34:35], v[20:21]\n"
    "v_exp_f32 v23, v23\n"
    "v_cvt_pk_bf16_f32 v232, v232, v233\n"
    "v_exp_f32 v24, v24\n"
    "v_pk_mul_f32 v[236:237], v[236:237], v[114:115] op_sel_hi:[1,0]\n"
    "v_exp_f32 v25, v25\n"
    "v_pk_fma_f32 v[28:29], v[28:29], s[86:87], v[118:119] op_sel_hi:[1,1,0]\n"
    "v_exp_f32 v26, v26\n"
    "v_cvt_pk_bf16_f32 v233, v234, v235\n"
    "v_exp_f32 v27, v27\n"
    "v_pk_mul_f32 v[238:239], v[238:239], v[114:115] op_sel_hi:[1,0]\n"
    "v_exp_f32 v28, v28\n"
    "v_pk_mul_f32 v[240:241], v[240:241], v[114:115] op_sel_hi:[1,0]\n"
    "v_exp_f32 v29, v29\n"
    "v_pk_add_f32 v[34:35], v[34:35], v[22:23]\n"
    "v_cvt_pk_bf16_f32 v234, v236, v237\n"
    "v_pk_mul_f32 v[242:243], v[242:243], v[114:115] op_sel_hi:[1,0]\n"
    "v_pk_mul_f32 v[244:245], v[244:245], v[114:115] op_sel_hi:[1,0]\n"
    "v_pk_mul_f32 v[246:247], v[246:247], v[114:115] op_sel_hi:[1,0]\n"
    "v_pk_mul_f32 v[248:249], v[248:249], v[114:115] op_sel_hi:[1,0]\n"
    "v_pk_fma_f32 v[30:31], v[30:31], s[86:87], v[118:119] op_sel_hi:[1,1,0]\n"
    "v_cvt_pk_bf16_f32 v235, v238, v239\n"
    "v_exp_f32 v30, v30\n"
    "v_cvt_pk_bf16_f32 v236, v240, v241\n"
    "v_exp_f32 v31, v31\n"
    "v_pk_add_f32 v[34:35], v[34:35], v[24:25]\n"
    "v_cvt_pk_bf16_f32 v237, v242, v243\n"
    "v_cvt_pk_bf16_f32 v238, v244, v245\n"
    "v_cvt_pk_bf16_f32 v239, v246, v247\n"
    "v_cvt_pk_bf16_f32 v240, v248, v249\n"
    "v_mov_b32_e64 v241, 0\n"
    "v_pk_fma_f32 v[32:33], v[32:33], s[86:87], v[118:119] op_sel_hi:[1,1,0]\n"
    "v_pk_add_f32 v[34:35], v[34:35], v[26:27]\n"
    "v_exp_f32 v32, v32\n"
    "v_pk_add_f32 v[34:35], v[34:35], v[28:29]\n"
    "v_exp_f32 v33, v33\n"
    "v_pk_fma_f32 v[80:81], v[80:81], s[86:87], v[122:123] op_sel_hi:[1,1,0]\n"
    "v_pk_fma_f32 v[82:83], v[82:83], s[86:87], v[122:123] op_sel_hi:[1,1,0]\n"
    "v_exp_f32 v80, v80\n"
    "v_pk_add_f32 v[34:35], v[34:35], v[30:31]\n"
    "v_exp_f32 v81, v81\n"
    "v_pk_fma_f32 v[84:85], v[84:85], s[86:87], v[122:123] op_sel_hi:[1,1,0]\n"
    "v_exp_f32 v82, v82\n"
    "v_pk_add_f32 v[34:35], v[34:35], v[32:33]\n"
    "v_exp_f32 v83, v83\n"
    "v_pk_fma_f32 v[86:87], v[86:87], s[86:87], v[122:123] op_sel_hi:[1,1,0]\n"
    "v_exp_f32 v84, v84\n"
    "v_add_f32 v118, v34, v35\n"
    "v_exp_f32 v85, v85\n"
    "v_pk_fma_f32 v[88:89], v[88:89], s[86:87], v[122:123] op_sel_hi:[1,1,0]\n"
    "v_exp_f32 v86, v86\n"
    "v_pk_add_f32 v[98:99], v[80:81], v[82:83]\n"
    "v_exp_f32 v87, v87\n"
    "v_pk_fma_f32 v[90:91], v[90:91], s[86:87], v[122:123] op_sel_hi:[1,1,0]\n"
    "v_rcp_f32 v118, v118\n"
    "v_pk_add_f32 v[98:99], v[98:99], v[84:85]\n"
    "v_exp_f32 v88, v88\n"
    "v_max3_f32 v127, v127, v176, v177\n"
    "v_exp_f32 v89, v89\n"
    "v_pk_fma_f32 v[92:93], v[92:93], s[86:87], v[122:123] op_sel_hi:[1,1,0]\n"
    "v_exp_f32 v90, v90\n"
    "v_pk_add_f32 v[98:99], v[98:99], v[86:87]\n"
    "v_exp_f32 v91, v91\n"
    "v_pk_mul_f32 v[16:17], v[16:17], v[118:119] op_sel_hi:[1,0]\n"
    "v_exp_f32 v92, v92\n"
    "v_pk_mul_f32 v[18:19], v[18:19], v[118:119] op_sel_hi:[1,0]\n"
    "v_exp_f32 v93, v93\n"
    "v_pk_mul_f32 v[20:21], v[20:21], v[118:119] op_sel_hi:[1,0]\n"
    "v_pk_mul_f32 v[22:23], v[22:23], v[118:119] op_sel_hi:[1,0]\n"
    "v_pk_mul_f32 v[24:25], v[24:25], v[118:119] op_sel_hi:[1,0]\n"
    "v_pk_mul_f32 v[26:27], v[26:27], v[118:119] op_sel_hi:[1,0]\n"
    "v_pk_mul_f32 v[28:29], v[28:29], v[118:119] op_sel_hi:[1,0]\n"
    "v_pk_mul_f32 v[30:31], v[30:31], v[118:119] op_sel_hi:[1,0]\n"
    "v_pk_mul_f32 v[32:33], v[32:33], v[118:119] op_sel_hi:[1,0]\n"
    "v_max3_f32 v126, v126, v178, v179\n"
    "v_max3_f32 v127, v127, v180, v181\n"
    "v_pk_fma_f32 v[94:95], v[94:95], s[86:87], v[122:123] op_sel_hi:[1,1,0]\n"
    "v_pk_add_f32 v[98:99], v[98:99], v[88:89]\n"
    "v_exp_f32 v94, v94\n"
    "v_cvt_pk_bf16_f32 v38, v16, v17\n"
    "v_exp_f32 v95, v95\n"
    "v_cvt_pk_bf16_f32 v39, v18, v19\n"
    "v_cvt_pk_bf16_f32 v40, v20, v21\n"
    "v_cvt_pk_bf16_f32 v41, v22, v23\n"
    "v_cvt_pk_bf16_f32 v42, v24, v25\n"
    "v_cvt_pk_bf16_f32 v43, v26, v27\n"
    "v_cvt_pk_bf16_f32 v44, v28, v29\n"
    "v_cvt_pk_bf16_f32 v45, v30, v31\n"
    "v_cvt_pk_bf16_f32 v46, v32, v33\n"
    "v_max_f32_e64 v126, v126, v127\n"
    "v_pk_fma_f32 v[96:97], v[96:97], s[86:87], v[122:123] op_sel_hi:[1,1,0]\n"
    "v_pk_add_f32 v[98:99], v[98:99], v[90:91]\n"
    "v_exp_f32 v96, v96\n"
    "v_mul_f32 v126, 0xbfb8aa3b, v126\n"
    "v_exp_f32 v97, v97\n"
    "v_pk_add_f32 v[98:99], v[98:99], v[92:93]\n"
    "v_pk_fma_f32 v[164:165], v[164:165], s[86:87], v[126:127] op_sel_hi:[1,1,0]\n"
    "v_pk_fma_f32 v[166:167], v[166:167], s[86:87], v[126:127] op_sel_hi:[1,1,0]\n"
    "v_exp_f32 v164, v164\n"
    "v_pk_add_f32 v[98:99], v[98:99], v[94:95]\n"
    "v_exp_f32 v165, v165\n"
    "v_pk_fma_f32 v[168:169], v[168:169], s[86:87], v[126:127] op_sel_hi:[1,1,0]\n"
    "v_exp_f32 v166, v166\n"
    "v_pk_add_f32 v[98:99], v[98:99], v[96:97]\n"
    "v_exp_f32 v167, v167\n"
    "v_pk_fma_f32 v[170:171], v[170:171], s[86:87], v[126:127] op_sel_hi:[1,1,0]\n"
    "v_exp_f32 v168, v168\n"
    "v_add_f32 v122, v98, v99\n"
    "v_exp_f32 v169, v169\n"
    "v_pk_fma_f32 v[172:173], v[172:173], s[86:87], v[126:127] op_sel_hi:[1,1,0]\n"
    "v_exp_f32 v170, v170\n"
    "v_pk_add_f32 v[182:183], v[164:165], v[166:167]\n"
    "v_exp_f32 v171, v171\n"
    "v_pk_fma_f32 v[174:175], v[174:175], s[86:87], v[126:127] op_sel_hi:[1,1,0]\n"
    "v_rcp_f32 v122, v122\n"
    "v_pk_add_f32 v[182:183], v[182:183], v[168:169]\n"
    "v_exp_f32 v172, v172\n"
    "v_pk_fma_f32 v[176:177], v[176:177], s[86:87], v[126:127] op_sel_hi:[1,1,0]\n"
    "v_exp_f32 v173, v173\n"
    "v_pk_add_f32 v[182:183], v[182:183], v[170:171]\n"
    "v_exp_f32 v174, v174\n"
    "v_pk_mul_f32 v[80:81], v[80:81], v[122:123] op_sel_hi:[1,0]\n"
    "v_exp_f32 v175, v175\n"
    "v_pk_mul_f32 v[82:83], v[82:83], v[122:123] op_sel_hi:[1,0]\n"
    "v_exp_f32 v176, v176\n"
    "v_pk_mul_f32 v[84:85], v[84:85], v[122:123] op_sel_hi:[1,0]\n"
    "v_exp_f32 v177, v177\n"
    "v_pk_mul_f32 v[86:87], v[86:87], v[122:123] op_sel_hi:[1,0]\n"
    "v_pk_mul_f32 v[88:89], v[88:89], v[122:123] op_sel_hi:[1,0]\n"
    "v_pk_mul_f32 v[90:91], v[90:91], v[122:123] op_sel_hi:[1,0]\n"
    "v_pk_mul_f32 v[92:93], v[92:93], v[122:123] op_sel_hi:[1,0]\n"
    "v_pk_mul_f32 v[94:95], v[94:95], v[122:123] op_sel_hi:[1,0]\n"
    "v_pk_mul_f32 v[96:97], v[96:97], v[122:123] op_sel_hi:[1,0]\n"
    "v_pk_fma_f32 v[178:179], v[178:179], s[86:87], v[126:127] op_sel_hi:[1,1,0]\n"
    "v_pk_add_f32 v[182:183], v[182:183], v[172:173]\n"
    "v_exp_f32 v178, v178\n"
    "v_cvt_pk_bf16_f32 v100, v80, v81\n"
    "v_exp_f32 v179, v179\n"
    "v_cvt_pk_bf16_f32 v101, v82, v83\n"
    "v_cvt_pk_bf16_f32 v102, v84, v85\n"
    "v_cvt_pk_bf16_f32 v103, v86, v87\n"
    "v_cvt_pk_bf16_f32 v104, v88, v89\n"
    "v_cvt_pk_bf16_f32 v105, v90, v91\n"
    "v_cvt_pk_bf16_f32 v106, v92, v93\n"
    "v_cvt_pk_bf16_f32 v107, v94, v95\n"
    "v_cvt_pk_bf16_f32 v108, v96, v97\n"
    "v_pk_fma_f32 v[180:181], v[180:181], s[86:87], v[126:127] op_sel_hi:[1,1,0]\n"
    "v_pk_add_f32 v[182:183], v[182:183], v[174:175]\n"
    "v_exp_f32 v180, v180\n"
    "v_pk_add_f32 v[182:183], v[182:183], v[176:177]\n"
    "v_exp_f32 v181, v181\n"
    "v_pk_add_f32 v[182:183], v[182:183], v[178:179]\n"
    "v_pk_add_f32 v[182:183], v[182:183], v[180:181]\n"
    "v_add_f32 v126, v182, v183\n"
    "v_rcp_f32_e64 v126, v126\n"
    "s_nop 0\n"
    "v_pk_mul_f32 v[164:165], v[164:165], v[126:127] op_sel_hi:[1,0]\n"
    "v_pk_mul_f32 v[166:167], v[166:167], v[126:127] op_sel_hi:[1,0]\n"
    "v_pk_mul_f32 v[168:169], v[168:169], v[126:127] op_sel_hi:[1,0]\n"
    "v_pk_mul_f32 v[170:171], v[170:171], v[126:127] op_sel_hi:[1,0]\n"
    "v_pk_mul_f32 v[172:173], v[172:173], v[126:127] op_sel_hi:[1,0]\n"
    "v_pk_mul_f32 v[174:175], v[174:175], v[126:127] op_sel_hi:[1,0]\n"
    "v_pk_mul_f32 v[176:177], v[176:177], v[126:127] op_sel_hi:[1,0]\n"
    "v_pk_mul_f32 v[178:179], v[178:179], v[126:127] op_sel_hi:[1,0]\n"
    "v_pk_mul_f32 v[180:181], v[180:181], v[126:127] op_sel_hi:[1,0]\n"
    "v_cvt_pk_bf16_f32 v184, v164, v165\n"
    "v_cvt_pk_bf16_f32 v185, v166, v167\n"
    "v_cvt_pk_bf16_f32 v186, v168, v169\n"
    "v_cvt_pk_bf16_f32 v187, v170, v171\n"
    "v_cvt_pk_bf16_f32 v188, v172, v173\n"
    "v_cvt_pk_bf16_f32 v189, v174, v175\n"
    "v_cvt_pk_bf16_f32 v190, v176, v177\n"
    "v_cvt_pk_bf16_f32 v191, v178, v179\n"
    "v_cvt_pk_bf16_f32 v192, v180, v181\n"
    "s_lshr_b32 s68, s35, 1\n"
    "s_and_b32 s70, s35, 1\n"
    "s_mul_i32 s69, s68, 3840\n"
    "v_mul_u32_u24 v8, 960, v3\n"
    "s_add_u32 s69, s69, 156032\n"
    "s_lshl1_add_u32 s72, s57, s68\n"
    "s_lshl1_add_u32 s90, s55, s70\n"
    "v_and_b32 v7, 7, v4\n"
    "s_lshl_b32 s91, s90, 4\n"
    "s_mul_i32 s90, s90, 221184\n"
    "v_add_u32 v8, s69, v8\n"
    "s_add_u32 s90, s90, 2097152\n"
    "s_lshl_b32 s89, s72, 12\n"
    "v_mad_u32_u24 v226, v7, 48, v8\n"
    "v_add_u32 v7, -1, v6\n"
    "s_add_u32 s89, s89, s90\n"
    "v_min_u32 v7, 2, v7\n"
    "s_add_u32 s89, s89, 147456\n"
    "s_lshl_b32 s67, s72, 9\n"
    "v_add_u32 v7, 16, v7\n"
    "s_add_u32 s90, s90, s67\n"
    "v_mad_u32_u24 v228, v7, 48, v8\n"
    "v_lshrrev_b32 v9, 1, v3\n"
    "v_lshrrev_b32 v7, 3, v4\n"
    "s_add_u32 s90, s90, 212992\n"
    "s_add_u32 s91, s72, s91\n"
    "v_lshlrev_b32 v229, 3, v4\n"
    "v_lshlrev_b32_e64 v10, 30, v3\n"
    "v_cmp_eq_u32 s[76:77], v9, v7\n"
    "v_add_u32 v227, 384, v226\n"
    "v_mov_b32 v7, 163712\n"
    "s_lshl_b32 s91, s91, 7\n"
    "v_lshl_add_u32 v229, v9, 2, v229\n"
    "v_and_b32 v10, 0x40000000, v10\n"
    "v_cndmask_b32_e64 v226, v7, v226, s[76:77]\n"
    "v_cndmask_b32_e64 v227, v7, v227, s[76:77]\n"
    "v_cndmask_b32_e64 v228, v7, v228, s[76:77]\n"
    "s_add_u32 s91, s91, 1966080\n"
    "v_or_b32 v229, v229, v10\n"
    "s_barrier\n"
    "s_and_b32 s60, s35, 1\n"
    "s_lshr_b32 s61, s35, 1\n"
    "s_mul_i32 s62, s58, 221184\n"
    "s_lshl1_add_u32 s67, s60, s61\n"
    "v_lshlrev_b32 v14, 3, v3\n"
    "s_lshl_b32 s65, s67, 10\n"
    "s_add_u32 s62, s62, 2097152\n"
    "s_lshl_b32 s69, s61, 13\n"
    "s_mul_i32 s66, s67, 2048\n"
    "s_add_u32 s63, s62, s69\n"
    "s_add_u32 s14, s56, 0\n"
    "v_add_u32 v14, 135552, v14\n"
    "s_sub_u32 s64, s14, 9\n"
    "v_mul_u32_u24 v199, 128, v4\n"
    "s_cselect_b32 s14, s14, s64\n"
    "v_add3_u32 v199, v199, v14, s66\n"
    "s_lshl_b32 s14, s14, 14\n"
    "ds_read_b64 v[242:243], v199 offset:0\n"
    "s_add_u32 s14, s14, s63\n"
    "s_add_u32 s15, s56, 1\n"
    "ds_read_b64 v[110:111], v14 offset:8192\n"
    "s_sub_u32 s64, s15, 9\n"
    "ds_read_b64 v[244:245], v199 offset:32\n"
    "s_cselect_b32 s15, s15, s64\n"
    "ds_read_b64 v[112:113], v14 offset:8224\n"
    "s_lshl_b32 s15, s15, 14\n"
    "ds_read_b64 v[246:247], v199 offset:64\n"
    "s_add_u32 s15, s15, s63\n"
    "s_add_u32 s16, s56, 2\n"
    "ds_read_b64 v[114:115], v14 offset:8256\n"
    "s_sub_u32 s64, s16, 9\n"
    "ds_read_b64 v[248:249], v199 offset:96\n"
    "s_cselect_b32 s16, s16, s64\n"
    "ds_read_b64 v[116:117], v14 offset:8288\n"
    "s_lshl_b32 s16, s16, 14\n"
    "v_lshrrev_b32 v7, 1, v3\n"
    "s_add_u32 s16, s16, s63\n"
    "s_add_u32 s17, s56, 3\n"
    "v_and_b32 v8, 1, v3\n"
    "s_sub_u32 s64, s17, 9\n"
    "v_lshrrev_b32 v9, 1, v4\n"
    "s_cselect_b32 s17, s17, s64\n"
    "v_mul_u32_u24 v8, 8192, v8\n"
    "s_lshl_b32 s17, s17, 14\n"
    "v_lshlrev_b32 v13, 4, v0\n"
    "s_add_u32 s17, s17, s63\n"
    "s_add_u32 s18, s56, 4\n"
    "v_add_u32 v194, s65, v13\n"
    "s_sub_u32 s64, s18, 9\n"
    "v_xor_b32 v195, 1024, v194\n"
    "s_cselect_b32 s18, s18, s64\n"
    "s_waitcnt lgkmcnt(7)\n"
    "v_lshlrev_b32 v16, 16, v242\n"
    "s_lshl_b32 s18, s18, 14\n"
    "v_and_b32 v17, 0xffff0000, v242\n"
    "s_add_u32 s18, s18, s63\n"
    "s_add_u32 s19, s56, 5\n"
    "s_waitcnt lgkmcnt(6)\n"
    "v_lshlrev_b32 v120, 16, v110\n"
    "s_sub_u32 s64, s19, 9\n"
    "v_and_b32 v121, 0xffff0000, v110\n"
    "s_cselect_b32 s19, s19, s64\n"
    "v_lshlrev_b32 v18, 16, v243\n"
    "s_lshl_b32 s19, s19, 14\n"
    "v_and_b32 v19, 0xffff0000, v243\n"
    "s_add_u32 s19, s19, s63\n"
    "s_add_u32 s20, s56, 6\n"
    "v_lshlrev_b32 v122, 16, v111\n"
    "s_sub_u32 s64, s20, 9\n"
    "v_and_b32 v123, 0xffff0000, v111\n"
    "s_cselect_b32 s20, s20, s64\n"
    "s_waitcnt lgkmcnt(5)\n"
    "v_lshlrev_b32 v20, 16, v244\n"
    "s_lshl_b32 s20, s20, 14\n"
    "v_and_b32 v21, 0xffff0000, v244\n"
    "s_add_u32 s20, s20, s63\n"
    "s_add_u32 s21, s56, 7\n"
    "s_waitcnt lgkmcnt(4)\n"
    "v_lshlrev_b32 v124, 16, v112\n"
    "s_sub_u32 s64, s21, 9\n"
    "v_and_b32 v125, 0xffff0000, v112\n"
    "s_cselect_b32 s21, s21, s64\n"
    "v_lshlrev_b32 v22, 16, v245\n"
    "s_lshl_b32 s21, s21, 14\n"
    "v_and_b32 v23, 0xffff0000, v245\n"
    "s_add_u32 s21, s21, s63\n"
    "s_add_u32 s22, s56, 8\n"
    "v_lshlrev_b32 v126, 16, v113\n"
    "s_sub_u32 s64, s22, 9\n"
    "v_and_b32 v127, 0xffff0000, v113\n"
    "s_cselect_b32 s22, s22, s64\n"
    "s_waitcnt lgkmcnt(3)\n"
    "v_lshlrev_b32 v24, 16, v246\n"
    "s_lshl_b32 s22, s22, 14\n"
    "v_and_b32 v25, 0xffff0000, v246\n"
    "s_add_u32 s22, s22, s63\n"
    "s_lshl_b32 s68, s61, 2\n"
    "s_waitcnt lgkmcnt(2)\n"
    "v_lshlrev_b32 v128, 16, v114\n"
    "v_or_b32 v7, s68, v7\n"
    "v_and_b32 v129, 0xffff0000, v114\n"
    "v_xor_b32 v10, v7, v9\n"
    "v_lshlrev_b32_e64 v26, 16, v247\n"
    "v_lshl_add_u32 v10, v10, 4, v8\n"
    "v_and_b32 v27, 0xffff0000, v247\n"
    "v_lshl_add_u32 v204, v4, 7, v10\n"
    "v_lshlrev_b32 v130, 16, v115\n"
    "v_xor_b32 v205, 32, v204\n"
    "v_and_b32 v131, 0xffff0000, v115\n"
    "s_waitcnt lgkmcnt(1)\n"
    "v_lshlrev_b32 v28, 16, v248\n"
    "v_and_b32 v29, 0xffff0000, v248\n"
    "s_waitcnt lgkmcnt(0)\n"
    "v_lshlrev_b32 v132, 16, v116\n"
    "v_and_b32 v133, 0xffff0000, v116\n"
    "v_lshlrev_b32 v30, 16, v249\n"
    "v_and_b32 v31, 0xffff0000, v249\n"
    "v_lshlrev_b32 v134, 16, v117\n"
    "v_and_b32 v135, 0xffff0000, v117\n"
    "ds_read_b128 v[144:147], v204 offset:0\n"
    "ds_read_b128 v[148:151], v204 offset:2048\n"
    "ds_read_b128 v[152:155], v204 offset:4096\n"
    "ds_read_b128 v[156:159], v204 offset:6144\n"
    "ds_read_b128 v[164:167], v205 offset:0\n"
    "ds_read_b128 v[168:171], v205 offset:2048\n"
    "ds_read_b128 v[172:175], v205 offset:4096\n"
    "ds_read_b128 v[176:179], v205 offset:6144\n"
    "ds_read_b128 v[56:59], v204 offset:16384\n"
    "ds_read_b128 v[60:63], v204 offset:18432\n"
    "ds_read_b128 v[64:67], v204 offset:20480\n"
    "ds_read_b128 v[68:71], v204 offset:22528\n"
    "ds_read_b128 v[76:79], v205 offset:16384\n"
    "ds_read_b128 v[80:83], v205 offset:18432\n"
    "ds_read_b128 v[84:87], v205 offset:20480\n"
    "ds_read_b128 v[88:91], v205 offset:22528\n"
    "v_bfe_u32 v5, v0, 2, 2\n"
    "v_and_b32_e64 v6, 3, v0\n"
    "v_add_u32 v196, 4096, v194\n"
    "v_add_u32 v197, 4096, v195\n"
    "v_pk_mul_f32 v[16:17], v[16:17], v[120:121]\n"
    "v_pk_mul_f32 v[18:19], v[18:19], v[122:123]\n"
    "v_pk_mul_f32 v[20:21], v[20:21], v[124:125]\n"
    "v_pk_mul_f32 v[22:23], v[22:23], v[126:127]\n"
    "v_pk_mul_f32 v[24:25], v[24:25], v[128:129]\n"
    "v_pk_mul_f32 v[26:27], v[26:27], v[130:131]\n"
    "v_pk_mul_f32 v[28:29], v[28:29], v[132:133]\n"
    "v_pk_mul_f32 v[30:31], v[30:31], v[134:135]\n"
    "s_setprio 2\n"
    "s_nop 0\n"
    "s_waitcnt lgkmcnt(0)\n"
    "s_barrier\n"
    "ds_read_b128 v[130:133], v228 offset:0\n"
    "ds_read_b128 v[134:137], v228 offset:16\n"
    "ds_read_b128 v[110:113], v226 offset:0\n"
    "ds_read_b128 v[120:123], v227 offset:0\n"
    "ds_read_b64 v[138:139], v228 offset:32\n"
    "ds_read_b128 v[114:117], v226 offset:16\n"
    "ds_read_b128 v[124:127], v227 offset:16\n"
    "ds_read_b64 v[118:119], v226 offset:32\n"
    "ds_read_b64 v[128:129], v227 offset:32\n"
    "s_waitcnt lgkmcnt(8)\n"
    "v_mfma_f32_16x16x32_bf16 v[250:253], v[130:133], v[232:235], 0\n"
    "s_waitcnt lgkmcnt(7)\n"
    "v_mfma_f32_16x16x32_bf16 v[250:253], v[134:137], v[236:239], v[250:253]\n"
    "s_waitcnt lgkmcnt(6)\n"
    "v_mfma_f32_16x16x32_bf16 v[242:245], v[110:113], v[232:235], 0\n"
    "s_waitcnt lgkmcnt(5)\n"
    "v_mfma_f32_16x16x32_bf16 v[246:249], v[120:123], v[232:235], 0\n"
    "s_waitcnt lgkmcnt(4)\n"
    "v_mfma_f32_16x16x16_bf16 v[250:253], v[138:139], v[240:241], v[250:253]\n"
    "s_waitcnt lgkmcnt(3)\n"
    "v_mfma_f32_16x16x32_bf16 v[242:245], v[114:117], v[236:239], v[242:245]\n"
    "s_waitcnt lgkmcnt(2)\n"
    "v_mfma_f32_16x16x32_bf16 v[246:249], v[124:127], v[236:239], v[246:249]\n"
    "s_nop 3\n"
    "v_add_f32 v250, 0x358637bd, v250\n"
    "s_waitcnt lgkmcnt(1)\n"
    "v_mfma_f32_16x16x16_bf16 v[242:245], v[118:119], v[240:241], v[242:245]\n"
    "v_rcp_f32 v250, v250\n"
    "s_waitcnt lgkmcnt(0)\n"
    "v_mfma_f32_16x16x16_bf16 v[246:249], v[128:129], v[240:241], v[246:249]\n"
    "v_mul_f32 v251, v251, v250\n"
    "v_mul_f32 v252, v252, v250\n"
    "v_mfma_f32_16x16x32_bf16 v[96:99], v[130:133], v[38:41], 0\n"
    "v_cvt_pk_bf16_f32 v222, v251, v252\n"
    "s_nop 0\n"
    "v_mul_f32 v242, v242, v250\n"
    "v_mfma_f32_16x16x32_bf16 v[96:99], v[134:137], v[42:45], v[96:99]\n"
    "v_mul_f32 v243, v243, v250\n"
    "v_mul_f32 v244, v244, v250\n"
    "v_mfma_f32_16x16x32_bf16 v[48:51], v[110:113], v[38:41], 0\n"
    "v_mul_f32 v245, v245, v250\n"
    "v_mul_f32 v246, v246, v250\n"
    "v_mfma_f32_16x16x32_bf16 v[52:55], v[120:123], v[38:41], 0\n"
    "v_mul_f32 v247, v247, v250\n"
    "v_mul_f32 v248, v248, v250\n"
    "v_mfma_f32_16x16x16_bf16 v[96:99], v[138:139], v[46:47], v[96:99]\n"
    "v_mul_f32 v249, v249, v250\n"
    "v_cvt_pk_bf16_f32 v206, v242, v243\n"
    "v_mfma_f32_16x16x32_bf16 v[48:51], v[114:117], v[42:45], v[48:51]\n"
    "v_cvt_pk_bf16_f32 v207, v244, v245\n"
    "v_cvt_pk_bf16_f32 v208, v246, v247\n"
    "v_mfma_f32_16x16x32_bf16 v[52:55], v[124:127], v[42:45], v[52:55]\n"
    "v_cvt_pk_bf16_f32 v209, v248, v249\n"
    "s_nop 0\n"
    "v_add_f32 v96, 0x358637bd, v96\n"
    "v_mfma_f32_16x16x16_bf16 v[48:51], v[118:119], v[46:47], v[48:51]\n"
    "buffer_store_dwordx4 v[206:209], v230, s[36:39], s89 offen offset:0 sc1\n"
    "v_mfma_f32_16x16x16_bf16 v[52:55], v[128:129], v[46:47], v[52:55]\n"
    "buffer_store_dword v222, v229, s[36:39], s90 offen offset:0 sc1\n"
    "v_mfma_f32_16x16x32_bf16 v[250:253], v[130:133], v[100:103], 0\n"
    "v_rcp_f32 v96, v96\n"
    "v_mfma_f32_16x16x32_bf16 v[250:253], v[134:137], v[104:107], v[250:253]\n"
    "v_mul_f32 v97, v97, v96\n"
    "v_mul_f32 v98, v98, v96\n"
    "v_mfma_f32_16x16x32_bf16 v[242:245], v[110:113], v[100:103], 0\n"
    "v_mul_f32 v48, v48, v96\n"
    "v_mul_f32 v49, v49, v96\n"
    "v_mfma_f32_16x16x32_bf16 v[246:249], v[120:123], v[100:103], 0\n"
    "v_mul_f32 v50, v50, v96\n"
    "v_mul_f32 v51, v51, v96\n"
    "v_mfma_f32_16x16x16_bf16 v[250:253], v[138:139], v[108:109], v[250:253]\n"
    "v_mul_f32 v52, v52, v96\n"
    "v_mul_f32 v53, v53, v96\n"
    "v_mfma_f32_16x16x32_bf16 v[242:245], v[114:117], v[104:107], v[242:245]\n"
    "v_mul_f32 v54, v54, v96\n"
    "v_mul_f32 v55, v55, v96\n"
    "v_mfma_f32_16x16x32_bf16 v[246:249], v[124:127], v[104:107], v[246:249]\n"
    "v_cvt_pk_bf16_f32 v223, v97, v98\n"
    "v_cvt_pk_bf16_f32 v210, v48, v49\n"
    "v_mfma_f32_16x16x16_bf16 v[242:245], v[118:119], v[108:109], v[242:245]\n"
    "v_cvt_pk_bf16_f32 v211, v50, v51\n"
    "v_cvt_pk_bf16_f32 v212, v52, v53\n"
    "v_mfma_f32_16x16x16_bf16 v[246:249], v[128:129], v[108:109], v[246:249]\n"
    "v_cvt_pk_bf16_f32 v213, v54, v55\n"
    "v_add_f32 v250, 0x358637bd, v250\n"
    "v_mfma_f32_16x16x32_bf16 v[96:99], v[130:133], v[184:187], 0\n"
    "buffer_store_dwordx4 v[210:213], v230, s[36:39], s89 offen offset:1024 sc1\n"
    "v_mfma_f32_16x16x32_bf16 v[96:99], v[134:137], v[188:191], v[96:99]\n"
    "buffer_store_dword v223, v229, s[36:39], s90 offen offset:128 sc1\n"
    "v_mfma_f32_16x16x32_bf16 v[48:51], v[110:113], v[184:187], 0\n"
    "v_rcp_f32 v250, v250\n"
    "v_mfma_f32_16x16x32_bf16 v[52:55], v[120:123], v[184:187], 0\n"
    "v_mul_f32 v242, v242, v250\n"
    "v_mul_f32 v243, v243, v250\n"
    "v_mfma_f32_16x16x16_bf16 v[96:99], v[138:139], v[192:193], v[96:99]\n"
    "v_mul_f32 v244, v244, v250\n"
    "v_mul_f32 v245, v245, v250\n"
    "v_mfma_f32_16x16x32_bf16 v[48:51], v[114:117], v[188:191], v[48:51]\n"
    "v_mul_f32 v246, v246, v250\n"
    "v_mul_f32 v247, v247, v250\n"
    "v_mfma_f32_16x16x32_bf16 v[52:55], v[124:127], v[188:191], v[52:55]\n"
    "v_mul_f32 v248, v248, v250\n"
    "v_mul_f32_e64 v249, v249, v250\n"
    "v_mfma_f32_16x16x16_bf16 v[48:51], v[118:119], v[192:193], v[48:51]\n"
    "v_cvt_pk_bf16_f32 v214, v242, v243\n"
    "v_mov_b32 v140, 155648\n"
    "ds_read_b32 v140, v140\n"
    "v_cvt_pk_bf16_f32 v215, v244, v245\n"
    "v_mfma_f32_16x16x16_bf16 v[52:55], v[128:129], v[192:193], v[52:55]\n"
    "v_cvt_pk_bf16_f32 v216, v246, v247\n"
    "v_cvt_pk_bf16_f32 v217, v248, v249\n"
    "v_mul_f32 v251, v251, v250\n"
    "buffer_store_dwordx4 v[214:217], v230, s[36:39], s89 offen offset:2048 sc1\n"
    "v_mul_f32 v252, v252, v250\n"
    "v_add_f32 v96, 0x358637bd, v96\n"
    "v_cvt_pk_bf16_f32 v224, v251, v252\n"
    "v_rcp_f32_e64 v96, v96\n"
    "buffer_store_dword v224, v229, s[36:39], s90 offen offset:256 sc1\n"
    "v_pk_mul_f32 v[48:49], v[48:49], v[96:97] op_sel_hi:[1,0]\n"
    "v_pk_mul_f32 v[50:51], v[50:51], v[96:97] op_sel_hi:[1,0]\n"
    "v_pk_mul_f32 v[52:53], v[52:53], v[96:97] op_sel_hi:[1,0]\n"
    "v_pk_mul_f32 v[54:55], v[54:55], v[96:97] op_sel_hi:[1,0]\n"
    "v_cvt_pk_bf16_f32 v218, v48, v49\n"
    "v_cvt_pk_bf16_f32 v219, v50, v51\n"
    "v_cvt_pk_bf16_f32 v220, v52, v53\n"
    "v_cvt_pk_bf16_f32 v221, v54, v55\n"
    "v_mul_f32 v97, v97, v96\n"
    "buffer_store_dwordx4 v[218:221], v230, s[36:39], s89 offen offset:3072 sc1\n"
    "v_mul_f32 v98, v98, v96\n"
    "v_cvt_pk_bf16_f32 v225, v97, v98\n"
    "buffer_store_dword v225, v229, s[36:39], s90 offen offset:384 sc1\n"
    "s_waitcnt lgkmcnt(0)\n"
    "v_readfirstlane_b32 s88, v140\n"
    "s_nop 0\n"
    "s_cmp_eq_u32 s88, 0\n"
    "s_cbranch_scc0 .Lfl_a\n"
    "s_waitcnt vmcnt(0)\n"
    "v_mov_b32 v248, s34\n"
    "v_mov_b32 v249, s34\n"
    "v_mov_b32 v250, s34\n"
    "v_mov_b32 v251, s34\n"
    "s_mov_b64 exec, 0xff\n"
    "buffer_store_dwordx4 v[248:251], v13, s[36:39], s91 offen sc1\n"
    "s_mov_b64 exec, -1\n"
    ".Lfl_a:\n"
    "s_nop 0\n"
    "s_waitcnt lgkmcnt(0)\n"
    "s_barrier\n"
    "buffer_load_dwordx4 v[96:99], v194, s[36:39], s14 offen sc1\n"
    "buffer_load_dwordx4 v[100:103], v195, s[36:39], s14 offen sc1\n"
    "buffer_load_dwordx4 v[104:107], v196, s[36:39], s14 offen sc1\n"
    "buffer_load_dwordx4 v[108:111], v197, s[36:39], s14 offen sc1\n"
    "buffer_load_dwordx4 v[112:115], v194, s[36:39], s15 offen sc1\n"
    "buffer_load_dwordx4 v[116:119], v195, s[36:39], s15 offen sc1\n"
    "buffer_load_dwordx4 v[120:123], v196, s[36:39], s15 offen sc1\n"
    "buffer_load_dwordx4 v[124:127], v197, s[36:39], s15 offen sc1\n"
    "v_lshlrev_b32 v7, 7, v4\n"
    "v_lshlrev_b32 v12, 4, v4\n"
    "s_lshl_b32 s76, s58, 11\n"
    "s_add_u32 s76, s76, 1966080\n"
    "s_lshl2_add_u32 s66, s58, s67\n"
    "s_lshl_b32 s66, s66, 8\n"
    "s_add_u32 s66, s66, 90112\n"
    "s_xor_b32 s68, s66, 256\n"
    "v_and_b32 v9, 1, v3\n"
    "v_lshl_add_u32 v9, v9, 2, v5\n"
    "v_lshrrev_b32_e64 v10, 1, v3\n"
    "v_lshl_add_u32 v10, v10, 4, v9\n"
    "v_mul_u32_u24 v10, 128, v10\n"
    "s_mul_i32 s69, s61, 4096\n"
    "s_add_u32 s69, s69, 61440\n"
    "v_add_u32 v10, s69, v10\n"
    "v_lshl_add_u32 v200, v6, 3, v10\n"
    "v_and_b32 v9, 6, v9\n"
    "v_lshl_add_u32 v200, v9, 4, v200\n"
    "v_xor_b32 v201, 32, v200\n"
    "v_xor_b32 v202, 64, v200\n"
    "v_xor_b32 v203, 96, v200\n"
    "s_cmp_eq_u32 s88, 0\n"
    "s_cselect_b32 s88, 0x40000000, 0\n"
    "v_or_b32 v252, s88, v13\n"
    "s_waitcnt vmcnt(8)\n"
    "v_mov_b32 v248, s34\n"
    "v_mov_b32 v249, s34\n"
    "v_mov_b32 v250, s34\n"
    "v_mov_b32 v251, s34\n"
    "s_mov_b64 exec, 0xff\n"
    "buffer_store_dwordx4 v[248:251], v252, s[36:39], s91 offen sc1\n"
    "s_mov_b64 exec, -1\n"
    "s_lshl_b32 s69, s61, 5\n"
    "s_add_u32 s69, s69, s67\n"
    "s_lshl_b32 s69, s69, 7\n"
    "s_add_u32 s69, s69, 212992\n"
    "v_lshlrev_b32 v14, 3, v4\n"
    "v_and_b32 v15, 2, v3\n"
    "v_add_u32 v15, v15, v3\n"
    "v_lshl_add_u32 v14, v15, 9, v14\n"
    "v_add_u32 v14, s69, v14\n"
    "v_xor_b32 v15, 128, v14\n"
    "s_lshl_b32 s69, s58, 5\n"
    "s_add_u32 s69, s69, s67\n"
    "s_mul_i32 s69, s69, 4608\n"
    "s_and_b32 s70, s54, 3\n"
    "s_mul_i32 s70, s70, 256\n"
    "s_add_u32 s70, s70, 0\n"
    "s_add_u32 s69, s69, s70\n"
    "s_mul_i32 s70, s35, 5120\n"
    "s_add_u32 s70, s70, 0\n"
    "s_xor_b32 s70, s35, 2\n"
    "s_mul_i32 s70, s70, 5120\n"
    "s_add_u32 s70, s70, 0\n"
    "s_mov_b32 s40, s32\n"
    "s_and_b32 s41, s33, 0xffff\n"
    "s_mov_b32 s42, 4718592\n"
    "s_mov_b32 s43, 0x20000\n"
    "s_lshl2_add_u32 s70, s61, s67\n"
    "s_mul_i32 s71, s70, 4352\n"
    "s_xor_b32 s70, s70, 1\n"
    "s_mul_i32 s70, s70, 4352\n"
    "v_mul_u32_u24 v9, 272, v4\n"
    "v_lshl_add_u32 v9, v3, 4, v9\n"
    "v_add_u32 v8, s71, v9\n"
    "v_add_u32 v9, s70, v9\n"
    "s_mul_i32 s70, s67, 4352\n"
    "v_add_u32 v10, 0, v0\n"
    "v_lshrrev_b32 v11, 4, v10\n"
    "v_add_lshl_u32 v206, v10, v11, 4\n"
    "v_add_u32 v206, s70, v206\n"
    "v_mul_u32_u24 v211, 16, v11\n"
    "v_sub_u32 v10, v10, v211\n"
    "v_lshrrev_b32 v211, 2, v11\n"
    "v_lshlrev_b32 v211, 1, v211\n"
    "v_and_or_b32 v211, v11, 1, v211\n"
    "v_bfe_u32 v11, v11, 1, 1\n"
    "v_lshl_add_u32 v211, v211, 3, v11\n"
    "v_mul_u32_u24 v211, 2304, v211\n"
    "v_lshl_add_u32 v211, v10, 4, v211\n"
    "v_add_u32 v10, 64, v0\n"
    "v_lshrrev_b32 v11, 4, v10\n"
    "v_add_lshl_u32 v207, v10, v11, 4\n"
    "v_add_u32 v207, s70, v207\n"
    "v_mul_u32_u24 v212, 16, v11\n"
    "v_sub_u32 v10, v10, v212\n"
    "v_lshrrev_b32 v212, 2, v11\n"
    "v_lshlrev_b32_e64 v212, 1, v212\n"
    "v_and_or_b32 v212, v11, 1, v212\n"
    "v_bfe_u32 v11, v11, 1, 1\n"
    "v_lshl_add_u32 v212, v212, 3, v11\n"
    "v_mul_u32_u24 v212, 2304, v212\n"
    "v_lshl_add_u32 v212, v10, 4, v212\n"
    "v_add_u32 v10, 128, v0\n"
    "v_lshrrev_b32 v11, 4, v10\n"
    "v_add_lshl_u32 v208, v10, v11, 4\n"
    "v_add_u32 v208, s70, v208\n"
    "v_mul_u32_u24 v213, 16, v11\n"
    "v_sub_u32 v10, v10, v213\n"
    "v_lshrrev_b32 v213, 2, v11\n"
    "v_lshlrev_b32 v213, 1, v213\n"
    "v_and_or_b32 v213, v11, 1, v213\n"
    "v_bfe_u32 v11, v11, 1, 1\n"
    "v_lshl_add_u32 v213, v213, 3, v11\n"
    "v_mul_u32_u24 v213, 2304, v213\n"
    "v_lshl_add_u32 v213, v10, 4, v213\n"
    "v_add_u32 v10, 192, v0\n"
    "v_lshrrev_b32 v11, 4, v10\n"
    "v_add_lshl_u32 v209, v10, v11, 4\n"
    "v_add_u32 v209, s70, v209\n"
    "v_mul_u32_u24 v214, 16, v11\n"
    "v_sub_u32 v10, v10, v214\n"
    "v_lshrrev_b32 v214, 2, v11\n"
    "v_lshlrev_b32 v214, 1, v214\n"
    "v_and_or_b32 v214, v11, 1, v214\n"
    "v_bfe_u32 v11, v11, 1, 1\n"
    "v_lshl_add_u32 v214, v214, 3, v11\n"
    "v_mul_u32_u24 v214, 2304, v214\n"
    "s_waitcnt vmcnt(8)\n"
    "v_mfma_f32_16x16x32_bf16 v[16:19], v[144:147], v[96:99], v[16:19]\n"
    "v_lshl_add_u32 v214, v10, 4, v214\n"
    "s_lshl_b32 s65, s61, 12\n"
    "s_waitcnt vmcnt(7)\n"
    "v_mfma_f32_16x16x32_bf16 v[36:39], v[144:147], v[100:103], 0\n"
    "s_add_u32 s63, s62, s65\n"
    "s_add_u32 s63, s63, 147456\n"
    "v_mfma_f32_16x16x32_bf16 v[20:23], v[148:151], v[96:99], v[20:23]\n"
    "buffer_load_dwordx4 v[128:131], v194, s[36:39], s16 offen sc1\n"
    "v_mfma_f32_16x16x32_bf16 v[40:43], v[148:151], v[100:103], 0\n"
    "s_add_u32 s23, s80, 0\n"
    "s_and_b32 s23, s23, 7\n"
    "v_mfma_f32_16x16x32_bf16 v[24:27], v[152:155], v[96:99], v[24:27]\n"
    "s_lshl_b32 s23, s23, 13\n"
    "s_add_u32 s23, s23, s63\n"
    "v_mfma_f32_16x16x32_bf16 v[44:47], v[152:155], v[100:103], 0\n"
    "s_add_u32 s24, s80, 1\n"
    "s_and_b32 s24, s24, 7\n"
    "v_mfma_f32_16x16x32_bf16 v[28:31], v[156:159], v[96:99], v[28:31]\n"
    "buffer_load_dwordx4 v[132:135], v195, s[36:39], s16 offen sc1\n"
    "v_mfma_f32_16x16x32_bf16 v[48:51], v[156:159], v[100:103], 0\n"
    "s_lshl_b32 s24, s24, 13\n"
    "s_add_u32 s24, s24, s63\n"
    "s_waitcnt vmcnt(8)\n"
    "v_mfma_f32_16x16x32_bf16 v[16:19], v[164:167], v[104:107], v[16:19]\n"
    "s_add_u32 s25, s80, 2\n"
    "s_and_b32 s25, s25, 7\n"
    "s_waitcnt vmcnt(7)\n"
    "v_mfma_f32_16x16x32_bf16 v[36:39], v[164:167], v[108:111], v[36:39]\n"
    "s_lshl_b32 s25, s25, 13\n"
    "s_add_u32 s25, s25, s63\n"
    "v_mfma_f32_16x16x32_bf16 v[20:23], v[168:171], v[104:107], v[20:23]\n"
    "buffer_load_dwordx4 v[136:139], v196, s[36:39], s16 offen sc1\n"
    "v_mfma_f32_16x16x32_bf16 v[40:43], v[168:171], v[108:111], v[40:43]\n"
    "s_add_u32 s26, s80, 3\n"
    "s_and_b32 s26, s26, 7\n"
    "v_mfma_f32_16x16x32_bf16 v[24:27], v[172:175], v[104:107], v[24:27]\n"
    "s_lshl_b32 s26, s26, 13\n"
    "s_add_u32 s26, s26, s63\n"
    "v_mfma_f32_16x16x32_bf16 v[44:47], v[172:175], v[108:111], v[44:47]\n"
    "s_add_u32 s27, s80, 4\n"
    "s_and_b32 s27, s27, 7\n"
    "v_mfma_f32_16x16x32_bf16 v[28:31], v[176:179], v[104:107], v[28:31]\n"
    "buffer_load_dwordx4 v[140:143], v197, s[36:39], s16 offen sc1\n"
    "v_mfma_f32_16x16x32_bf16 v[48:51], v[176:179], v[108:111], v[48:51]\n"
    "s_lshl_b32 s27, s27, 13\n"
    "s_add_u32 s27, s27, s63\n"
    "s_waitcnt vmcnt(8)\n"
    "v_mfma_f32_16x16x32_bf16 v[16:19], v[56:59], v[112:115], v[16:19]\n"
    "s_add_u32 s28, s80, 5\n"
    "s_and_b32 s28, s28, 7\n"
    "s_waitcnt vmcnt(7)\n"
    "v_mfma_f32_16x16x32_bf16 v[36:39], v[56:59], v[116:119], v[36:39]\n"
    "s_lshl_b32 s28, s28, 13\n"
    "s_add_u32 s28, s28, s63\n"
    "v_mfma_f32_16x16x32_bf16 v[20:23], v[60:63], v[112:115], v[20:23]\n"
    "buffer_load_dwordx4 v[96:99], v194, s[36:39], s17 offen sc1\n"
    "v_mfma_f32_16x16x32_bf16 v[40:43], v[60:63], v[116:119], v[40:43]\n"
    "s_add_u32 s29, s80, 6\n"
    "s_and_b32 s29, s29, 7\n"
    "v_mfma_f32_16x16x32_bf16 v[24:27], v[64:67], v[112:115], v[24:27]\n"
    "s_lshl_b32 s29, s29, 13\n"
    "s_add_u32 s29, s29, s63\n"
    "v_mfma_f32_16x16x32_bf16 v[44:47], v[64:67], v[116:119], v[44:47]\n"
    "s_add_u32 s30, s80, 7\n"
    "s_and_b32 s30, s30, 7\n"
    "v_mfma_f32_16x16x32_bf16 v[28:31], v[68:71], v[112:115], v[28:31]\n"
    "buffer_load_dwordx4 v[100:103], v195, s[36:39], s17 offen sc1\n"
    "v_mfma_f32_16x16x32_bf16 v[48:51], v[68:71], v[116:119], v[48:51]\n"
    "s_lshl_b32 s30, s30, 13\n"
    "s_add_u32 s30, s30, s63\n"
    "s_waitcnt lgkmcnt(0)\n"
    "s_barrier\n"
    "s_waitcnt vmcnt(8)\n"
    "v_mfma_f32_16x16x32_bf16 v[16:19], v[76:79], v[120:123], v[16:19]\n"
    "ds_read_b128 v[144:147], v204 offset:0\n"
    "s_waitcnt vmcnt(7)\n"
    "v_mfma_f32_16x16x32_bf16 v[36:39], v[76:79], v[124:127], v[36:39]\n"
    "ds_read_b128 v[148:151], v204 offset:2048\n"
    "v_mfma_f32_16x16x32_bf16 v[20:23], v[80:83], v[120:123], v[20:23]\n"
    "buffer_load_dwordx4 v[104:107], v196, s[36:39], s17 offen sc1\n"
    "v_mfma_f32_16x16x32_bf16 v[40:43], v[80:83], v[124:127], v[40:43]\n"
    "ds_read_b128 v[152:155], v204 offset:4096\n"
    "v_mfma_f32_16x16x32_bf16 v[24:27], v[84:87], v[120:123], v[24:27]\n"
    "ds_read_b128 v[156:159], v204 offset:6144\n"
    "v_mfma_f32_16x16x32_bf16 v[44:47], v[84:87], v[124:127], v[44:47]\n"
    "v_mfma_f32_16x16x32_bf16 v[28:31], v[88:91], v[120:123], v[28:31]\n"
    "buffer_load_dwordx4 v[108:111], v197, s[36:39], s17 offen sc1\n"
    "v_mfma_f32_16x16x32_bf16 v[48:51], v[88:91], v[124:127], v[48:51]\n"
    "s_waitcnt vmcnt(7) lgkmcnt(3)\n"
    "v_mfma_f32_16x16x32_bf16 v[16:19], v[144:147], v[128:131], v[16:19]\n"
    "ds_read_b128 v[164:167], v205 offset:0\n"
    "s_waitcnt vmcnt(6)\n"
    "v_mfma_f32_16x16x32_bf16 v[36:39], v[144:147], v[132:135], v[36:39]\n"
    "ds_read_b128 v[168:171], v205 offset:2048\n"
    "s_waitcnt lgkmcnt(4)\n"
    "v_mfma_f32_16x16x32_bf16 v[20:23], v[148:151], v[128:131], v[20:23]\n"
    "buffer_load_dwordx4 v[112:115], v194, s[36:39], s18 offen sc1\n"
    "v_mfma_f32_16x16x32_bf16 v[40:43], v[148:151], v[132:135], v[40:43]\n"
    "ds_read_b128 v[172:175], v205 offset:4096\n"
    "s_waitcnt lgkmcnt(4)\n"
    "v_mfma_f32_16x16x32_bf16 v[24:27], v[152:155], v[128:131], v[24:27]\n"
    "ds_read_b128 v[176:179], v205 offset:6144\n"
    "v_mfma_f32_16x16x32_bf16 v[44:47], v[152:155], v[132:135], v[44:47]\n"
    "s_waitcnt lgkmcnt(4)\n"
    "v_mfma_f32_16x16x32_bf16 v[28:31], v[156:159], v[128:131], v[28:31]\n"
    "buffer_load_dwordx4 v[116:119], v195, s[36:39], s18 offen sc1\n"
    "v_mfma_f32_16x16x32_bf16 v[48:51], v[156:159], v[132:135], v[48:51]\n"
    "s_waitcnt lgkmcnt(0)\n"
    "s_barrier\n"
    "s_waitcnt vmcnt(7)\n"
    "v_mfma_f32_16x16x32_bf16 v[16:19], v[164:167], v[136:139], v[16:19]\n"
    "ds_read_b128 v[144:147], v204 offset:16384\n"
    "s_waitcnt vmcnt(6)\n"
    "v_mfma_f32_16x16x32_bf16 v[36:39], v[164:167], v[140:143], v[36:39]\n"
    "ds_read_b128 v[148:151], v204 offset:18432\n"
    "v_mfma_f32_16x16x32_bf16 v[20:23], v[168:171], v[136:139], v[20:23]\n"
    "buffer_load_dwordx4 v[120:123], v196, s[36:39], s18 offen sc1\n"
    "v_mfma_f32_16x16x32_bf16 v[40:43], v[168:171], v[140:143], v[40:43]\n"
    "ds_read_b128 v[152:155], v204 offset:20480\n"
    "v_mfma_f32_16x16x32_bf16 v[24:27], v[172:175], v[136:139], v[24:27]\n"
    "ds_read_b128 v[156:159], v204 offset:22528\n"
    "v_mfma_f32_16x16x32_bf16 v[44:47], v[172:175], v[140:143], v[44:47]\n"
    "v_mfma_f32_16x16x32_bf16 v[28:31], v[176:179], v[136:139], v[28:31]\n"
    "buffer_load_dwordx4 v[124:127], v197, s[36:39], s18 offen sc1\n"
    "v_mfma_f32_16x16x32_bf16 v[48:51], v[176:179], v[140:143], v[48:51]\n"
    "s_waitcnt vmcnt(7) lgkmcnt(3)\n"
    "v_mfma_f32_16x16x32_bf16 v[16:19], v[144:147], v[96:99], v[16:19]\n"
    "ds_read_b128 v[164:167], v205 offset:16384\n"
    "s_waitcnt vmcnt(6)\n"
    "v_mfma_f32_16x16x32_bf16 v[36:39], v[144:147], v[100:103], v[36:39]\n"
    "ds_read_b128 v[168:171], v205 offset:18432\n"
    "s_waitcnt lgkmcnt(4)\n"
    "v_mfma_f32_16x16x32_bf16 v[20:23], v[148:151], v[96:99], v[20:23]\n"
    "buffer_load_dwordx4 v[128:131], v194, s[36:39], s19 offen sc1\n"
    "v_mfma_f32_16x16x32_bf16 v[40:43], v[148:151], v[100:103], v[40:43]\n"
    "ds_read_b128 v[172:175], v205 offset:20480\n"
    "s_waitcnt lgkmcnt(4)\n"
    "v_mfma_f32_16x16x32_bf16 v[24:27], v[152:155], v[96:99], v[24:27]\n"
    "ds_read_b128 v[176:179], v205 offset:22528\n"
    "v_mfma_f32_16x16x32_bf16 v[44:47], v[152:155], v[100:103], v[44:47]\n"
    "s_waitcnt lgkmcnt(4)\n"
    "v_mfma_f32_16x16x32_bf16 v[28:31], v[156:159], v[96:99], v[28:31]\n"
    "buffer_load_dwordx4 v[132:135], v195, s[36:39], s19 offen sc1\n"
    "v_mfma_f32_16x16x32_bf16 v[48:51], v[156:159], v[100:103], v[48:51]\n"
    "s_waitcnt lgkmcnt(0)\n"
    "s_barrier\n"
    "s_waitcnt vmcnt(7)\n"
    "v_mfma_f32_16x16x32_bf16 v[16:19], v[164:167], v[104:107], v[16:19]\n"
    "ds_read_b128 v[144:147], v204 offset:32768\n"
    "s_waitcnt vmcnt(6)\n"
    "v_mfma_f32_16x16x32_bf16 v[36:39], v[164:167], v[108:111], v[36:39]\n"
    "ds_read_b128 v[148:151], v204 offset:34816\n"
    "v_mfma_f32_16x16x32_bf16 v[20:23], v[168:171], v[104:107], v[20:23]\n"
    "buffer_load_dwordx4 v[136:139], v196, s[36:39], s19 offen sc1\n"
    "v_mfma_f32_16x16x32_bf16 v[40:43], v[168:171], v[108:111], v[40:43]\n"
    "ds_read_b128 v[152:155], v204 offset:36864\n"
    "v_mfma_f32_16x16x32_bf16 v[24:27], v[172:175], v[104:107], v[24:27]\n"
    "ds_read_b128 v[156:159], v204 offset:38912\n"
    "v_mfma_f32_16x16x32_bf16 v[44:47], v[172:175], v[108:111], v[44:47]\n"
    "v_mfma_f32_16x16x32_bf16 v[28:31], v[176:179], v[104:107], v[28:31]\n"
    "buffer_load_dwordx4 v[140:143], v197, s[36:39], s19 offen sc1\n"
    "v_mfma_f32_16x16x32_bf16 v[48:51], v[176:179], v[108:111], v[48:51]\n"
    "s_waitcnt vmcnt(7) lgkmcnt(3)\n"
    "v_mfma_f32_16x16x32_bf16 v[16:19], v[144:147], v[112:115], v[16:19]\n"
    "ds_read_b128 v[164:167], v205 offset:32768\n"
    "s_waitcnt vmcnt(6)\n"
    "v_mfma_f32_16x16x32_bf16 v[36:39], v[144:147], v[116:119], v[36:39]\n"
    "ds_read_b128 v[168:171], v205 offset:34816\n"
    "s_waitcnt lgkmcnt(4)\n"
    "v_mfma_f32_16x16x32_bf16 v[20:23], v[148:151], v[112:115], v[20:23]\n"
    "buffer_load_dwordx4 v[96:99], v194, s[36:39], s20 offen sc1\n"
    "v_mfma_f32_16x16x32_bf16 v[40:43], v[148:151], v[116:119], v[40:43]\n"
    "ds_read_b128 v[172:175], v205 offset:36864\n"
    "s_waitcnt lgkmcnt(4)\n"
    "v_mfma_f32_16x16x32_bf16 v[24:27], v[152:155], v[112:115], v[24:27]\n"
    "ds_read_b128 v[176:179], v205 offset:38912\n"
    "v_mfma_f32_16x16x32_bf16 v[44:47], v[152:155], v[116:119], v[44:47]\n"
    "s_waitcnt lgkmcnt(4)\n"
    "v_mfma_f32_16x16x32_bf16 v[28:31], v[156:159], v[112:115], v[28:31]\n"
    "buffer_load_dwordx4 v[100:103], v195, s[36:39], s20 offen sc1\n"
    "v_mfma_f32_16x16x32_bf16 v[48:51], v[156:159], v[116:119], v[48:51]\n"
    "s_waitcnt lgkmcnt(0)\n"
    "s_barrier\n"
    "s_waitcnt vmcnt(7)\n"
    "v_mfma_f32_16x16x32_bf16 v[16:19], v[164:167], v[120:123], v[16:19]\n"
    "ds_read_b128 v[144:147], v204 offset:0\n"
    "s_waitcnt vmcnt(6)\n"
    "v_mfma_f32_16x16x32_bf16 v[36:39], v[164:167], v[124:127], v[36:39]\n"
    "buffer_load_dwordx4 v[104:107], v196, s[36:39], s20 offen sc1\n"
    "v_mfma_f32_16x16x32_bf16 v[20:23], v[168:171], v[120:123], v[20:23]\n"
    "ds_read_b128 v[148:151], v204 offset:2048\n"
    "v_mfma_f32_16x16x32_bf16 v[40:43], v[168:171], v[124:127], v[40:43]\n"
    "buffer_load_dwordx4 v[108:111], v197, s[36:39], s20 offen sc1\n"
    "v_mfma_f32_16x16x32_bf16 v[24:27], v[172:175], v[120:123], v[24:27]\n"
    "ds_read_b128 v[152:155], v204 offset:4096\n"
    "v_mfma_f32_16x16x32_bf16 v[44:47], v[172:175], v[124:127], v[44:47]\n"
    "buffer_load_dwordx4 v[184:187], v12, s[36:39], s66 offen sc1\n"
    "v_mfma_f32_16x16x32_bf16 v[28:31], v[176:179], v[120:123], v[28:31]\n"
    "ds_read_b128 v[156:159], v204 offset:6144\n"
    "v_mfma_f32_16x16x32_bf16 v[48:51], v[176:179], v[124:127], v[48:51]\n"
    "buffer_load_dwordx4 v[188:191], v12, s[36:39], s68 offen sc1\n"
    "s_waitcnt vmcnt(9) lgkmcnt(3)\n"
    "v_mfma_f32_16x16x32_bf16 v[16:19], v[144:147], v[128:131], v[16:19]\n"
    "ds_read_b128 v[164:167], v205 offset:0\n"
    "s_waitcnt vmcnt(8)\n"
    "v_mfma_f32_16x16x32_bf16 v[36:39], v[144:147], v[132:135], v[36:39]\n"
    "ds_read_b128 v[168:171], v205 offset:2048\n"
    "s_waitcnt lgkmcnt(4)\n"
    "v_mfma_f32_16x16x32_bf16 v[20:23], v[148:151], v[128:131], v[20:23]\n"
    "buffer_load_dwordx4 v[112:115], v194, s[36:39], s21 offen sc1\n"
    "v_mfma_f32_16x16x32_bf16 v[40:43], v[148:151], v[132:135], v[40:43]\n"
    "ds_read_b128 v[172:175], v205 offset:4096\n"
    "s_waitcnt lgkmcnt(4)\n"
    "v_mfma_f32_16x16x32_bf16 v[24:27], v[152:155], v[128:131], v[24:27]\n"
    "ds_read_b128 v[176:179], v205 offset:6144\n"
    "v_mfma_f32_16x16x32_bf16 v[44:47], v[152:155], v[132:135], v[44:47]\n"
    "s_waitcnt lgkmcnt(4)\n"
    "v_mfma_f32_16x16x32_bf16 v[28:31], v[156:159], v[128:131], v[28:31]\n"
    "buffer_load_dwordx4 v[116:119], v195, s[36:39], s21 offen sc1\n"
    "v_mfma_f32_16x16x32_bf16 v[48:51], v[156:159], v[132:135], v[48:51]\n"
    "s_waitcnt lgkmcnt(0)\n"
    "s_barrier\n"
    "s_waitcnt vmcnt(9)\n"
    "v_mfma_f32_16x16x32_bf16 v[16:19], v[164:167], v[136:139], v[16:19]\n"
    "ds_read_b128 v[144:147], v204 offset:16384\n"
    "s_waitcnt vmcnt(8)\n"
    "v_mfma_f32_16x16x32_bf16 v[36:39], v[164:167], v[140:143], v[36:39]\n"
    "ds_read_b128 v[148:151], v204 offset:18432\n"
    "v_mfma_f32_16x16x32_bf16 v[20:23], v[168:171], v[136:139], v[20:23]\n"
    "buffer_load_dwordx4 v[120:123], v196, s[36:39], s21 offen sc1\n"
    "v_mfma_f32_16x16x32_bf16 v[40:43], v[168:171], v[140:143], v[40:43]\n"
    "ds_read_b128 v[152:155], v204 offset:20480\n"
    "v_mfma_f32_16x16x32_bf16 v[24:27], v[172:175], v[136:139], v[24:27]\n"
    "ds_read_b128 v[156:159], v204 offset:22528\n"
    "v_mfma_f32_16x16x32_bf16 v[44:47], v[172:175], v[140:143], v[44:47]\n"
    "v_mfma_f32_16x16x32_bf16 v[28:31], v[176:179], v[136:139], v[28:31]\n"
    "buffer_load_dwordx4 v[124:127], v197, s[36:39], s21 offen sc1\n"
    "v_mfma_f32_16x16x32_bf16 v[48:51], v[176:179], v[140:143], v[48:51]\n"
    "s_waitcnt vmcnt(9) lgkmcnt(3)\n"
    "v_mfma_f32_16x16x32_bf16 v[16:19], v[144:147], v[96:99], v[16:19]\n"
    "ds_read_b128 v[164:167], v205 offset:16384\n"
    "s_waitcnt vmcnt(8)\n"
    "v_mfma_f32_16x16x32_bf16 v[36:39], v[144:147], v[100:103], v[36:39]\n"
    "ds_read_b128 v[168:171], v205 offset:18432\n"
    "s_waitcnt lgkmcnt(4)\n"
    "v_mfma_f32_16x16x32_bf16 v[20:23], v[148:151], v[96:99], v[20:23]\n"
    "buffer_load_dwordx4 v[128:131], v194, s[36:39], s22 offen sc1\n"
    "v_mfma_f32_16x16x32_bf16 v[40:43], v[148:151], v[100:103], v[40:43]\n"
    "ds_read_b128 v[172:175], v205 offset:20480\n"
    "s_waitcnt lgkmcnt(4)\n"
    "v_mfma_f32_16x16x32_bf16 v[24:27], v[152:155], v[96:99], v[24:27]\n"
    "ds_read_b128 v[176:179], v205 offset:22528\n"
    "v_mfma_f32_16x16x32_bf16 v[44:47], v[152:155], v[100:103], v[44:47]\n"
    "s_waitcnt lgkmcnt(4)\n"
    "v_mfma_f32_16x16x32_bf16 v[28:31], v[156:159], v[96:99], v[28:31]\n"
    "buffer_load_dwordx4 v[132:135], v195, s[36:39], s22 offen sc1\n"
    "v_mfma_f32_16x16x32_bf16 v[48:51], v[156:159], v[100:103], v[48:51]\n"
    "s_waitcnt lgkmcnt(0)\n"
    "s_barrier\n"
    "s_waitcnt vmcnt(9)\n"
    "v_mfma_f32_16x16x32_bf16 v[16:19], v[164:167], v[104:107], v[16:19]\n"
    "ds_read_b128 v[144:147], v204 offset:32768\n"
    "s_waitcnt vmcnt(8)\n"
    "v_mfma_f32_16x16x32_bf16 v[36:39], v[164:167], v[108:111], v[36:39]\n"
    "ds_read_b128 v[148:151], v204 offset:34816\n"
    "v_mfma_f32_16x16x32_bf16 v[20:23], v[168:171], v[104:107], v[20:23]\n"
    "buffer_load_dwordx4 v[136:139], v196, s[36:39], s22 offen sc1\n"
    "v_mfma_f32_16x16x32_bf16 v[40:43], v[168:171], v[108:111], v[40:43]\n"
    "ds_read_b128 v[152:155], v204 offset:36864\n"
    "v_mfma_f32_16x16x32_bf16 v[24:27], v[172:175], v[104:107], v[24:27]\n"
    "ds_read_b128 v[156:159], v204 offset:38912\n"
    "v_mfma_f32_16x16x32_bf16 v[44:47], v[172:175], v[108:111], v[44:47]\n"
    "v_add_u32 v11, 154624, v12\n"
    "ds_read_b64 v[192:193], v11\n"
    "v_mfma_f32_16x16x32_bf16 v[28:31], v[176:179], v[104:107], v[28:31]\n"
    "buffer_load_dwordx4 v[140:143], v197, s[36:39], s22 offen sc1\n"
    "v_mfma_f32_16x16x32_bf16 v[48:51], v[176:179], v[108:111], v[48:51]\n"
    "s_waitcnt lgkmcnt(0)\n"
    "v_xor_b32 v192, s34, v192\n"
    "v_xor_b32 v193, s34, v193\n"
    "s_waitcnt vmcnt(9)\n"
    "v_xor_b32 v185, s34, v185\n"
    "v_xor_b32 v187, s34, v187\n"
    "s_waitcnt vmcnt(8)\n"
    "v_xor_b32 v189, s34, v189\n"
    "v_xor_b32 v191, s34, v191\n"
    "v_or3_b32 v192, v192, v193, v185\n"
    "v_or3_b32 v193, v187, v189, v191\n"
    "v_or_b32 v192, v192, v193\n"
    "v_cmp_ne_u32 vcc, 0, v192\n"
    "s_nop 0\n"
    "s_cbranch_vccz .Lo_ready4\n"
    "s_mov_b32 s77, 262144\n"
    ".Lspin7:\n"
    "buffer_load_dwordx2 v[192:193], v7, s[36:39], s76 offen sc1\n"
    "buffer_load_dwordx4 v[184:187], v12, s[36:39], s66 offen sc1\n"
    "buffer_load_dwordx4 v[188:191], v12, s[36:39], s68 offen sc1\n"
    "s_waitcnt vmcnt(0)\n"
    "v_xor_b32 v192, s34, v192\n"
    "v_xor_b32 v193, s34, v193\n"
    "v_xor_b32 v185, s34, v185\n"
    "v_xor_b32 v187, s34, v187\n"
    "v_xor_b32 v189, s34, v189\n"
    "v_xor_b32_e64 v191, s34, v191\n"
    "v_or3_b32 v192, v192, v193, v185\n"
    "v_or3_b32 v193, v187, v189, v191\n"
    "v_or_b32 v192, v192, v193\n"
    "v_cmp_ne_u32 vcc, 0, v192\n"
    "s_nop 0\n"
    "s_cbranch_vccz .Lspun7\n"
    "s_sleep 1\n"
    "s_sub_u32 s77, s77, 1\n"
    "s_cmp_lg_u32 s77, 0\n"
    "s_cbranch_scc1 .Lspin7\n"
    ".Lspun7:\n"
    ".Lo_ready4:\n"
    "buffer_load_dwordx4 v[96:99], v194, s[36:39], s23 offen sc1\n"
    "s_waitcnt vmcnt(8)\n"
    "v_mfma_f32_16x16x32_bf16 v[16:19], v[144:147], v[112:115], v[16:19]\n"
    "ds_read_b128 v[164:167], v205 offset:32768\n"
    "s_waitcnt vmcnt(7)\n"
    "v_mfma_f32_16x16x32_bf16 v[36:39], v[144:147], v[116:119], v[36:39]\n"
    "ds_read_b128 v[168:171], v205 offset:34816\n"
    "v_mfma_f32_16x16x32_bf16 v[20:23], v[148:151], v[112:115], v[20:23]\n"
    "buffer_load_dwordx4 v[100:103], v195, s[36:39], s23 offen sc1\n"
    "v_mfma_f32_16x16x32_bf16 v[40:43], v[148:151], v[116:119], v[40:43]\n"
    "ds_read_b128 v[172:175], v205 offset:36864\n"
    "v_mfma_f32_16x16x32_bf16 v[24:27], v[152:155], v[112:115], v[24:27]\n"
    "ds_read_b128 v[176:179], v205 offset:38912\n"
    "v_mfma_f32_16x16x32_bf16 v[44:47], v[152:155], v[116:119], v[44:47]\n"
    "v_mfma_f32_16x16x32_bf16 v[28:31], v[156:159], v[112:115], v[28:31]\n"
    "buffer_load_dwordx4 v[104:107], v194, s[36:39], s24 offen sc1\n"
    "v_mfma_f32_16x16x32_bf16 v[48:51], v[156:159], v[116:119], v[48:51]\n"
    "s_waitcnt lgkmcnt(0)\n"
    "s_barrier\n"
    "s_waitcnt vmcnt(8)\n"
    "v_mfma_f32_16x16x32_bf16 v[16:19], v[164:167], v[120:123], v[16:19]\n"
    "ds_read_b128 v[144:147], v204 offset:0\n"
    "s_waitcnt vmcnt(7)\n"
    "v_mfma_f32_16x16x32_bf16 v[36:39], v[164:167], v[124:127], v[36:39]\n"
    "ds_read_b128 v[148:151], v204 offset:2048\n"
    "v_mfma_f32_16x16x32_bf16 v[20:23], v[168:171], v[120:123], v[20:23]\n"
    "buffer_load_dwordx4 v[108:111], v195, s[36:39], s24 offen sc1\n"
    "v_mfma_f32_16x16x32_bf16 v[40:43], v[168:171], v[124:127], v[40:43]\n"
    "ds_read_b128 v[152:155], v204 offset:4096\n"
    "v_mfma_f32_16x16x32_bf16 v[24:27], v[172:175], v[120:123], v[24:27]\n"
    "ds_read_b128 v[156:159], v204 offset:6144\n"
    "v_mfma_f32_16x16x32_bf16 v[44:47], v[172:175], v[124:127], v[44:47]\n"
    "v_sub_f32 v185, 1.0, v184\n"
    "v_sub_f32 v189, 1.0, v188\n"
    "v_mfma_f32_16x16x32_bf16 v[28:31], v[176:179], v[120:123], v[28:31]\n"
    "buffer_load_dwordx4 v[216:219], v194, s[36:39], s25 offen sc1\n"
    "v_mfma_f32_16x16x32_bf16 v[48:51], v[176:179], v[124:127], v[48:51]\n"
    "s_waitcnt vmcnt(8) lgkmcnt(3)\n"
    "v_mfma_f32_16x16x32_bf16 v[16:19], v[144:147], v[128:131], v[16:19]\n"
    "ds_read_b128 v[164:167], v205 offset:0\n"
    "s_waitcnt vmcnt(7)\n"
    "v_mfma_f32_16x16x32_bf16 v[36:39], v[144:147], v[132:135], v[36:39]\n"
    "buffer_load_dwordx4 v[220:223], v195, s[36:39], s25 offen sc1\n"
    "s_waitcnt lgkmcnt(3)\n"
    "v_mfma_f32_16x16x32_bf16 v[20:23], v[148:151], v[128:131], v[20:23]\n"
    "ds_read_b128 v[168:171], v205 offset:2048\n"
    "v_mfma_f32_16x16x32_bf16 v[40:43], v[148:151], v[132:135], v[40:43]\n"
    "ds_read_b128 v[172:175], v205 offset:4096\n"
    "s_waitcnt lgkmcnt(4)\n"
    "v_mfma_f32_16x16x32_bf16 v[24:27], v[152:155], v[128:131], v[24:27]\n"
    "buffer_load_dwordx4 v[224:227], v194, s[36:39], s26 offen sc1\n"
    "v_mfma_f32_16x16x32_bf16 v[44:47], v[152:155], v[132:135], v[44:47]\n"
    "ds_read_b128 v[176:179], v205 offset:6144\n"
    "s_waitcnt lgkmcnt(4)\n"
    "v_mfma_f32_16x16x32_bf16 v[28:31], v[156:159], v[128:131], v[28:31]\n"
    "buffer_load_dwordx4 v[228:231], v195, s[36:39], s26 offen sc1\n"
    "v_mfma_f32_16x16x32_bf16 v[48:51], v[156:159], v[132:135], v[48:51]\n"
    "s_waitcnt lgkmcnt(0)\n"
    "s_barrier\n"
    "s_waitcnt vmcnt(9)\n"
    "v_mfma_f32_16x16x32_bf16 v[16:19], v[164:167], v[136:139], v[16:19]\n"
    "ds_read_b64_tr_b16 v[144:145], v200 offset:0\n"
    "s_waitcnt vmcnt(8)\n"
    "v_mfma_f32_16x16x32_bf16 v[36:39], v[164:167], v[140:143], v[36:39]\n"
    "ds_read_b64_tr_b16 v[146:147], v200 offset:1024\n"
    "v_mfma_f32_16x16x32_bf16 v[20:23], v[168:171], v[136:139], v[20:23]\n"
    "ds_read_b64_tr_b16 v[148:149], v201 offset:0\n"
    "buffer_load_dwordx4 v[232:235], v194, s[36:39], s27 offen sc1\n"
    "v_mfma_f32_16x16x32_bf16 v[40:43], v[168:171], v[140:143], v[40:43]\n"
    "ds_read_b64_tr_b16 v[150:151], v201 offset:1024\n"
    "v_mfma_f32_16x16x32_bf16 v[24:27], v[172:175], v[136:139], v[24:27]\n"
    "ds_read_b64_tr_b16 v[152:153], v202 offset:0\n"
    "v_mfma_f32_16x16x32_bf16 v[44:47], v[172:175], v[140:143], v[44:47]\n"
    "ds_read_b64_tr_b16 v[154:155], v202 offset:1024\n"
    "buffer_load_dwordx4 v[236:239], v195, s[36:39], s27 offen sc1\n"
    "v_mfma_f32_16x16x32_bf16 v[28:31], v[176:179], v[136:139], v[28:31]\n"
    "ds_read_b64_tr_b16 v[156:157], v203 offset:0\n"
    "v_mfma_f32_16x16x32_bf16 v[48:51], v[176:179], v[140:143], v[48:51]\n"
    "ds_read_b64_tr_b16 v[158:159], v203 offset:1024\n"
    "s_waitcnt vmcnt(9) lgkmcnt(6)\n"
    "v_mfma_f32_16x16x32_bf16 v[56:59], v[144:147], v[96:99], 0\n"
    "ds_read_b64_tr_b16 v[164:165], v200 offset:8192\n"
    "s_waitcnt vmcnt(8)\n"
    "v_mfma_f32_16x16x32_bf16 v[76:79], v[144:147], v[100:103], 0\n"
    "ds_read_b64_tr_b16 v[166:167], v200 offset:9216\n"
    "buffer_load_dwordx4 v[240:243], v194, s[36:39], s28 offen sc1\n"
    "s_waitcnt lgkmcnt(6)\n"
    "v_mfma_f32_16x16x32_bf16 v[60:63], v[148:151], v[96:99], 0\n"
    "ds_read_b64_tr_b16 v[168:169], v201 offset:8192\n"
    "v_mfma_f32_16x16x32_bf16 v[80:83], v[148:151], v[100:103], 0\n"
    "ds_read_b64_tr_b16 v[170:171], v201 offset:9216\n"
    "s_waitcnt lgkmcnt(6)\n"
    "v_mfma_f32_16x16x32_bf16 v[64:67], v[152:155], v[96:99], 0\n"
    "ds_read_b64_tr_b16 v[172:173], v202 offset:8192\n"
    "v_mfma_f32_16x16x32_bf16 v[84:87], v[152:155], v[100:103], 0\n"
    "ds_read_b64_tr_b16 v[174:175], v202 offset:9216\n"
    "buffer_load_dwordx4 v[244:247], v195, s[36:39], s28 offen sc1\n"
    "s_waitcnt lgkmcnt(6)\n"
    "v_mfma_f32_16x16x32_bf16 v[68:71], v[156:159], v[96:99], 0\n"
    "ds_read_b64_tr_b16 v[176:177], v203 offset:8192\n"
    "v_mfma_f32_16x16x32_bf16 v[88:91], v[156:159], v[100:103], 0\n"
    "ds_read_b64_tr_b16 v[178:179], v203 offset:9216\n"
    "s_waitcnt vmcnt(9) lgkmcnt(6)\n"
    "v_mfma_f32_16x16x32_bf16 v[56:59], v[164:167], v[104:107], v[56:59]\n"
    "ds_read_b64_tr_b16 v[144:145], v200 offset:16384\n"
    "v_mul_f32 v51, v51, v188\n"
    "s_waitcnt vmcnt(8)\n"
    "v_mfma_f32_16x16x32_bf16 v[76:79], v[164:167], v[108:111], v[76:79]\n"
    "ds_read_b64_tr_b16 v[146:147], v200 offset:17408\n"
    "buffer_load_dwordx4 v[112:115], v194, s[36:39], s29 offen sc1\n"
    "s_waitcnt lgkmcnt(6)\n"
    "v_mfma_f32_16x16x32_bf16 v[60:63], v[168:171], v[104:107], v[60:63]\n"
    "ds_read_b64_tr_b16 v[148:149], v201 offset:16384\n"
    "v_mul_f32 v50, v50, v188\n"
    "v_mfma_f32_16x16x32_bf16 v[80:83], v[168:171], v[108:111], v[80:83]\n"
    "ds_read_b64_tr_b16 v[150:151], v201 offset:17408\n"
    "v_mul_f32 v49, v49, v188\n"
    "s_waitcnt lgkmcnt(6)\n"
    "v_mfma_f32_16x16x32_bf16 v[64:67], v[172:175], v[104:107], v[64:67]\n"
    "ds_read_b64_tr_b16 v[152:153], v202 offset:16384\n"
    "v_mul_f32 v48, v48, v188\n"
    "v_mfma_f32_16x16x32_bf16 v[84:87], v[172:175], v[108:111], v[84:87]\n"
    "ds_read_b64_tr_b16 v[154:155], v202 offset:17408\n"
    "buffer_load_dwordx4 v[116:119], v195, s[36:39], s29 offen sc1\n"
    "v_mul_f32 v47, v47, v188\n"
    "s_waitcnt lgkmcnt(6)\n"
    "v_mfma_f32_16x16x32_bf16 v[68:71], v[176:179], v[104:107], v[68:71]\n"
    "ds_read_b64_tr_b16 v[156:157], v203 offset:16384\n"
    "v_mfma_f32_16x16x32_bf16 v[88:91], v[176:179], v[108:111], v[88:91]\n"
    "ds_read_b64_tr_b16 v[158:159], v203 offset:17408\n"
    "v_mul_f32 v46, v46, v188\n"
    "s_waitcnt vmcnt(9) lgkmcnt(6)\n"
    "v_mfma_f32_16x16x32_bf16 v[56:59], v[144:147], v[216:219], v[56:59]\n"
    "ds_read_b64_tr_b16 v[164:165], v200 offset:24576\n"
    "v_mul_f32 v45, v45, v188\n"
    "s_waitcnt vmcnt(8)\n"
    "v_mfma_f32_16x16x32_bf16 v[76:79], v[144:147], v[220:223], v[76:79]\n"
    "ds_read_b64_tr_b16 v[166:167], v200 offset:25600\n"
    "buffer_load_dwordx4 v[120:123], v194, s[36:39], s30 offen sc1\n"
    "s_waitcnt lgkmcnt(6)\n"
    "v_mfma_f32_16x16x32_bf16 v[60:63], v[148:151], v[216:219], v[60:63]\n"
    "ds_read_b64_tr_b16 v[168:169], v201 offset:24576\n"
    "v_mul_f32 v44, v44, v188\n"
    "v_mfma_f32_16x16x32_bf16 v[80:83], v[148:151], v[220:223], v[80:83]\n"
    "ds_read_b64_tr_b16 v[170:171], v201 offset:25600\n"
    "v_mul_f32 v43, v43, v188\n"
    "s_waitcnt lgkmcnt(6)\n"
    "v_mfma_f32_16x16x32_bf16 v[64:67], v[152:155], v[216:219], v[64:67]\n"
    "ds_read_b64_tr_b16 v[172:173], v202 offset:24576\n"
    "v_mul_f32 v42, v42, v188\n"
    "v_mfma_f32_16x16x32_bf16 v[84:87], v[152:155], v[220:223], v[84:87]\n"
    "ds_read_b64_tr_b16 v[174:175], v202 offset:25600\n"
    "buffer_load_dwordx4 v[124:127], v195, s[36:39], s30 offen sc1\n"
    "v_mul_f32 v41, v41, v188\n"
    "s_waitcnt lgkmcnt(6)\n"
    "v_mfma_f32_16x16x32_bf16 v[68:71], v[156:159], v[216:219], v[68:71]\n"
    "ds_read_b64_tr_b16 v[176:177], v203 offset:24576\n"
    "v_mfma_f32_16x16x32_bf16 v[88:91], v[156:159], v[220:223], v[88:91]\n"
    "ds_read_b64_tr_b16 v[178:179], v203 offset:25600\n"
    "v_mul_f32 v40, v40, v188\n"
    "s_waitcnt vmcnt(9) lgkmcnt(6)\n"
    "v_mfma_f32_16x16x32_bf16 v[56:59], v[164:167], v[224:227], v[56:59]\n"
    "ds_read_b64_tr_b16 v[144:145], v200 offset:32768\n"
    "v_mul_f32 v39, v39, v188\n"
    "s_waitcnt vmcnt(8)\n"
    "v_mfma_f32_16x16x32_bf16 v[76:79], v[164:167], v[228:231], v[76:79]\n"
    "ds_read_b64_tr_b16 v[146:147], v200 offset:33792\n"
    "buffer_load_dwordx2 v[128:129], v14, s[36:39], s62 offen offset:0 sc1\n"
    "s_waitcnt lgkmcnt(6)\n"
    "v_mfma_f32_16x16x32_bf16 v[60:63], v[168:171], v[224:227], v[60:63]\n"
    "ds_read_b64_tr_b16 v[148:149], v201 offset:32768\n"
    "v_mul_f32 v38, v38, v188\n"
    "v_mfma_f32_16x16x32_bf16 v[80:83], v[168:171], v[228:231], v[80:83]\n"
    "ds_read_b64_tr_b16 v[150:151], v201 offset:33792\n"
    "v_mul_f32 v37, v37, v188\n"
    "s_waitcnt lgkmcnt(6)\n"
    "v_mfma_f32_16x16x32_bf16 v[64:67], v[172:175], v[224:227], v[64:67]\n"
    "ds_read_b64_tr_b16 v[152:153], v202 offset:32768\n"
    "v_mul_f32 v36, v36, v188\n"
    "v_mfma_f32_16x16x32_bf16 v[84:87], v[172:175], v[228:231], v[84:87]\n"
    "ds_read_b64_tr_b16 v[154:155], v202 offset:33792\n"
    "buffer_load_dwordx2 v[130:131], v14, s[36:39], s62 offen offset:1024 sc1\n"
    "v_mul_f32 v31, v31, v184\n"
    "s_waitcnt lgkmcnt(6)\n"
    "v_mfma_f32_16x16x32_bf16 v[68:71], v[176:179], v[224:227], v[68:71]\n"
    "ds_read_b64_tr_b16 v[156:157], v203 offset:32768\n"
    "v_mfma_f32_16x16x32_bf16 v[88:91], v[176:179], v[228:231], v[88:91]\n"
    "ds_read_b64_tr_b16 v[158:159], v203 offset:33792\n"
    "v_mul_f32 v30, v30, v184\n"
    "s_waitcnt vmcnt(9) lgkmcnt(6)\n"
    "v_mfma_f32_16x16x32_bf16 v[56:59], v[144:147], v[232:235], v[56:59]\n"
    "ds_read_b64_tr_b16 v[164:165], v200 offset:40960\n"
    "v_mul_f32 v29, v29, v184\n"
    "s_waitcnt vmcnt(8)\n"
    "v_mfma_f32_16x16x32_bf16 v[76:79], v[144:147], v[236:239], v[76:79]\n"
    "ds_read_b64_tr_b16 v[166:167], v200 offset:41984\n"
    "v_add_u32 v200, 49152, v200\n"
    "buffer_load_dwordx2 v[132:133], v15, s[36:39], s62 offen offset:0 sc1\n"
    "s_waitcnt lgkmcnt(6)\n"
    "v_mfma_f32_16x16x32_bf16 v[60:63], v[148:151], v[232:235], v[60:63]\n"
    "ds_read_b64_tr_b16 v[168:169], v201 offset:40960\n"
    "v_mul_f32 v28, v28, v184\n"
    "v_mfma_f32_16x16x32_bf16 v[80:83], v[148:151], v[236:239], v[80:83]\n"
    "ds_read_b64_tr_b16 v[170:171], v201 offset:41984\n"
    "v_add_u32 v201, 49152, v201\n"
    "v_mul_f32 v27, v27, v184\n"
    "s_waitcnt lgkmcnt(6)\n"
    "v_mfma_f32_16x16x32_bf16 v[64:67], v[152:155], v[232:235], v[64:67]\n"
    "ds_read_b64_tr_b16 v[172:173], v202 offset:40960\n"
    "v_mul_f32 v26, v26, v184\n"
    "v_mfma_f32_16x16x32_bf16 v[84:87], v[152:155], v[236:239], v[84:87]\n"
    "ds_read_b64_tr_b16 v[174:175], v202 offset:41984\n"
    "v_add_u32 v202, 49152, v202\n"
    "buffer_load_dwordx2 v[134:135], v15, s[36:39], s62 offen offset:1024 sc1\n"
    "v_mul_f32 v25, v25, v184\n"
    "s_waitcnt lgkmcnt(6)\n"
    "v_mfma_f32_16x16x32_bf16 v[68:71], v[156:159], v[232:235], v[68:71]\n"
    "ds_read_b64_tr_b16 v[176:177], v203 offset:40960\n"
    "v_mfma_f32_16x16x32_bf16 v[88:91], v[156:159], v[236:239], v[88:91]\n"
    "ds_read_b64_tr_b16 v[178:179], v203 offset:41984\n"
    "v_add_u32 v203, 49152, v203\n"
    "v_mul_f32 v24, v24, v184\n"
    "s_waitcnt vmcnt(9) lgkmcnt(6)\n"
    "v_mfma_f32_16x16x32_bf16 v[56:59], v[164:167], v[240:243], v[56:59]\n"
    "ds_read_b64_tr_b16 v[144:145], v200 offset:0\n"
    "v_mul_f32 v23, v23, v184\n"
    "s_waitcnt vmcnt(8)\n"
    "v_mfma_f32_16x16x32_bf16 v[76:79], v[164:167], v[244:247], v[76:79]\n"
    "ds_read_b64_tr_b16 v[146:147], v200 offset:1024\n"
    "s_waitcnt lgkmcnt(6)\n"
    "v_mfma_f32_16x16x32_bf16 v[60:63], v[168:171], v[240:243], v[60:63]\n"
    "ds_read_b64_tr_b16 v[148:149], v201 offset:0\n"
    "v_mul_f32 v22, v22, v184\n"
    "v_mfma_f32_16x16x32_bf16 v[80:83], v[168:171], v[244:247], v[80:83]\n"
    "ds_read_b64_tr_b16 v[150:151], v201 offset:1024\n"
    "v_mul_f32 v21, v21, v184\n"
    "s_waitcnt lgkmcnt(6)\n"
    "v_mfma_f32_16x16x32_bf16 v[64:67], v[172:175], v[240:243], v[64:67]\n"
    "ds_read_b64_tr_b16 v[152:153], v202 offset:0\n"
    "v_mul_f32 v20, v20, v184\n"
    "v_mfma_f32_16x16x32_bf16 v[84:87], v[172:175], v[244:247], v[84:87]\n"
    "ds_read_b64_tr_b16 v[154:155], v202 offset:1024\n"
    "v_mul_f32 v19, v19, v184\n"
    "s_waitcnt lgkmcnt(6)\n"
    "v_mfma_f32_16x16x32_bf16 v[68:71], v[176:179], v[240:243], v[68:71]\n"
    "ds_read_b64_tr_b16 v[156:157], v203 offset:0\n"
    "v_mfma_f32_16x16x32_bf16 v[88:91], v[176:179], v[244:247], v[88:91]\n"
    "ds_read_b64_tr_b16 v[158:159], v203 offset:1024\n"
    "v_mul_f32 v18, v18, v184\n"
    "s_waitcnt vmcnt(7) lgkmcnt(6)\n"
    "v_mfma_f32_16x16x32_bf16 v[56:59], v[144:147], v[112:115], v[56:59]\n"
    "ds_read_b64_tr_b16 v[164:165], v200 offset:8192\n"
    "v_mul_f32 v17, v17, v184\n"
    "s_waitcnt vmcnt(6)\n"
    "v_mfma_f32_16x16x32_bf16 v[76:79], v[144:147], v[116:119], v[76:79]\n"
    "ds_read_b64_tr_b16 v[166:167], v200 offset:9216\n"
    "s_waitcnt lgkmcnt(6)\n"
    "v_mfma_f32_16x16x32_bf16 v[60:63], v[148:151], v[112:115], v[60:63]\n"
    "ds_read_b64_tr_b16 v[168:169], v201 offset:8192\n"
    "v_mul_f32 v16, v16, v184\n"
    "v_mfma_f32_16x16x32_bf16 v[80:83], v[148:151], v[116:119], v[80:83]\n"
    "ds_read_b64_tr_b16 v[170:171], v201 offset:9216\n"
    "s_waitcnt lgkmcnt(6)\n"
    "v_mfma_f32_16x16x32_bf16 v[64:67], v[152:155], v[112:115], v[64:67]\n"
    "ds_read_b64_tr_b16 v[172:173], v202 offset:8192\n"
    "v_mfma_f32_16x16x32_bf16 v[84:87], v[152:155], v[116:119], v[84:87]\n"
    "ds_read_b64_tr_b16 v[174:175], v202 offset:9216\n"
    "s_waitcnt lgkmcnt(6)\n"
    "v_mfma_f32_16x16x32_bf16 v[68:71], v[156:159], v[112:115], v[68:71]\n"
    "ds_read_b64_tr_b16 v[176:177], v203 offset:8192\n"
    "v_mfma_f32_16x16x32_bf16 v[88:91], v[156:159], v[116:119], v[88:91]\n"
    "ds_read_b64_tr_b16 v[178:179], v203 offset:9216\n"
    "s_waitcnt vmcnt(5) lgkmcnt(6)\n"
    "v_mfma_f32_16x16x32_bf16 v[56:59], v[164:167], v[120:123], v[56:59]\n"
    "ds_read_b64_tr_b16 v[144:145], v200 offset:16384\n"
    "s_waitcnt vmcnt(4)\n"
    "v_mfma_f32_16x16x32_bf16 v[76:79], v[164:167], v[124:127], v[76:79]\n"
    "ds_read_b64_tr_b16 v[146:147], v200 offset:17408\n"
    "s_waitcnt lgkmcnt(6)\n"
    "v_mfma_f32_16x16x32_bf16 v[60:63], v[168:171], v[120:123], v[60:63]\n"
    "ds_read_b64_tr_b16 v[148:149], v201 offset:16384\n"
    "v_mfma_f32_16x16x32_bf16 v[80:83], v[168:171], v[124:127], v[80:83]\n"
    "ds_read_b64_tr_b16 v[150:151], v201 offset:17408\n"
    "s_waitcnt lgkmcnt(6)\n"
    "v_mfma_f32_16x16x32_bf16 v[64:67], v[172:175], v[120:123], v[64:67]\n"
    "ds_read_b64_tr_b16 v[152:153], v202 offset:16384\n"
    "v_mfma_f32_16x16x32_bf16 v[84:87], v[172:175], v[124:127], v[84:87]\n"
    "ds_read_b64_tr_b16 v[154:155], v202 offset:17408\n"
    "s_waitcnt lgkmcnt(6)\n"
    "v_mfma_f32_16x16x32_bf16 v[68:71], v[176:179], v[120:123], v[68:71]\n"
    "ds_read_b64_tr_b16 v[156:157], v203 offset:16384\n"
    "v_mfma_f32_16x16x32_bf16 v[88:91], v[176:179], v[124:127], v[88:91]\n"
    "ds_read_b64_tr_b16 v[158:159], v203 offset:17408\n"
    "s_waitcnt vmcnt(2) lgkmcnt(6)\n"
    "v_mfma_f32_16x16x32_bf16 v[56:59], v[144:147], v[128:131], v[56:59]\n"
    "s_waitcnt vmcnt(0)\n"
    "v_mfma_f32_16x16x32_bf16 v[76:79], v[144:147], v[132:135], v[76:79]\n"
    "s_waitcnt lgkmcnt(4)\n"
    "v_mfma_f32_16x16x32_bf16 v[60:63], v[148:151], v[128:131], v[60:63]\n"
    "s_nop 3\n"
    "v_fmac_f32 v16, v56, v185\n"
    "v_mfma_f32_16x16x32_bf16 v[80:83], v[148:151], v[132:135], v[80:83]\n"
    "v_fmac_f32 v17, v57, v185\n"
    "v_fmac_f32 v18, v58, v185\n"
    "s_waitcnt lgkmcnt(2)\n"
    "v_mfma_f32_16x16x32_bf16 v[64:67], v[152:155], v[128:131], v[64:67]\n"
    "v_fmac_f32 v19, v59, v185\n"
    "v_fmac_f32 v36, v76, v189\n"
    "v_mfma_f32_16x16x32_bf16 v[84:87], v[152:155], v[132:135], v[84:87]\n"
    "ds_write_b128 v8, v[16:19] offset:0\n"
    "s_waitcnt lgkmcnt(1)\n"
    "v_mfma_f32_16x16x32_bf16 v[68:71], v[156:159], v[128:131], v[68:71]\n"
    "v_fmac_f32 v37, v77, v189\n"
    "v_fmac_f32 v38, v78, v189\n"
    "v_mfma_f32_16x16x32_bf16 v[88:91], v[156:159], v[132:135], v[88:91]\n"
    "v_fmac_f32 v39, v79, v189\n"
    "v_fmac_f32 v20, v60, v185\n"
    "v_fmac_f32 v21, v61, v185\n"
    "ds_write_b128 v9, v[36:39] offset:0\n"
    "v_fmac_f32 v22, v62, v185\n"
    "v_fmac_f32 v23, v63, v185\n"
    "v_fmac_f32 v40, v80, v189\n"
    "v_fmac_f32 v41, v81, v189\n"
    "v_fmac_f32 v42, v82, v189\n"
    "v_fmac_f32_e64 v43, v83, v189\n"
    "v_pk_fma_f32 v[24:25], v[64:65], v[184:185], v[24:25] op_sel:[0,1,0]\n"
    "v_pk_fma_f32 v[26:27], v[66:67], v[184:185], v[26:27] op_sel:[0,1,0]\n"
    "v_pk_fma_f32 v[44:45], v[84:85], v[188:189], v[44:45] op_sel:[0,1,0]\n"
    "ds_write_b128 v8, v[20:23] offset:64\n"
    "v_pk_fma_f32 v[46:47], v[86:87], v[188:189], v[46:47] op_sel:[0,1,0]\n"
    "v_pk_fma_f32 v[28:29], v[68:69], v[184:185], v[28:29] op_sel:[0,1,0]\n"
    "v_pk_fma_f32 v[30:31], v[70:71], v[184:185], v[30:31] op_sel:[0,1,0]\n"
    "v_pk_fma_f32 v[48:49], v[88:89], v[188:189], v[48:49] op_sel:[0,1,0]\n"
    "v_pk_fma_f32 v[50:51], v[90:91], v[188:189], v[50:51] op_sel:[0,1,0]\n"
    "ds_write_b128 v9, v[40:43] offset:64\n"
    "ds_write_b128 v8, v[24:27] offset:128\n"
    "ds_write_b128 v9, v[44:47] offset:128\n"
    "ds_write_b128 v8, v[28:31] offset:192\n"
    "ds_write_b128 v9, v[48:51] offset:192\n"
    "s_waitcnt lgkmcnt(0)\n"
    "s_barrier\n"
    "ds_read_b128 v[16:19], v206\n"
    "ds_read_b128 v[56:59], v206 offset:17408\n"
    "ds_read_b128 v[20:23], v207\n"
    "ds_read_b128 v[60:63], v207 offset:17408\n"
    "ds_read_b128 v[24:27], v208\n"
    "ds_read_b128 v[64:67], v208 offset:17408\n"
    "ds_read_b128 v[28:31], v209\n"
    "ds_read_b128 v[68:71], v209 offset:17408\n"
    "s_waitcnt lgkmcnt(6)\n"
    "v_pk_add_f32 v[16:17], v[16:17], v[56:57]\n"
    "v_pk_add_f32 v[18:19], v[18:19], v[58:59]\n"
    "buffer_store_dwordx4 v[16:19], v211, s[40:43], s69 offen offset:0 sc1\n"
    "s_waitcnt lgkmcnt(4)\n"
    "v_pk_add_f32 v[20:21], v[20:21], v[60:61]\n"
    "v_pk_add_f32 v[22:23], v[22:23], v[62:63]\n"
    "buffer_store_dwordx4 v[20:23], v212, s[40:43], s69 offen offset:0 sc1\n"
    "s_waitcnt lgkmcnt(2)\n"
    "v_pk_add_f32 v[24:25], v[24:25], v[64:65]\n"
    "v_pk_add_f32 v[26:27], v[26:27], v[66:67]\n"
    "buffer_store_dwordx4 v[24:27], v213, s[40:43], s69 offen offset:0 sc1\n"
    "s_waitcnt lgkmcnt(0)\n"
    "v_pk_add_f32 v[28:29], v[28:29], v[68:69]\n"
    "v_pk_add_f32 v[30:31], v[30:31], v[70:71]\n"
    "buffer_store_dwordx4 v[28:31], v214, s[40:43], s69 offen offset:0 sc1\n"
    "s_endpgm\n"
    ".Lc_ssm:\n"
    "s_mul_i32 s60, s57, 48\n"
    "v_min_u32 v140, 47, v0\n"
    "v_add_u32 v141, s60, v140\n"
    "v_lshlrev_b32_e64 v141, 1, v141\n"
    "global_load_ushort v142, v141, s[20:21]\n"
    "global_load_ushort v144, v141, s[16:17]\n"
    "global_load_ushort v145, v141, s[18:19]\n"
    "v_mov_b64 v[16:17], 0\n"
    "v_mov_b64 v[18:19], 0\n"
    "v_mov_b64 v[20:21], 0\n"
    "v_mov_b64 v[22:23], 0\n"
    "v_mov_b64 v[24:25], 0\n"
    "v_mov_b64 v[26:27], 0\n"
    "v_mov_b64 v[28:29], 0\n"
    "v_mov_b64 v[30:31], 0\n"
    "v_mov_b64 v[32:33], 0\n"
    "v_mov_b64 v[34:35], 0\n"
    "v_mov_b64 v[36:37], 0\n"
    "v_mov_b64 v[38:39], 0\n"
    "v_mov_b64 v[40:41], 0\n"
    "v_mov_b64 v[42:43], 0\n"
    "v_mov_b64 v[44:45], 0\n"
    "v_mov_b64 v[46:47], 0\n"
    "v_mov_b64 v[48:49], 0\n"
    "v_mov_b64 v[50:51], 0\n"
    "v_mov_b64 v[52:53], 0\n"
    "v_mov_b64 v[54:55], 0\n"
    "v_mov_b64 v[56:57], 0\n"
    "v_mov_b64 v[58:59], 0\n"
    "v_mov_b64 v[60:61], 0\n"
    "v_mov_b64 v[62:63], 0\n"
    "v_mov_b64 v[64:65], 0\n"
    "v_mov_b64 v[66:67], 0\n"
    "v_mov_b64 v[68:69], 0\n"
    "v_mov_b64 v[70:71], 0\n"
    "v_mov_b64 v[72:73], 0\n"
    "v_mov_b64 v[74:75], 0\n"
    "v_mov_b64 v[76:77], 0\n"
    "v_mov_b64 v[78:79], 0\n"
    "v_mov_b64 v[80:81], 0\n"
    "v_mov_b64 v[82:83], 0\n"
    "v_mov_b64 v[84:85], 0\n"
    "v_mov_b64 v[86:87], 0\n"
    "v_mov_b64 v[88:89], 0\n"
    "v_mov_b64 v[90:91], 0\n"
    "v_mov_b64 v[92:93], 0\n"
    "v_mov_b64 v[94:95], 0\n"
    "v_mov_b64 v[96:97], 0\n"
    "v_mov_b64 v[98:99], 0\n"
    "v_mov_b64 v[100:101], 0\n"
    "v_mov_b64 v[102:103], 0\n"
    "v_mov_b64 v[104:105], 0\n"
    "v_mov_b64 v[106:107], 0\n"
    "v_mov_b64 v[108:109], 0\n"
    "v_mov_b64_e64 v[110:111], 0\n"
    "s_lshl3_add_u32 s63, s57, s55\n"
    "s_mul_i32 s63, s63, 768\n"
    "s_add_u32 s63, s63, 16384\n"
    "v_lshlrev_b32 v236, 4, v140\n"
    "v_mov_b32 v232, 0\n"
    "v_mov_b32 v233, 0\n"
    "v_mov_b32 v234, 0\n"
    "v_mov_b32 v235, 1\n"
    "s_cmp_eq_u32 s35, 1\n"
    "s_cbranch_scc0 .Lpre_s\n"
    "buffer_store_dwordx4 v[232:235], v236, s[36:39], s63 offen sc1\n"
    ".Lpre_s:\n"
    "s_waitcnt vmcnt(0)\n"
    "s_cmp_eq_u32 s35, 0\n"
    "s_cbranch_scc0 .Ls_nolam\n"
    "v_mul_u32_u24 v140, 48, v140\n"
    "v_add_u32 v140, 159744, v140\n"
    "v_lshlrev_b32 v142, 16, v142\n"
    "v_mul_f32 v142, 0x3fb8aa3b, v142\n"
    "v_exp_f32 v142, v142\n"
    "v_lshlrev_b32 v144, 16, v144\n"
    "v_lshlrev_b32 v145, 16, v145\n"
    "v_cvt_pk_bf16_f32 v142, v142, v142\n"
    "v_lshlrev_b32 v142, 16, v142\n"
    "v_mul_f32 v146, v142, v144\n"
    "v_mul_f32 v147, v142, v145\n"
    "v_mul_f32 v146, 0x3fb8aa3b, v146\n"
    "v_exp_f32 v146, v146\n"
    "v_mul_f32 v148, 0x3f22f983, v147\n"
    "v_rndne_f32 v148, v148\n"
    "v_fmac_f32 v147, 0xbfc90000, v148\n"
    "v_fmac_f32 v147, 0xb9fda000, v148\n"
    "v_fmac_f32 v147, 0xb3a22169, v148\n"
    "v_cvt_i32_f32 v148, v148\n"
    "v_mul_f32 v149, v147, v147\n"
    "v_mov_b32 v150, 0x3c08839e\n"
    "v_fmac_f32 v150, 0xb94ca1f9, v149\n"
    "v_fmaak_f32 v150, v150, v149, 0xbe2aaaa3\n"
    "v_mul_f32_e64 v150, v150, v149\n"
    "v_fma_f32 v150, v150, v147, v147\n"
    "v_mov_b32 v151, 0xbab6061a\n"
    "v_fmac_f32 v151, 0x37ccf5ce, v149\n"
    "v_fmaak_f32 v151, v151, v149, 0x3d2aaaa5\n"
    "v_fma_f32 v151, v151, v149, -0.5\n"
    "v_fma_f32 v151, v151, v149, 1.0\n"
    "v_and_b32 v152, 1, v148\n"
    "v_cmp_eq_u32 vcc, 1, v152\n"
    "v_lshlrev_b32 v153, 30, v148\n"
    "v_add_u32 v148, 1, v148\n"
    "v_cndmask_b32 v154, v150, v151, vcc\n"
    "v_cndmask_b32 v155, v151, v150, vcc\n"
    "v_lshlrev_b32_e64 v148, 30, v148\n"
    "v_and_b32 v153, 0x80000000, v153\n"
    "v_and_b32 v148, 0x80000000, v148\n"
    "v_xor_b32 v154, v154, v153\n"
    "v_xor_b32 v155, v155, v148\n"
    "v_mul_f32 v156, v146, v155\n"
    "v_mul_f32 v157, v146, v154\n"
    "v_mul_f32 v150, v144, v144\n"
    "v_fmac_f32 v150, v145, v145\n"
    "v_rcp_f32 v150, v150\n"
    "v_add_f32 v151, -1.0, v156\n"
    "v_mul_f32 v158, v151, v144\n"
    "v_mul_f32 v159, v157, v144\n"
    "v_add_f32 v150, v150, v150\n"
    "v_fmac_f32 v158, v157, v145\n"
    "v_fma_f32 v159, -v151, v145, v159\n"
    "v_mul_f32 v158, v158, v150\n"
    "v_mul_f32 v159, v159, v150\n"
    "ds_write_b128 v140, v[156:159]\n"
    "v_mul_f32 v150, v157, v157\n"
    "v_mul_f32 v151, v156, v157\n"
    "v_fma_f32 v160, v156, v156, -v150\n"
    "v_add_f32 v161, v151, v151\n"
    "v_mul_f32 v150, v161, v161\n"
    "v_mul_f32 v151, v160, v161\n"
    "v_fma_f32 v160, v160, v160, -v150\n"
    "v_add_f32 v161, v151, v151\n"
    "v_mul_f32 v150, v161, v161\n"
    "v_mul_f32 v151, v160, v161\n"
    "v_fma_f32 v160, v160, v160, -v150\n"
    "v_add_f32 v161, v151, v151\n"
    "ds_write_b64 v140, v[160:161] offset:16\n"
    "v_mul_f32 v150, v161, v161\n"
    "v_mul_f32 v151, v160, v161\n"
    "v_fma_f32 v164, v160, v160, -v150\n"
    "v_add_f32 v165, v151, v151\n"
    "v_mul_f32 v150, v165, v165\n"
    "v_mul_f32 v151, v164, v165\n"
    "v_fma_f32 v166, v164, v164, -v150\n"
    "v_add_f32 v167, v151, v151\n"
    "v_mul_f32 v150, v167, v167\n"
    "v_mul_f32 v151, v166, v167\n"
    "v_fma_f32 v166, v166, v166, -v150\n"
    "v_add_f32 v167, v151, v151\n"
    "v_mul_f32 v150, v167, v167\n"
    "v_mul_f32 v151, v166, v167\n"
    "v_fma_f32 v166, v166, v166, -v150\n"
    "v_add_f32 v167, v151, v151\n"
    "v_mul_f32 v150, v167, v167\n"
    "v_mul_f32 v151, v166, v167\n"
    "v_fma_f32 v166, v166, v166, -v150\n"
    "v_add_f32 v167, v151, v151\n"
    "ds_write_b128 v140, v[164:167] offset:32\n"
    "s_waitcnt lgkmcnt(0)\n"
    ".Ls_nolam:\n"
    "v_mul_u32_u24 v14, 48, v4\n"
    "v_add_u32 v14, 159744, v14\n"
    "s_mul_i32 s60, s35, 1536\n"
    "v_lshlrev_b32_e64 v12, 3, v4\n"
    "v_add_u32 v12, 147456, v12\n"
    "v_mul_u32_u24 v11, 384, v3\n"
    "v_add3_u32 v11, v11, v12, s60\n"
    "v_lshlrev_b32 v13, 4, v0\n"
    "v_and_b32 v15, 1, v0\n"
    "v_lshlrev_b32 v15, 4, v15\n"
    "v_mov_b32 v112, 0x3f80\n"
    "v_lshlrev_b32 v15, v15, v112\n"
    "v_lshrrev_b32 v113, 1, v4\n"
    "v_lshlrev_b32 v112, 1, v3\n"
    "v_sub_u32 v113, v113, v112\n"
    "v_cmp_eq_u32 vcc, 0, v113\n"
    "s_nop 1\n"
    "v_cndmask_b32 v112, 0, v15, vcc\n"
    "v_cmp_eq_u32 vcc, 1, v113\n"
    "s_nop 1\n"
    "v_cndmask_b32 v113, 0, v15, vcc\n"
    "s_mov_b32 s60, 3\n"
    "s_waitcnt lgkmcnt(0)\n"
    "s_barrier\n"
    "ds_read_b128 v[184:187], v7 offset:0\n"
    "ds_read_b128 v[188:191], v7 offset:2048\n"
    "ds_read_b128 v[192:195], v7 offset:4096\n"
    "ds_read_b128 v[196:199], v7 offset:6144\n"
    "ds_read_b128 v[128:131], v9 offset:0\n"
    "ds_read_b128 v[132:135], v9 offset:2048\n"
    "ds_read_b128 v[136:139], v9 offset:4096\n"
    "ds_read_b128 v[140:143], v9 offset:6144\n"
    "ds_read_b128 v[144:147], v9 offset:8192\n"
    "ds_read_b128 v[148:151], v9 offset:10240\n"
    "ds_read_b128 v[216:219], v14 offset:0\n"
    "ds_read_b64 v[220:221], v14 offset:16\n"
    "ds_read_b128 v[222:225], v14 offset:768\n"
    "ds_read_b64 v[226:227], v14 offset:784\n"
    "ds_read_b128 v[228:231], v14 offset:1536\n"
    "ds_read_b64 v[232:233], v14 offset:1552\n"
    "s_waitcnt lgkmcnt(0)\n"
    ".Lss_loop:\n"
    "v_mfma_f32_16x16x32_bf16 v[16:19], v[184:187], v[128:131], v[16:19]\n"
    "ds_read_b128 v[200:203], v8 offset:0\n"
    "v_mfma_f32_16x16x32_bf16 v[20:23], v[188:191], v[128:131], v[20:23]\n"
    "ds_read_b128 v[204:207], v8 offset:2048\n"
    "v_mfma_f32_16x16x32_bf16 v[24:27], v[192:195], v[128:131], v[24:27]\n"
    "ds_read_b128 v[208:211], v8 offset:4096\n"
    "v_mfma_f32_16x16x32_bf16 v[28:31], v[196:199], v[128:131], v[28:31]\n"
    "ds_read_b128 v[212:215], v8 offset:6144\n"
    "v_mfma_f32_16x16x32_bf16 v[32:35], v[184:187], v[132:135], v[32:35]\n"
    "ds_read_b128 v[156:159], v10 offset:0\n"
    "v_mfma_f32_16x16x32_bf16 v[36:39], v[188:191], v[132:135], v[36:39]\n"
    "ds_read_b128 v[160:163], v10 offset:2048\n"
    "v_mfma_f32_16x16x32_bf16 v[40:43], v[192:195], v[132:135], v[40:43]\n"
    "ds_read_b128 v[164:167], v10 offset:4096\n"
    "v_mfma_f32_16x16x32_bf16 v[44:47], v[196:199], v[132:135], v[44:47]\n"
    "ds_read_b128 v[168:171], v10 offset:6144\n"
    "v_mfma_f32_16x16x32_bf16 v[48:51], v[184:187], v[136:139], v[48:51]\n"
    "ds_read_b128 v[172:175], v10 offset:8192\n"
    "v_mfma_f32_16x16x32_bf16 v[52:55], v[188:191], v[136:139], v[52:55]\n"
    "ds_read_b128 v[176:179], v10 offset:10240\n"
    "v_mfma_f32_16x16x32_bf16 v[56:59], v[192:195], v[136:139], v[56:59]\n"
    "v_mfma_f32_16x16x32_bf16 v[60:63], v[196:199], v[136:139], v[60:63]\n"
    "v_mfma_f32_16x16x32_bf16 v[64:67], v[184:187], v[140:143], v[64:67]\n"
    "v_mfma_f32_16x16x32_bf16 v[68:71], v[188:191], v[140:143], v[68:71]\n"
    "v_mfma_f32_16x16x32_bf16 v[72:75], v[192:195], v[140:143], v[72:75]\n"
    "v_mfma_f32_16x16x32_bf16 v[76:79], v[196:199], v[140:143], v[76:79]\n"
    "v_mfma_f32_16x16x32_bf16 v[80:83], v[184:187], v[144:147], v[80:83]\n"
    "v_mfma_f32_16x16x32_bf16 v[84:87], v[188:191], v[144:147], v[84:87]\n"
    "v_mfma_f32_16x16x32_bf16 v[88:91], v[192:195], v[144:147], v[88:91]\n"
    "v_mfma_f32_16x16x32_bf16 v[92:95], v[196:199], v[144:147], v[92:95]\n"
    "v_mfma_f32_16x16x32_bf16 v[96:99], v[184:187], v[148:151], v[96:99]\n"
    "v_mfma_f32_16x16x32_bf16 v[100:103], v[188:191], v[148:151], v[100:103]\n"
    "v_mfma_f32_16x16x32_bf16 v[104:107], v[192:195], v[148:151], v[104:107]\n"
    "v_mfma_f32_16x16x32_bf16 v[108:111], v[196:199], v[148:151], v[108:111]\n"
    "s_waitcnt lgkmcnt(0)\n"
    "s_barrier\n"
    "v_mfma_f32_16x16x32_bf16 v[16:19], v[200:203], v[156:159], v[16:19]\n"
    "ds_read_b128 v[184:187], v7 offset:8192\n"
    "v_mfma_f32_16x16x32_bf16 v[20:23], v[204:207], v[156:159], v[20:23]\n"
    "ds_read_b128 v[188:191], v7 offset:10240\n"
    "v_mfma_f32_16x16x32_bf16 v[24:27], v[208:211], v[156:159], v[24:27]\n"
    "ds_read_b128 v[192:195], v7 offset:12288\n"
    "v_mfma_f32_16x16x32_bf16 v[28:31], v[212:215], v[156:159], v[28:31]\n"
    "ds_read_b128 v[196:199], v7 offset:14336\n"
    "v_mfma_f32_16x16x32_bf16 v[32:35], v[200:203], v[160:163], v[32:35]\n"
    "ds_read_b128 v[128:131], v9 offset:16384\n"
    "v_mfma_f32_16x16x32_bf16 v[36:39], v[204:207], v[160:163], v[36:39]\n"
    "ds_read_b128 v[132:135], v9 offset:18432\n"
    "v_mfma_f32_16x16x32_bf16 v[40:43], v[208:211], v[160:163], v[40:43]\n"
    "ds_read_b128 v[136:139], v9 offset:20480\n"
    "v_mfma_f32_16x16x32_bf16 v[44:47], v[212:215], v[160:163], v[44:47]\n"
    "ds_read_b128 v[140:143], v9 offset:22528\n"
    "v_mfma_f32_16x16x32_bf16 v[48:51], v[200:203], v[164:167], v[48:51]\n"
    "ds_read_b128 v[144:147], v9 offset:24576\n"
    "v_mfma_f32_16x16x32_bf16 v[52:55], v[204:207], v[164:167], v[52:55]\n"
    "ds_read_b128 v[148:151], v9 offset:26624\n"
    "v_mfma_f32_16x16x32_bf16 v[56:59], v[208:211], v[164:167], v[56:59]\n"
    "v_mfma_f32_16x16x32_bf16 v[60:63], v[212:215], v[164:167], v[60:63]\n"
    "v_mfma_f32_16x16x32_bf16 v[64:67], v[200:203], v[168:171], v[64:67]\n"
    "v_mfma_f32_16x16x32_bf16 v[68:71], v[204:207], v[168:171], v[68:71]\n"
    "v_mfma_f32_16x16x32_bf16 v[72:75], v[208:211], v[168:171], v[72:75]\n"
    "v_mfma_f32_16x16x32_bf16 v[76:79], v[212:215], v[168:171], v[76:79]\n"
    "v_mfma_f32_16x16x32_bf16 v[80:83], v[200:203], v[172:175], v[80:83]\n"
    "v_mfma_f32_16x16x32_bf16 v[84:87], v[204:207], v[172:175], v[84:87]\n"
    "v_mfma_f32_16x16x32_bf16 v[88:91], v[208:211], v[172:175], v[88:91]\n"
    "v_mfma_f32_16x16x32_bf16 v[92:95], v[212:215], v[172:175], v[92:95]\n"
    "v_mfma_f32_16x16x32_bf16 v[96:99], v[200:203], v[176:179], v[96:99]\n"
    "v_mfma_f32_16x16x32_bf16 v[100:103], v[204:207], v[176:179], v[100:103]\n"
    "v_mfma_f32_16x16x32_bf16 v[104:107], v[208:211], v[176:179], v[104:107]\n"
    "v_mfma_f32_16x16x32_bf16 v[108:111], v[212:215], v[176:179], v[108:111]\n"
    "s_waitcnt lgkmcnt(0)\n"
    "v_mfma_f32_16x16x32_bf16 v[16:19], v[184:187], v[128:131], v[16:19]\n"
    "ds_read_b128 v[200:203], v8 offset:8192\n"
    "v_mfma_f32_16x16x32_bf16 v[20:23], v[188:191], v[128:131], v[20:23]\n"
    "ds_read_b128 v[204:207], v8 offset:10240\n"
    "v_mfma_f32_16x16x32_bf16 v[24:27], v[192:195], v[128:131], v[24:27]\n"
    "ds_read_b128 v[208:211], v8 offset:12288\n"
    "v_mfma_f32_16x16x32_bf16 v[28:31], v[196:199], v[128:131], v[28:31]\n"
    "ds_read_b128 v[212:215], v8 offset:14336\n"
    "v_mfma_f32_16x16x32_bf16 v[32:35], v[184:187], v[132:135], v[32:35]\n"
    "ds_read_b128 v[156:159], v10 offset:16384\n"
    "v_mfma_f32_16x16x32_bf16 v[36:39], v[188:191], v[132:135], v[36:39]\n"
    "ds_read_b128 v[160:163], v10 offset:18432\n"
    "v_mfma_f32_16x16x32_bf16 v[40:43], v[192:195], v[132:135], v[40:43]\n"
    "ds_read_b128 v[164:167], v10 offset:20480\n"
    "v_mfma_f32_16x16x32_bf16 v[44:47], v[196:199], v[132:135], v[44:47]\n"
    "ds_read_b128 v[168:171], v10 offset:22528\n"
    "v_mfma_f32_16x16x32_bf16 v[48:51], v[184:187], v[136:139], v[48:51]\n"
    "ds_read_b128 v[172:175], v10 offset:24576\n"
    "v_mfma_f32_16x16x32_bf16 v[52:55], v[188:191], v[136:139], v[52:55]\n"
    "ds_read_b128 v[176:179], v10 offset:26624\n"
    "v_mfma_f32_16x16x32_bf16 v[56:59], v[192:195], v[136:139], v[56:59]\n"
    "v_mfma_f32_16x16x32_bf16 v[60:63], v[196:199], v[136:139], v[60:63]\n"
    "v_mfma_f32_16x16x32_bf16 v[64:67], v[184:187], v[140:143], v[64:67]\n"
    "v_mfma_f32_16x16x32_bf16 v[68:71], v[188:191], v[140:143], v[68:71]\n"
    "v_mfma_f32_16x16x32_bf16 v[72:75], v[192:195], v[140:143], v[72:75]\n"
    "v_mfma_f32_16x16x32_bf16 v[76:79], v[196:199], v[140:143], v[76:79]\n"
    "v_mfma_f32_16x16x32_bf16 v[80:83], v[184:187], v[144:147], v[80:83]\n"
    "v_mfma_f32_16x16x32_bf16 v[84:87], v[188:191], v[144:147], v[84:87]\n"
    "v_mfma_f32_16x16x32_bf16 v[88:91], v[192:195], v[144:147], v[88:91]\n"
    "v_mfma_f32_16x16x32_bf16 v[92:95], v[196:199], v[144:147], v[92:95]\n"
    "v_mfma_f32_16x16x32_bf16 v[96:99], v[184:187], v[148:151], v[96:99]\n"
    "v_mfma_f32_16x16x32_bf16 v[100:103], v[188:191], v[148:151], v[100:103]\n"
    "v_mfma_f32_16x16x32_bf16 v[104:107], v[192:195], v[148:151], v[104:107]\n"
    "v_mfma_f32_16x16x32_bf16 v[108:111], v[196:199], v[148:151], v[108:111]\n"
    "s_waitcnt lgkmcnt(0)\n"
    "s_barrier\n"
    "v_mfma_f32_16x16x32_bf16 v[16:19], v[200:203], v[156:159], v[16:19]\n"
    "ds_read_b128 v[184:187], v7 offset:16384\n"
    "v_mfma_f32_16x16x32_bf16 v[20:23], v[204:207], v[156:159], v[20:23]\n"
    "ds_read_b128 v[188:191], v7 offset:18432\n"
    "v_mfma_f32_16x16x32_bf16 v[24:27], v[208:211], v[156:159], v[24:27]\n"
    "ds_read_b128 v[192:195], v7 offset:20480\n"
    "v_mfma_f32_16x16x32_bf16 v[28:31], v[212:215], v[156:159], v[28:31]\n"
    "ds_read_b128 v[196:199], v7 offset:22528\n"
    "v_mfma_f32_16x16x32_bf16 v[32:35], v[200:203], v[160:163], v[32:35]\n"
    "ds_read_b128 v[128:131], v9 offset:32768\n"
    "v_mfma_f32_16x16x32_bf16 v[36:39], v[204:207], v[160:163], v[36:39]\n"
    "ds_read_b128 v[132:135], v9 offset:34816\n"
    "v_mfma_f32_16x16x32_bf16 v[40:43], v[208:211], v[160:163], v[40:43]\n"
    "ds_read_b128 v[136:139], v9 offset:36864\n"
    "v_mfma_f32_16x16x32_bf16 v[44:47], v[212:215], v[160:163], v[44:47]\n"
    "ds_read_b128 v[140:143], v9 offset:38912\n"
    "v_mfma_f32_16x16x32_bf16 v[48:51], v[200:203], v[164:167], v[48:51]\n"
    "ds_read_b128 v[144:147], v9 offset:40960\n"
    "v_mfma_f32_16x16x32_bf16 v[52:55], v[204:207], v[164:167], v[52:55]\n"
    "ds_read_b128 v[148:151], v9 offset:43008\n"
    "v_mfma_f32_16x16x32_bf16 v[56:59], v[208:211], v[164:167], v[56:59]\n"
    "v_mfma_f32_16x16x32_bf16 v[60:63], v[212:215], v[164:167], v[60:63]\n"
    "v_mfma_f32_16x16x32_bf16 v[64:67], v[200:203], v[168:171], v[64:67]\n"
    "v_mfma_f32_16x16x32_bf16 v[68:71], v[204:207], v[168:171], v[68:71]\n"
    "v_mfma_f32_16x16x32_bf16 v[72:75], v[208:211], v[168:171], v[72:75]\n"
    "v_mfma_f32_16x16x32_bf16 v[76:79], v[212:215], v[168:171], v[76:79]\n"
    "v_mfma_f32_16x16x32_bf16 v[80:83], v[200:203], v[172:175], v[80:83]\n"
    "v_mfma_f32_16x16x32_bf16 v[84:87], v[204:207], v[172:175], v[84:87]\n"
    "v_mfma_f32_16x16x32_bf16 v[88:91], v[208:211], v[172:175], v[88:91]\n"
    "v_mfma_f32_16x16x32_bf16 v[92:95], v[212:215], v[172:175], v[92:95]\n"
    "v_mfma_f32_16x16x32_bf16 v[96:99], v[200:203], v[176:179], v[96:99]\n"
    "v_mfma_f32_16x16x32_bf16 v[100:103], v[204:207], v[176:179], v[100:103]\n"
    "v_mfma_f32_16x16x32_bf16 v[104:107], v[208:211], v[176:179], v[104:107]\n"
    "v_mfma_f32_16x16x32_bf16 v[108:111], v[212:215], v[176:179], v[108:111]\n"
    "s_waitcnt lgkmcnt(0)\n"
    "v_mfma_f32_16x16x32_bf16 v[16:19], v[184:187], v[128:131], v[16:19]\n"
    "ds_read_b128 v[200:203], v8 offset:16384\n"
    "v_mfma_f32_16x16x32_bf16 v[20:23], v[188:191], v[128:131], v[20:23]\n"
    "ds_read_b128 v[204:207], v8 offset:18432\n"
    "v_mfma_f32_16x16x32_bf16 v[24:27], v[192:195], v[128:131], v[24:27]\n"
    "ds_read_b128 v[208:211], v8 offset:20480\n"
    "v_mfma_f32_16x16x32_bf16 v[28:31], v[196:199], v[128:131], v[28:31]\n"
    "ds_read_b128 v[212:215], v8 offset:22528\n"
    "v_mfma_f32_16x16x32_bf16 v[32:35], v[184:187], v[132:135], v[32:35]\n"
    "ds_read_b128 v[156:159], v10 offset:32768\n"
    "v_mfma_f32_16x16x32_bf16 v[36:39], v[188:191], v[132:135], v[36:39]\n"
    "ds_read_b128 v[160:163], v10 offset:34816\n"
    "v_mfma_f32_16x16x32_bf16 v[40:43], v[192:195], v[132:135], v[40:43]\n"
    "ds_read_b128 v[164:167], v10 offset:36864\n"
    "v_mfma_f32_16x16x32_bf16 v[44:47], v[196:199], v[132:135], v[44:47]\n"
    "ds_read_b128 v[168:171], v10 offset:38912\n"
    "v_mfma_f32_16x16x32_bf16 v[48:51], v[184:187], v[136:139], v[48:51]\n"
    "ds_read_b128 v[172:175], v10 offset:40960\n"
    "v_mfma_f32_16x16x32_bf16 v[52:55], v[188:191], v[136:139], v[52:55]\n"
    "ds_read_b128 v[176:179], v10 offset:43008\n"
    "v_mfma_f32_16x16x32_bf16 v[56:59], v[192:195], v[136:139], v[56:59]\n"
    "v_mfma_f32_16x16x32_bf16 v[60:63], v[196:199], v[136:139], v[60:63]\n"
    "v_mfma_f32_16x16x32_bf16 v[64:67], v[184:187], v[140:143], v[64:67]\n"
    "v_mfma_f32_16x16x32_bf16 v[68:71], v[188:191], v[140:143], v[68:71]\n"
    "v_mfma_f32_16x16x32_bf16 v[72:75], v[192:195], v[140:143], v[72:75]\n"
    "v_mfma_f32_16x16x32_bf16 v[76:79], v[196:199], v[140:143], v[76:79]\n"
    "v_mfma_f32_16x16x32_bf16 v[80:83], v[184:187], v[144:147], v[80:83]\n"
    "v_mfma_f32_16x16x32_bf16 v[84:87], v[188:191], v[144:147], v[84:87]\n"
    "v_mfma_f32_16x16x32_bf16 v[88:91], v[192:195], v[144:147], v[88:91]\n"
    "v_mfma_f32_16x16x32_bf16 v[92:95], v[196:199], v[144:147], v[92:95]\n"
    "v_mfma_f32_16x16x32_bf16 v[96:99], v[184:187], v[148:151], v[96:99]\n"
    "v_mfma_f32_16x16x32_bf16 v[100:103], v[188:191], v[148:151], v[100:103]\n"
    "v_mfma_f32_16x16x32_bf16 v[104:107], v[192:195], v[148:151], v[104:107]\n"
    "v_mfma_f32_16x16x32_bf16 v[108:111], v[196:199], v[148:151], v[108:111]\n"
    "s_waitcnt lgkmcnt(0)\n"
    "s_barrier\n"
    "v_mfma_f32_16x16x32_bf16 v[16:19], v[200:203], v[156:159], v[16:19]\n"
    "ds_read_b128 v[184:187], v7 offset:0\n"
    "v_mfma_f32_16x16x32_bf16 v[20:23], v[204:207], v[156:159], v[20:23]\n"
    "ds_read_b128 v[188:191], v7 offset:2048\n"
    "v_mfma_f32_16x16x32_bf16 v[24:27], v[208:211], v[156:159], v[24:27]\n"
    "ds_read_b128 v[192:195], v7 offset:4096\n"
    "v_mfma_f32_16x16x32_bf16 v[28:31], v[212:215], v[156:159], v[28:31]\n"
    "ds_read_b128 v[196:199], v7 offset:6144\n"
    "v_mfma_f32_16x16x32_bf16 v[32:35], v[200:203], v[160:163], v[32:35]\n"
    "ds_read_b128 v[128:131], v9 offset:0\n"
    "v_mfma_f32_16x16x32_bf16 v[36:39], v[204:207], v[160:163], v[36:39]\n"
    "ds_read_b128 v[132:135], v9 offset:2048\n"
    "v_mfma_f32_16x16x32_bf16 v[40:43], v[208:211], v[160:163], v[40:43]\n"
    "ds_read_b128 v[136:139], v9 offset:4096\n"
    "v_mfma_f32_16x16x32_bf16 v[44:47], v[212:215], v[160:163], v[44:47]\n"
    "ds_read_b128 v[140:143], v9 offset:6144\n"
    "v_mfma_f32_16x16x32_bf16 v[48:51], v[200:203], v[164:167], v[48:51]\n"
    "ds_read_b128 v[144:147], v9 offset:8192\n"
    "v_mfma_f32_16x16x32_bf16 v[52:55], v[204:207], v[164:167], v[52:55]\n"
    "ds_read_b128 v[148:151], v9 offset:10240\n"
    "v_mfma_f32_16x16x32_bf16 v[56:59], v[208:211], v[164:167], v[56:59]\n"
    "v_mfma_f32_16x16x32_bf16 v[60:63], v[212:215], v[164:167], v[60:63]\n"
    "v_mfma_f32_16x16x32_bf16 v[64:67], v[200:203], v[168:171], v[64:67]\n"
    "v_mfma_f32_16x16x32_bf16 v[68:71], v[204:207], v[168:171], v[68:71]\n"
    "v_mfma_f32_16x16x32_bf16 v[72:75], v[208:211], v[168:171], v[72:75]\n"
    "v_mfma_f32_16x16x32_bf16 v[76:79], v[212:215], v[168:171], v[76:79]\n"
    "v_mfma_f32_16x16x32_bf16 v[80:83], v[200:203], v[172:175], v[80:83]\n"
    "v_mfma_f32_16x16x32_bf16 v[84:87], v[204:207], v[172:175], v[84:87]\n"
    "v_mfma_f32_16x16x32_bf16 v[88:91], v[208:211], v[172:175], v[88:91]\n"
    "v_mfma_f32_16x16x32_bf16 v[92:95], v[212:215], v[172:175], v[92:95]\n"
    "v_mfma_f32_16x16x32_bf16 v[96:99], v[200:203], v[176:179], v[96:99]\n"
    "v_mfma_f32_16x16x32_bf16 v[100:103], v[204:207], v[176:179], v[100:103]\n"
    "v_mfma_f32_16x16x32_bf16 v[104:107], v[208:211], v[176:179], v[104:107]\n"
    "v_mfma_f32_16x16x32_bf16 v[108:111], v[212:215], v[176:179], v[108:111]\n"
    "s_sub_u32 s60, s60, 1\n"
    "s_cmp_lg_u32 s60, 0\n"
    "s_waitcnt lgkmcnt(0)\n"
    "s_cbranch_scc1 .Lss_loop\n"
    "s_waitcnt vmcnt(0)\n"
    "v_readfirstlane_b32 s34, v253\n"
    "s_nop 0\n"
    "s_add_u32 s34, s34, 0x9E3779B9\n"
    "v_pk_fma_f32 v[156:157], v[216:217], v[64:65], v[18:19] op_sel:[1,0,0] neg_lo:[1,0,0] neg_hi:[1,0,0]\n"
    "v_pk_fma_f32 v[158:159], v[216:217], v[16:17], v[66:67] op_sel:[1,0,0]\n"
    "v_pk_fma_f32 v[152:153], v[216:217], v[16:17], v[156:157] op_sel_hi:[0,1,1]\n"
    "v_pk_fma_f32 v[154:155], v[216:217], v[64:65], v[158:159] op_sel_hi:[0,1,1]\n"
    "v_pk_fma_f32 v[158:159], v[216:217], v[152:153], v[68:69] op_sel:[1,0,0]\n"
    "v_pk_fma_f32 v[156:157], v[216:217], v[154:155], v[20:21] op_sel:[1,0,0] neg_lo:[1,0,0] neg_hi:[1,0,0]\n"
    "v_pk_fma_f32 v[154:155], v[216:217], v[154:155], v[158:159] op_sel_hi:[0,1,1]\n"
    "v_pk_fma_f32 v[152:153], v[216:217], v[152:153], v[156:157] op_sel_hi:[0,1,1]\n"
    "v_pk_fma_f32 v[156:157], v[216:217], v[154:155], v[22:23] op_sel:[1,0,0] neg_lo:[1,0,0] neg_hi:[1,0,0]\n"
    "v_pk_fma_f32 v[158:159], v[216:217], v[152:153], v[70:71] op_sel:[1,0,0]\n"
    "v_pk_fma_f32 v[152:153], v[216:217], v[152:153], v[156:157] op_sel_hi:[0,1,1]\n"
    "v_pk_fma_f32 v[154:155], v[216:217], v[154:155], v[158:159] op_sel_hi:[0,1,1]\n"
    "v_pk_fma_f32 v[158:159], v[216:217], v[152:153], v[72:73] op_sel:[1,0,0]\n"
    "v_pk_fma_f32 v[156:157], v[216:217], v[154:155], v[24:25] op_sel:[1,0,0] neg_lo:[1,0,0] neg_hi:[1,0,0]\n"
    "v_pk_fma_f32 v[154:155], v[216:217], v[154:155], v[158:159] op_sel_hi:[0,1,1]\n"
    "v_pk_fma_f32 v[152:153], v[216:217], v[152:153], v[156:157] op_sel_hi:[0,1,1]\n"
    "v_pk_fma_f32 v[156:157], v[216:217], v[154:155], v[26:27] op_sel:[1,0,0] neg_lo:[1,0,0] neg_hi:[1,0,0]\n"
    "v_pk_fma_f32 v[158:159], v[216:217], v[152:153], v[74:75] op_sel:[1,0,0]\n"
    "v_pk_fma_f32 v[152:153], v[216:217], v[152:153], v[156:157] op_sel_hi:[0,1,1]\n"
    "v_pk_fma_f32 v[154:155], v[216:217], v[154:155], v[158:159] op_sel_hi:[0,1,1]\n"
    "v_pk_fma_f32 v[158:159], v[216:217], v[152:153], v[76:77] op_sel:[1,0,0]\n"
    "v_pk_fma_f32 v[156:157], v[216:217], v[154:155], v[28:29] op_sel:[1,0,0] neg_lo:[1,0,0] neg_hi:[1,0,0]\n"
    "v_pk_fma_f32 v[154:155], v[216:217], v[154:155], v[158:159] op_sel_hi:[0,1,1]\n"
    "v_pk_fma_f32 v[152:153], v[216:217], v[152:153], v[156:157] op_sel_hi:[0,1,1]\n"
    "v_pk_fma_f32 v[156:157], v[216:217], v[154:155], v[30:31] op_sel:[1,0,0] neg_lo:[1,0,0] neg_hi:[1,0,0]\n"
    "v_pk_fma_f32 v[158:159], v[216:217], v[152:153], v[78:79] op_sel:[1,0,0]\n"
    "v_pk_fma_f32 v[152:153], v[216:217], v[152:153], v[156:157] op_sel_hi:[0,1,1]\n"
    "v_pk_fma_f32 v[154:155], v[216:217], v[154:155], v[158:159] op_sel_hi:[0,1,1]\n"
    "v_pk_fma_f32 v[164:165], v[222:223], v[80:81], v[34:35] op_sel:[1,0,0] neg_lo:[1,0,0] neg_hi:[1,0,0]\n"
    "v_fma_f32 v156, -v221, v154, v153\n"
    "v_fma_f32 v157, v221, v152, v155\n"
    "v_fmac_f32 v156, v220, v152\n"
    "v_fmac_f32 v157, v220, v154\n"
    "v_pk_fma_f32 v[166:167], v[222:223], v[32:33], v[82:83] op_sel:[1,0,0]\n"
    "ds_write_b64 v11, v[156:157] offset:0\n"
    "v_pk_fma_f32 v[160:161], v[222:223], v[32:33], v[164:165] op_sel_hi:[0,1,1]\n"
    "v_pk_fma_f32 v[162:163], v[222:223], v[80:81], v[166:167] op_sel_hi:[0,1,1]\n"
    "v_pk_fma_f32 v[166:167], v[222:223], v[160:161], v[84:85] op_sel:[1,0,0]\n"
    "v_pk_fma_f32 v[164:165], v[222:223], v[162:163], v[36:37] op_sel:[1,0,0] neg_lo:[1,0,0] neg_hi:[1,0,0]\n"
    "v_pk_fma_f32 v[162:163], v[222:223], v[162:163], v[166:167] op_sel_hi:[0,1,1]\n"
    "v_pk_fma_f32 v[160:161], v[222:223], v[160:161], v[164:165] op_sel_hi:[0,1,1]\n"
    "v_pk_fma_f32 v[164:165], v[222:223], v[162:163], v[38:39] op_sel:[1,0,0] neg_lo:[1,0,0] neg_hi:[1,0,0]\n"
    "v_pk_fma_f32 v[166:167], v[222:223], v[160:161], v[86:87] op_sel:[1,0,0]\n"
    "v_pk_fma_f32 v[160:161], v[222:223], v[160:161], v[164:165] op_sel_hi:[0,1,1]\n"
    "v_pk_fma_f32 v[162:163], v[222:223], v[162:163], v[166:167] op_sel_hi:[0,1,1]\n"
    "v_pk_fma_f32 v[166:167], v[222:223], v[160:161], v[88:89] op_sel:[1,0,0]\n"
    "v_pk_fma_f32 v[164:165], v[222:223], v[162:163], v[40:41] op_sel:[1,0,0] neg_lo:[1,0,0] neg_hi:[1,0,0]\n"
    "v_pk_fma_f32 v[162:163], v[222:223], v[162:163], v[166:167] op_sel_hi:[0,1,1]\n"
    "v_pk_fma_f32 v[160:161], v[222:223], v[160:161], v[164:165] op_sel_hi:[0,1,1]\n"
    "v_pk_fma_f32 v[164:165], v[222:223], v[162:163], v[42:43] op_sel:[1,0,0] neg_lo:[1,0,0] neg_hi:[1,0,0]\n"
    "v_pk_fma_f32 v[166:167], v[222:223], v[160:161], v[90:91] op_sel:[1,0,0]\n"
    "v_pk_fma_f32 v[160:161], v[222:223], v[160:161], v[164:165] op_sel_hi:[0,1,1]\n"
    "v_pk_fma_f32 v[162:163], v[222:223], v[162:163], v[166:167] op_sel_hi:[0,1,1]\n"
    "v_pk_fma_f32 v[166:167], v[222:223], v[160:161], v[92:93] op_sel:[1,0,0]\n"
    "v_pk_fma_f32 v[164:165], v[222:223], v[162:163], v[44:45] op_sel:[1,0,0] neg_lo:[1,0,0] neg_hi:[1,0,0]\n"
    "v_pk_fma_f32 v[162:163], v[222:223], v[162:163], v[166:167] op_sel_hi:[0,1,1]\n"
    "v_pk_fma_f32 v[160:161], v[222:223], v[160:161], v[164:165] op_sel_hi:[0,1,1]\n"
    "v_pk_fma_f32 v[164:165], v[222:223], v[162:163], v[46:47] op_sel:[1,0,0] neg_lo:[1,0,0] neg_hi:[1,0,0]\n"
    "v_pk_fma_f32 v[166:167], v[222:223], v[160:161], v[94:95] op_sel:[1,0,0]\n"
    "v_pk_fma_f32 v[160:161], v[222:223], v[160:161], v[164:165] op_sel_hi:[0,1,1]\n"
    "v_pk_fma_f32 v[162:163], v[222:223], v[162:163], v[166:167] op_sel_hi:[0,1,1]\n"
    "v_pk_fma_f32 v[172:173], v[228:229], v[96:97], v[50:51] op_sel:[1,0,0] neg_lo:[1,0,0] neg_hi:[1,0,0]\n"
    "v_fma_f32 v164, -v227, v162, v161\n"
    "v_fma_f32 v165, v227, v160, v163\n"
    "v_fmac_f32 v164, v226, v160\n"
    "v_fmac_f32 v165, v226, v162\n"
    "v_pk_fma_f32 v[174:175], v[228:229], v[48:49], v[98:99] op_sel:[1,0,0]\n"
    "ds_write_b64 v11, v[164:165] offset:128\n"
    "v_pk_fma_f32 v[168:169], v[228:229], v[48:49], v[172:173] op_sel_hi:[0,1,1]\n"
    "v_pk_fma_f32 v[170:171], v[228:229], v[96:97], v[174:175] op_sel_hi:[0,1,1]\n"
    "v_pk_fma_f32 v[174:175], v[228:229], v[168:169], v[100:101] op_sel:[1,0,0]\n"
    "v_pk_fma_f32 v[172:173], v[228:229], v[170:171], v[52:53] op_sel:[1,0,0] neg_lo:[1,0,0] neg_hi:[1,0,0]\n"
    "v_pk_fma_f32 v[170:171], v[228:229], v[170:171], v[174:175] op_sel_hi:[0,1,1]\n"
    "v_pk_fma_f32 v[168:169], v[228:229], v[168:169], v[172:173] op_sel_hi:[0,1,1]\n"
    "v_pk_fma_f32 v[172:173], v[228:229], v[170:171], v[54:55] op_sel:[1,0,0] neg_lo:[1,0,0] neg_hi:[1,0,0]\n"
    "v_pk_fma_f32 v[174:175], v[228:229], v[168:169], v[102:103] op_sel:[1,0,0]\n"
    "v_pk_fma_f32 v[168:169], v[228:229], v[168:169], v[172:173] op_sel_hi:[0,1,1]\n"
    "v_pk_fma_f32 v[170:171], v[228:229], v[170:171], v[174:175] op_sel_hi:[0,1,1]\n"
    "v_pk_fma_f32 v[174:175], v[228:229], v[168:169], v[104:105] op_sel:[1,0,0]\n"
    "v_pk_fma_f32 v[172:173], v[228:229], v[170:171], v[56:57] op_sel:[1,0,0] neg_lo:[1,0,0] neg_hi:[1,0,0]\n"
    "v_pk_fma_f32 v[170:171], v[228:229], v[170:171], v[174:175] op_sel_hi:[0,1,1]\n"
    "v_pk_fma_f32 v[168:169], v[228:229], v[168:169], v[172:173] op_sel_hi:[0,1,1]\n"
    "v_pk_fma_f32 v[172:173], v[228:229], v[170:171], v[58:59] op_sel:[1,0,0] neg_lo:[1,0,0] neg_hi:[1,0,0]\n"
    "v_pk_fma_f32 v[174:175], v[228:229], v[168:169], v[106:107] op_sel:[1,0,0]\n"
    "v_pk_fma_f32 v[168:169], v[228:229], v[168:169], v[172:173] op_sel_hi:[0,1,1]\n"
    "v_pk_fma_f32 v[170:171], v[228:229], v[170:171], v[174:175] op_sel_hi:[0,1,1]\n"
    "v_pk_fma_f32 v[174:175], v[228:229], v[168:169], v[108:109] op_sel:[1,0,0]\n"
    "v_pk_fma_f32 v[172:173], v[228:229], v[170:171], v[60:61] op_sel:[1,0,0] neg_lo:[1,0,0] neg_hi:[1,0,0]\n"
    "v_pk_fma_f32 v[170:171], v[228:229], v[170:171], v[174:175] op_sel_hi:[0,1,1]\n"
    "v_pk_fma_f32 v[168:169], v[228:229], v[168:169], v[172:173] op_sel_hi:[0,1,1]\n"
    "v_pk_fma_f32 v[172:173], v[228:229], v[170:171], v[62:63] op_sel:[1,0,0] neg_lo:[1,0,0] neg_hi:[1,0,0]\n"
    "v_pk_fma_f32 v[174:175], v[228:229], v[168:169], v[110:111] op_sel:[1,0,0]\n"
    "v_pk_fma_f32 v[168:169], v[228:229], v[168:169], v[172:173] op_sel_hi:[0,1,1]\n"
    "v_pk_fma_f32 v[170:171], v[228:229], v[170:171], v[174:175] op_sel_hi:[0,1,1]\n"
    "v_fma_f32 v172, -v233, v170, v169\n"
    "v_fma_f32 v173, v233, v168, v171\n"
    "v_fmac_f32 v172, v232, v168\n"
    "v_fmac_f32 v173, v232, v170\n"
    "ds_write_b64 v11, v[172:173] offset:256\n"
    "s_waitcnt lgkmcnt(0)\n"
    "s_barrier\n"
    "s_and_b32 s62, s35, 1\n"
    "v_pk_mul_f32 v[238:239], v[220:221], v[220:221] op_sel:[1,1] op_sel_hi:[1,0] neg_lo:[1,0]\n"
    "v_pk_mul_f32 v[244:245], v[226:227], v[226:227] op_sel:[1,1] op_sel_hi:[1,0] neg_lo:[1,0]\n"
    "v_pk_mul_f32 v[250:251], v[232:233], v[232:233] op_sel:[1,1] op_sel_hi:[1,0] neg_lo:[1,0]\n"
    "s_nop 0\n"
    "s_cselect_b64 s[66:67], -1, 0\n"
    "s_and_b32 s62, s35, 2\n"
    "v_pk_fma_f32 v[236:237], v[220:221], v[220:221], v[238:239] op_sel_hi:[0,1,1]\n"
    "v_pk_fma_f32 v[242:243], v[226:227], v[226:227], v[244:245] op_sel_hi:[0,1,1]\n"
    "v_pk_fma_f32 v[248:249], v[232:233], v[232:233], v[250:251] op_sel_hi:[0,1,1]\n"
    "s_mov_b32 s62, 0xffff0000\n"
    "s_mov_b32 s63, 0xffff0000\n"
    "v_pk_mul_f32 v[238:239], v[236:237], v[236:237] op_sel:[1,1] op_sel_hi:[1,0] neg_lo:[1,0]\n"
    "v_pk_mul_f32 v[244:245], v[242:243], v[242:243] op_sel:[1,1] op_sel_hi:[1,0] neg_lo:[1,0]\n"
    "v_pk_mul_f32 v[250:251], v[248:249], v[248:249] op_sel:[1,1] op_sel_hi:[1,0] neg_lo:[1,0]\n"
    "v_cndmask_b32_e64 v234, 1.0, v236, s[62:63]\n"
    "v_cndmask_b32_e64 v235, 0, v237, s[62:63]\n"
    "v_cndmask_b32_e64 v240, 1.0, v242, s[62:63]\n"
    "v_cndmask_b32_e64 v241, 0, v243, s[62:63]\n"
    "v_cndmask_b32_e64 v246, 1.0, v248, s[62:63]\n"
    "v_cndmask_b32_e64 v247, 0, v249, s[62:63]\n"
    "v_pk_fma_f32 v[236:237], v[236:237], v[236:237], v[238:239] op_sel_hi:[0,1,1]\n"
    "v_pk_fma_f32 v[242:243], v[242:243], v[242:243], v[244:245] op_sel_hi:[0,1,1]\n"
    "v_pk_fma_f32 v[248:249], v[248:249], v[248:249], v[250:251] op_sel_hi:[0,1,1]\n"
    "v_pk_mul_f32 v[238:239], v[234:235], v[236:237] op_sel:[1,1] op_sel_hi:[1,0] neg_lo:[1,0]\n"
    "v_pk_mul_f32 v[244:245], v[240:241], v[242:243] op_sel:[1,1] op_sel_hi:[1,0] neg_lo:[1,0]\n"
    "v_pk_mul_f32 v[250:251], v[246:247], v[248:249] op_sel:[1,1] op_sel_hi:[1,0] neg_lo:[1,0]\n"
    "s_mov_b32 s64, 0\n"
    "s_mov_b32 s65, -1\n"
    "v_pk_fma_f32 v[238:239], v[234:235], v[236:237], v[238:239] op_sel_hi:[0,1,1]\n"
    "v_pk_fma_f32 v[244:245], v[240:241], v[242:243], v[244:245] op_sel_hi:[0,1,1]\n"
    "v_pk_fma_f32 v[250:251], v[246:247], v[248:249], v[250:251] op_sel_hi:[0,1,1]\n"
    "s_nop 0\n"
    "s_cselect_b64 s[68:69], -1, 0\n"
    "v_cndmask_b32_e64 v234, v234, v238, s[64:65]\n"
    "v_cndmask_b32_e64 v235, v235, v239, s[64:65]\n"
    "v_cndmask_b32_e64 v240, v240, v244, s[64:65]\n"
    "v_cndmask_b32_e64 v241, v241, v245, s[64:65]\n"
    "v_cndmask_b32_e64 v246, v246, v250, s[64:65]\n"
    "v_cndmask_b32_e64 v247, v247, v251, s[64:65]\n"
    "s_lshl2_add_u32 s70, s55, s35\n"
    "v_pk_mul_f32 v[238:239], v[236:237], v[236:237] op_sel:[1,1] op_sel_hi:[1,0] neg_lo:[1,0]\n"
    "v_pk_mul_f32 v[244:245], v[242:243], v[242:243] op_sel:[1,1] op_sel_hi:[1,0] neg_lo:[1,0]\n"
    "v_pk_mul_f32 v[250:251], v[248:249], v[248:249] op_sel:[1,1] op_sel_hi:[1,0] neg_lo:[1,0]\n"
    "s_lshl_b32 s73, s70, 4\n"
    "v_pk_fma_f32 v[236:237], v[236:237], v[236:237], v[238:239] op_sel_hi:[0,1,1]\n"
    "v_pk_fma_f32 v[242:243], v[242:243], v[242:243], v[244:245] op_sel_hi:[0,1,1]\n"
    "v_pk_fma_f32 v[248:249], v[248:249], v[248:249], v[250:251] op_sel_hi:[0,1,1]\n"
    "s_mul_i32 s70, s70, 18\n"
    "v_pk_mul_f32 v[238:239], v[234:235], v[236:237] op_sel:[1,1] op_sel_hi:[1,0] neg_lo:[1,0]\n"
    "v_pk_mul_f32 v[244:245], v[240:241], v[242:243] op_sel:[1,1] op_sel_hi:[1,0] neg_lo:[1,0]\n"
    "v_pk_mul_f32 v[250:251], v[246:247], v[248:249] op_sel:[1,1] op_sel_hi:[1,0] neg_lo:[1,0]\n"
    "s_add_u32 s70, s70, s57\n"
    "v_pk_mul_f32 v[176:177], v[218:219], v[64:65] op_sel:[1,0]\n"
    "v_pk_mul_f32 v[178:179], v[218:219], v[16:17] op_sel:[1,0]\n"
    "v_pk_mul_f32 v[180:181], v[218:219], v[66:67] op_sel:[1,0]\n"
    "v_pk_mul_f32 v[182:183], v[218:219], v[18:19] op_sel:[1,0]\n"
    "v_pk_mul_f32 v[184:185], v[218:219], v[68:69] op_sel:[1,0]\n"
    "v_pk_mul_f32 v[186:187], v[218:219], v[20:21] op_sel:[1,0]\n"
    "v_pk_mul_f32 v[188:189], v[218:219], v[70:71] op_sel:[1,0]\n"
    "v_pk_mul_f32 v[190:191], v[218:219], v[22:23] op_sel:[1,0]\n"
    "v_pk_mul_f32 v[192:193], v[218:219], v[72:73] op_sel:[1,0]\n"
    "v_pk_mul_f32 v[194:195], v[218:219], v[24:25] op_sel:[1,0]\n"
    "v_pk_mul_f32 v[196:197], v[218:219], v[74:75] op_sel:[1,0]\n"
    "v_pk_mul_f32 v[198:199], v[218:219], v[26:27] op_sel:[1,0]\n"
    "v_pk_fma_f32 v[238:239], v[234:235], v[236:237], v[238:239] op_sel_hi:[0,1,1]\n"
    "v_pk_fma_f32 v[244:245], v[240:241], v[242:243], v[244:245] op_sel_hi:[0,1,1]\n"
    "v_pk_fma_f32 v[250:251], v[246:247], v[248:249], v[250:251] op_sel_hi:[0,1,1]\n"
    "s_mul_i32 s70, s70, 12288\n"
    "v_pk_fma_f32 v[16:17], v[218:219], v[16:17], v[176:177] op_sel_hi:[0,1,1] neg_lo:[0,0,1] neg_hi:[0,0,1]\n"
    "v_pk_fma_f32 v[64:65], v[218:219], v[64:65], v[178:179] op_sel_hi:[0,1,1] neg_lo:[1,0,1] neg_hi:[1,0,1]\n"
    "v_pk_fma_f32 v[18:19], v[218:219], v[18:19], v[180:181] op_sel_hi:[0,1,1] neg_lo:[0,0,1] neg_hi:[0,0,1]\n"
    "v_pk_fma_f32 v[66:67], v[218:219], v[66:67], v[182:183] op_sel_hi:[0,1,1] neg_lo:[1,0,1] neg_hi:[1,0,1]\n"
    "v_pk_fma_f32 v[20:21], v[218:219], v[20:21], v[184:185] op_sel_hi:[0,1,1] neg_lo:[0,0,1] neg_hi:[0,0,1]\n"
    "v_pk_fma_f32 v[68:69], v[218:219], v[68:69], v[186:187] op_sel_hi:[0,1,1] neg_lo:[1,0,1] neg_hi:[1,0,1]\n"
    "v_pk_fma_f32 v[22:23], v[218:219], v[22:23], v[188:189] op_sel_hi:[0,1,1] neg_lo:[0,0,1] neg_hi:[0,0,1]\n"
    "v_pk_fma_f32 v[70:71], v[218:219], v[70:71], v[190:191] op_sel_hi:[0,1,1] neg_lo:[1,0,1] neg_hi:[1,0,1]\n"
    "v_pk_fma_f32 v[24:25], v[218:219], v[24:25], v[192:193] op_sel_hi:[0,1,1] neg_lo:[0,0,1] neg_hi:[0,0,1]\n"
    "v_pk_fma_f32 v[72:73], v[218:219], v[72:73], v[194:195] op_sel_hi:[0,1,1] neg_lo:[1,0,1] neg_hi:[1,0,1]\n"
    "v_pk_fma_f32 v[26:27], v[218:219], v[26:27], v[196:197] op_sel_hi:[0,1,1] neg_lo:[0,0,1] neg_hi:[0,0,1]\n"
    "v_pk_fma_f32 v[74:75], v[218:219], v[74:75], v[198:199] op_sel_hi:[0,1,1] neg_lo:[1,0,1] neg_hi:[1,0,1]\n"
    "v_cndmask_b32_e64 v234, v234, v238, s[66:67]\n"
    "v_cndmask_b32_e64 v235, v235, v239, s[66:67]\n"
    "v_cndmask_b32_e64 v240, v240, v244, s[66:67]\n"
    "v_cndmask_b32_e64 v241, v241, v245, s[66:67]\n"
    "v_cndmask_b32_e64 v246, v246, v250, s[66:67]\n"
    "v_cndmask_b32_e64 v247, v247, v251, s[66:67]\n"
    "v_pk_mul_f32 v[176:177], v[218:219], v[76:77] op_sel:[1,0]\n"
    "v_pk_mul_f32 v[178:179], v[218:219], v[28:29] op_sel:[1,0]\n"
    "v_pk_mul_f32 v[180:181], v[218:219], v[78:79] op_sel:[1,0]\n"
    "v_pk_mul_f32 v[182:183], v[218:219], v[30:31] op_sel:[1,0]\n"
    "v_pk_mul_f32 v[184:185], v[224:225], v[80:81] op_sel:[1,0]\n"
    "v_pk_mul_f32 v[186:187], v[224:225], v[32:33] op_sel:[1,0]\n"
    "v_pk_mul_f32 v[188:189], v[224:225], v[82:83] op_sel:[1,0]\n"
    "v_pk_mul_f32 v[190:191], v[224:225], v[34:35] op_sel:[1,0]\n"
    "v_pk_mul_f32 v[192:193], v[224:225], v[84:85] op_sel:[1,0]\n"
    "v_pk_mul_f32 v[194:195], v[224:225], v[36:37] op_sel:[1,0]\n"
    "v_pk_mul_f32 v[196:197], v[224:225], v[86:87] op_sel:[1,0]\n"
    "v_pk_mul_f32 v[198:199], v[224:225], v[38:39] op_sel:[1,0]\n"
    "s_add_u32 s70, s70, 2097152\n"
    "v_pk_mul_f32 v[238:239], v[236:237], v[236:237] op_sel:[1,1] op_sel_hi:[1,0] neg_lo:[1,0]\n"
    "v_pk_mul_f32 v[244:245], v[242:243], v[242:243] op_sel:[1,1] op_sel_hi:[1,0] neg_lo:[1,0]\n"
    "v_pk_mul_f32 v[250:251], v[248:249], v[248:249] op_sel:[1,1] op_sel_hi:[1,0] neg_lo:[1,0]\n"
    "v_pk_fma_f32 v[28:29], v[218:219], v[28:29], v[176:177] op_sel_hi:[0,1,1] neg_lo:[0,0,1] neg_hi:[0,0,1]\n"
    "v_pk_fma_f32 v[76:77], v[218:219], v[76:77], v[178:179] op_sel_hi:[0,1,1] neg_lo:[1,0,1] neg_hi:[1,0,1]\n"
    "v_pk_fma_f32 v[30:31], v[218:219], v[30:31], v[180:181] op_sel_hi:[0,1,1] neg_lo:[0,0,1] neg_hi:[0,0,1]\n"
    "v_pk_fma_f32 v[78:79], v[218:219], v[78:79], v[182:183] op_sel_hi:[0,1,1] neg_lo:[1,0,1] neg_hi:[1,0,1]\n"
    "v_pk_fma_f32 v[32:33], v[224:225], v[32:33], v[184:185] op_sel_hi:[0,1,1] neg_lo:[0,0,1] neg_hi:[0,0,1]\n"
    "v_pk_fma_f32 v[80:81], v[224:225], v[80:81], v[186:187] op_sel_hi:[0,1,1] neg_lo:[1,0,1] neg_hi:[1,0,1]\n"
    "v_pk_fma_f32 v[34:35], v[224:225], v[34:35], v[188:189] op_sel_hi:[0,1,1] neg_lo:[0,0,1] neg_hi:[0,0,1]\n"
    "v_pk_fma_f32 v[82:83], v[224:225], v[82:83], v[190:191] op_sel_hi:[0,1,1] neg_lo:[1,0,1] neg_hi:[1,0,1]\n"
    "v_pk_fma_f32 v[36:37], v[224:225], v[36:37], v[192:193] op_sel_hi:[0,1,1] neg_lo:[0,0,1] neg_hi:[0,0,1]\n"
    "v_pk_fma_f32 v[84:85], v[224:225], v[84:85], v[194:195] op_sel_hi:[0,1,1] neg_lo:[1,0,1] neg_hi:[1,0,1]\n"
    "v_pk_fma_f32 v[38:39], v[224:225], v[38:39], v[196:197] op_sel_hi:[0,1,1] neg_lo:[0,0,1] neg_hi:[0,0,1]\n"
    "v_pk_fma_f32 v[86:87], v[224:225], v[86:87], v[198:199] op_sel_hi:[0,1,1] neg_lo:[1,0,1] neg_hi:[1,0,1]\n"
    "s_add_u32 s71, s70, 4096\n"
    "v_pk_fma_f32 v[236:237], v[236:237], v[236:237], v[238:239] op_sel_hi:[0,1,1]\n"
    "v_pk_fma_f32 v[242:243], v[242:243], v[242:243], v[244:245] op_sel_hi:[0,1,1]\n"
    "v_pk_fma_f32 v[248:249], v[248:249], v[248:249], v[250:251] op_sel_hi:[0,1,1]\n"
    "v_pk_mul_f32 v[176:177], v[224:225], v[88:89] op_sel:[1,0]\n"
    "v_pk_mul_f32 v[178:179], v[224:225], v[40:41] op_sel:[1,0]\n"
    "v_pk_mul_f32 v[180:181], v[224:225], v[90:91] op_sel:[1,0]\n"
    "v_pk_mul_f32 v[182:183], v[224:225], v[42:43] op_sel:[1,0]\n"
    "v_pk_mul_f32 v[184:185], v[224:225], v[92:93] op_sel:[1,0]\n"
    "v_pk_mul_f32 v[186:187], v[224:225], v[44:45] op_sel:[1,0]\n"
    "v_pk_mul_f32 v[188:189], v[224:225], v[94:95] op_sel:[1,0]\n"
    "v_pk_mul_f32 v[190:191], v[224:225], v[46:47] op_sel:[1,0]\n"
    "v_pk_mul_f32 v[192:193], v[230:231], v[96:97] op_sel:[1,0]\n"
    "v_pk_mul_f32 v[194:195], v[230:231], v[48:49] op_sel:[1,0]\n"
    "v_pk_mul_f32 v[196:197], v[230:231], v[98:99] op_sel:[1,0]\n"
    "v_pk_mul_f32 v[198:199], v[230:231], v[50:51] op_sel:[1,0]\n"
    "s_add_u32 s72, s70, 8192\n"
    "v_pk_mul_f32 v[238:239], v[234:235], v[236:237] op_sel:[1,1] op_sel_hi:[1,0] neg_lo:[1,0]\n"
    "v_pk_mul_f32 v[244:245], v[240:241], v[242:243] op_sel:[1,1] op_sel_hi:[1,0] neg_lo:[1,0]\n"
    "v_pk_mul_f32 v[250:251], v[246:247], v[248:249] op_sel:[1,1] op_sel_hi:[1,0] neg_lo:[1,0]\n"
    "s_nop 0\n"
    "s_add_u32 s73, s57, s73\n"
    "v_pk_fma_f32 v[40:41], v[224:225], v[40:41], v[176:177] op_sel_hi:[0,1,1] neg_lo:[0,0,1] neg_hi:[0,0,1]\n"
    "v_pk_fma_f32 v[88:89], v[224:225], v[88:89], v[178:179] op_sel_hi:[0,1,1] neg_lo:[1,0,1] neg_hi:[1,0,1]\n"
    "v_pk_fma_f32 v[42:43], v[224:225], v[42:43], v[180:181] op_sel_hi:[0,1,1] neg_lo:[0,0,1] neg_hi:[0,0,1]\n"
    "v_pk_fma_f32 v[90:91], v[224:225], v[90:91], v[182:183] op_sel_hi:[0,1,1] neg_lo:[1,0,1] neg_hi:[1,0,1]\n"
    "v_pk_fma_f32 v[44:45], v[224:225], v[44:45], v[184:185] op_sel_hi:[0,1,1] neg_lo:[0,0,1] neg_hi:[0,0,1]\n"
    "v_pk_fma_f32 v[92:93], v[224:225], v[92:93], v[186:187] op_sel_hi:[0,1,1] neg_lo:[1,0,1] neg_hi:[1,0,1]\n"
    "v_pk_fma_f32 v[46:47], v[224:225], v[46:47], v[188:189] op_sel_hi:[0,1,1] neg_lo:[0,0,1] neg_hi:[0,0,1]\n"
    "v_pk_fma_f32 v[94:95], v[224:225], v[94:95], v[190:191] op_sel_hi:[0,1,1] neg_lo:[1,0,1] neg_hi:[1,0,1]\n"
    "v_pk_fma_f32 v[48:49], v[230:231], v[48:49], v[192:193] op_sel_hi:[0,1,1] neg_lo:[0,0,1] neg_hi:[0,0,1]\n"
    "v_pk_fma_f32 v[96:97], v[230:231], v[96:97], v[194:195] op_sel_hi:[0,1,1] neg_lo:[1,0,1] neg_hi:[1,0,1]\n"
    "v_pk_fma_f32 v[50:51], v[230:231], v[50:51], v[196:197] op_sel_hi:[0,1,1] neg_lo:[0,0,1] neg_hi:[0,0,1]\n"
    "v_pk_fma_f32 v[98:99], v[230:231], v[98:99], v[198:199] op_sel_hi:[0,1,1] neg_lo:[1,0,1] neg_hi:[1,0,1]\n"
    "v_pk_fma_f32 v[238:239], v[234:235], v[236:237], v[238:239] op_sel_hi:[0,1,1]\n"
    "v_pk_fma_f32 v[244:245], v[240:241], v[242:243], v[244:245] op_sel_hi:[0,1,1]\n"
    "v_pk_fma_f32 v[250:251], v[246:247], v[248:249], v[250:251] op_sel_hi:[0,1,1]\n"
    "v_pk_mul_f32 v[176:177], v[230:231], v[100:101] op_sel:[1,0]\n"
    "v_pk_mul_f32 v[178:179], v[230:231], v[52:53] op_sel:[1,0]\n"
    "v_pk_mul_f32 v[180:181], v[230:231], v[102:103] op_sel:[1,0]\n"
    "v_pk_mul_f32 v[182:183], v[230:231], v[54:55] op_sel:[1,0]\n"
    "v_pk_mul_f32 v[184:185], v[230:231], v[104:105] op_sel:[1,0]\n"
    "v_pk_mul_f32 v[186:187], v[230:231], v[56:57] op_sel:[1,0]\n"
    "v_pk_mul_f32 v[188:189], v[230:231], v[106:107] op_sel:[1,0]\n"
    "v_pk_mul_f32 v[190:191], v[230:231], v[58:59] op_sel:[1,0]\n"
    "v_pk_mul_f32 v[192:193], v[230:231], v[108:109] op_sel:[1,0]\n"
    "v_pk_mul_f32 v[194:195], v[230:231], v[60:61] op_sel:[1,0]\n"
    "v_pk_mul_f32 v[196:197], v[230:231], v[110:111] op_sel:[1,0]\n"
    "v_pk_mul_f32 v[198:199], v[230:231], v[62:63] op_sel:[1,0]\n"
    "s_nop 0\n"
    "s_lshl_b32 s73, s73, 7\n"
    "v_cndmask_b32_e64 v234, v234, v238, s[68:69]\n"
    "v_cndmask_b32_e64 v235, v235, v239, s[68:69]\n"
    "v_cndmask_b32_e64 v240, v240, v244, s[68:69]\n"
    "v_cndmask_b32_e64 v241, v241, v245, s[68:69]\n"
    "v_cndmask_b32_e64 v246, v246, v250, s[68:69]\n"
    "v_cndmask_b32_e64 v247, v247, v251, s[68:69]\n"
    "v_pk_fma_f32 v[52:53], v[230:231], v[52:53], v[176:177] op_sel_hi:[0,1,1] neg_lo:[0,0,1] neg_hi:[0,0,1]\n"
    "v_pk_fma_f32 v[100:101], v[230:231], v[100:101], v[178:179] op_sel_hi:[0,1,1] neg_lo:[1,0,1] neg_hi:[1,0,1]\n"
    "v_pk_fma_f32 v[54:55], v[230:231], v[54:55], v[180:181] op_sel_hi:[0,1,1] neg_lo:[0,0,1] neg_hi:[0,0,1]\n"
    "v_pk_fma_f32 v[102:103], v[230:231], v[102:103], v[182:183] op_sel_hi:[0,1,1] neg_lo:[1,0,1] neg_hi:[1,0,1]\n"
    "v_pk_fma_f32 v[56:57], v[230:231], v[56:57], v[184:185] op_sel_hi:[0,1,1] neg_lo:[0,0,1] neg_hi:[0,0,1]\n"
    "v_pk_fma_f32 v[104:105], v[230:231], v[104:105], v[186:187] op_sel_hi:[0,1,1] neg_lo:[1,0,1] neg_hi:[1,0,1]\n"
    "v_pk_fma_f32 v[58:59], v[230:231], v[58:59], v[188:189] op_sel_hi:[0,1,1] neg_lo:[0,0,1] neg_hi:[0,0,1]\n"
    "v_pk_fma_f32 v[106:107], v[230:231], v[106:107], v[190:191] op_sel_hi:[0,1,1] neg_lo:[1,0,1] neg_hi:[1,0,1]\n"
    "v_pk_fma_f32 v[60:61], v[230:231], v[60:61], v[192:193] op_sel_hi:[0,1,1] neg_lo:[0,0,1] neg_hi:[0,0,1]\n"
    "v_pk_fma_f32 v[108:109], v[230:231], v[108:109], v[194:195] op_sel_hi:[0,1,1] neg_lo:[1,0,1] neg_hi:[1,0,1]\n"
    "v_pk_fma_f32 v[62:63], v[230:231], v[62:63], v[196:197] op_sel_hi:[0,1,1] neg_lo:[0,0,1] neg_hi:[0,0,1]\n"
    "v_pk_fma_f32 v[110:111], v[230:231], v[110:111], v[198:199] op_sel_hi:[0,1,1] neg_lo:[1,0,1] neg_hi:[1,0,1]\n"
    "s_add_u32 s73, s73, 1900544\n"
    "s_waitcnt lgkmcnt(0)\n"
    "s_barrier\n"
    "ds_read_b64 v[176:177], v11 offset:6144\n"
    "ds_read_b64 v[236:237], v12 offset:0\n"
    "ds_read_b64 v[178:179], v11 offset:6272\n"
    "ds_read_b64 v[242:243], v12 offset:128\n"
    "ds_read_b64 v[180:181], v11 offset:6400\n"
    "ds_read_b64 v[248:249], v12 offset:256\n"
    "s_waitcnt lgkmcnt(4)\n"
    "v_pk_fma_f32 v[238:239], v[234:235], v[236:237], v[176:177] op_sel:[1,1,0] op_sel_hi:[1,0,1] neg_lo:[1,0,0]\n"
    "v_pk_fma_f32 v[176:177], v[234:235], v[236:237], v[238:239] op_sel_hi:[0,1,1]\n"
    "v_fma_f32 v153, -v221, v177, v152\n"
    "v_fma_f32 v155, v221, v176, v154\n"
    "v_fmac_f32 v153, v220, v176\n"
    "v_fmac_f32 v155, v220, v177\n"
    "v_mov_b32 v152, v176\n"
    "v_mov_b32_e64 v154, v177\n"
    "v_pk_mul_f32 v[156:157], v[218:219], v[154:155] op_sel:[1,0]\n"
    "v_pk_mul_f32 v[158:159], v[218:219], v[152:153] op_sel:[1,0]\n"
    "v_pk_fma_f32 v[152:153], v[218:219], v[152:153], v[156:157] op_sel_hi:[0,1,1] neg_lo:[0,0,1] neg_hi:[0,0,1]\n"
    "v_pk_fma_f32 v[154:155], v[218:219], v[154:155], v[158:159] op_sel_hi:[0,1,1] neg_lo:[1,0,1] neg_hi:[1,0,1]\n"
    "v_pk_fma_f32 v[156:157], v[216:217], v[154:155], v[16:17] op_sel:[1,0,0]\n"
    "v_pk_fma_f32 v[158:159], v[216:217], v[152:153], v[64:65] op_sel:[1,0,0] neg_lo:[1,0,0] neg_hi:[1,0,0]\n"
    "v_pk_fma_f32 v[16:17], v[216:217], v[152:153], v[156:157] op_sel_hi:[0,1,1]\n"
    "v_pk_fma_f32 v[64:65], v[216:217], v[154:155], v[158:159] op_sel_hi:[0,1,1]\n"
    "v_pk_fma_f32 v[156:157], v[216:217], v[64:65], v[18:19] op_sel:[1,0,0]\n"
    "v_pk_fma_f32 v[158:159], v[216:217], v[16:17], v[66:67] op_sel:[1,0,0] neg_lo:[1,0,0] neg_hi:[1,0,0]\n"
    "v_pk_fma_f32 v[18:19], v[216:217], v[16:17], v[156:157] op_sel_hi:[0,1,1]\n"
    "v_pk_fma_f32 v[66:67], v[216:217], v[64:65], v[158:159] op_sel_hi:[0,1,1]\n"
    "v_cvt_pk_bf16_f32 v114, v16, v17\n"
    "v_cvt_pk_bf16_f32 v115, v18, v19\n"
    "v_cvt_pk_bf16_f32 v116, v64, v65\n"
    "v_cvt_pk_bf16_f32 v117, v66, v67\n"
    "v_mfma_f32_16x16x16_bf16 v[128:131], v[114:115], v[112:113], 0\n"
    "s_nop 0\n"
    "s_nop 0\n"
    "v_mfma_f32_16x16x16_bf16 v[132:135], v[116:117], v[112:113], 0\n"
    "v_pk_fma_f32 v[156:157], v[216:217], v[66:67], v[20:21] op_sel:[1,0,0]\n"
    "v_pk_fma_f32 v[158:159], v[216:217], v[18:19], v[68:69] op_sel:[1,0,0] neg_lo:[1,0,0] neg_hi:[1,0,0]\n"
    "v_pk_fma_f32 v[20:21], v[216:217], v[18:19], v[156:157] op_sel_hi:[0,1,1]\n"
    "v_pk_fma_f32 v[68:69], v[216:217], v[66:67], v[158:159] op_sel_hi:[0,1,1]\n"
    "v_pk_fma_f32 v[156:157], v[216:217], v[68:69], v[22:23] op_sel:[1,0,0]\n"
    "v_pk_fma_f32 v[158:159], v[216:217], v[20:21], v[70:71] op_sel:[1,0,0] neg_lo:[1,0,0] neg_hi:[1,0,0]\n"
    "v_pk_fma_f32 v[22:23], v[216:217], v[20:21], v[156:157] op_sel_hi:[0,1,1]\n"
    "v_pk_fma_f32 v[70:71], v[216:217], v[68:69], v[158:159] op_sel_hi:[0,1,1]\n"
    "v_cvt_pk_bf16_f32 v114, v20, v21\n"
    "v_cvt_pk_bf16_f32 v115, v22, v23\n"
    "v_cvt_pk_bf16_f32 v116, v68, v69\n"
    "v_cvt_pk_bf16_f32 v117, v70, v71\n"
    "v_mfma_f32_16x16x16_bf16 v[136:139], v[114:115], v[112:113], 0\n"
    "v_cvt_pk_bf16_f32 v184, v128, v129\n"
    "v_cvt_pk_bf16_f32 v186, v132, v133\n"
    "v_mfma_f32_16x16x16_bf16 v[140:143], v[116:117], v[112:113], 0\n"
    "v_cvt_pk_bf16_f32 v185, v130, v131\n"
    "v_cvt_pk_bf16_f32 v187, v134, v135\n"
    "v_permlane16_swap_b32 v184, v186\n"
    "s_nop 0\n"
    "v_permlane16_swap_b32_e64 v185, v187\n"
    "buffer_store_dwordx4 v[184:187], v13, s[36:39], s70 offen offset:0 sc1\n"
    "v_pk_fma_f32 v[156:157], v[216:217], v[70:71], v[24:25] op_sel:[1,0,0]\n"
    "v_pk_fma_f32 v[158:159], v[216:217], v[22:23], v[72:73] op_sel:[1,0,0] neg_lo:[1,0,0] neg_hi:[1,0,0]\n"
    "v_pk_fma_f32 v[24:25], v[216:217], v[22:23], v[156:157] op_sel_hi:[0,1,1]\n"
    "v_pk_fma_f32 v[72:73], v[216:217], v[70:71], v[158:159] op_sel_hi:[0,1,1]\n"
    "v_pk_fma_f32 v[156:157], v[216:217], v[72:73], v[26:27] op_sel:[1,0,0]\n"
    "v_pk_fma_f32 v[158:159], v[216:217], v[24:25], v[74:75] op_sel:[1,0,0] neg_lo:[1,0,0] neg_hi:[1,0,0]\n"
    "v_pk_fma_f32 v[26:27], v[216:217], v[24:25], v[156:157] op_sel_hi:[0,1,1]\n"
    "v_pk_fma_f32 v[74:75], v[216:217], v[72:73], v[158:159] op_sel_hi:[0,1,1]\n"
    "v_cvt_pk_bf16_f32 v114, v24, v25\n"
    "v_cvt_pk_bf16_f32 v115, v26, v27\n"
    "v_cvt_pk_bf16_f32 v116, v72, v73\n"
    "v_cvt_pk_bf16_f32 v117, v74, v75\n"
    "v_mfma_f32_16x16x16_bf16 v[128:131], v[114:115], v[112:113], 0\n"
    "v_cvt_pk_bf16_f32 v188, v136, v137\n"
    "v_cvt_pk_bf16_f32 v190, v140, v141\n"
    "v_mfma_f32_16x16x16_bf16 v[132:135], v[116:117], v[112:113], 0\n"
    "v_cvt_pk_bf16_f32 v189, v138, v139\n"
    "v_cvt_pk_bf16_f32 v191, v142, v143\n"
    "v_permlane16_swap_b32 v188, v190\n"
    "s_nop 0\n"
    "v_permlane16_swap_b32_e64 v189, v191\n"
    "buffer_store_dwordx4 v[188:191], v13, s[36:39], s70 offen offset:1024 sc1\n"
    "v_pk_fma_f32 v[156:157], v[216:217], v[74:75], v[28:29] op_sel:[1,0,0]\n"
    "v_pk_fma_f32 v[158:159], v[216:217], v[26:27], v[76:77] op_sel:[1,0,0] neg_lo:[1,0,0] neg_hi:[1,0,0]\n"
    "v_pk_fma_f32 v[28:29], v[216:217], v[26:27], v[156:157] op_sel_hi:[0,1,1]\n"
    "v_pk_fma_f32 v[76:77], v[216:217], v[74:75], v[158:159] op_sel_hi:[0,1,1]\n"
    "v_pk_fma_f32 v[156:157], v[216:217], v[76:77], v[30:31] op_sel:[1,0,0]\n"
    "v_pk_fma_f32 v[158:159], v[216:217], v[28:29], v[78:79] op_sel:[1,0,0] neg_lo:[1,0,0] neg_hi:[1,0,0]\n"
    "v_pk_fma_f32 v[30:31], v[216:217], v[28:29], v[156:157] op_sel_hi:[0,1,1]\n"
    "v_pk_fma_f32 v[78:79], v[216:217], v[76:77], v[158:159] op_sel_hi:[0,1,1]\n"
    "v_cvt_pk_bf16_f32 v114, v28, v29\n"
    "v_cvt_pk_bf16_f32 v115, v30, v31\n"
    "v_cvt_pk_bf16_f32 v116, v76, v77\n"
    "v_cvt_pk_bf16_f32 v117, v78, v79\n"
    "v_mfma_f32_16x16x16_bf16 v[136:139], v[114:115], v[112:113], 0\n"
    "v_cvt_pk_bf16_f32 v192, v128, v129\n"
    "v_cvt_pk_bf16_f32 v194, v132, v133\n"
    "v_mfma_f32_16x16x16_bf16 v[140:143], v[116:117], v[112:113], 0\n"
    "v_cvt_pk_bf16_f32 v193, v130, v131\n"
    "v_cvt_pk_bf16_f32 v195, v134, v135\n"
    "v_permlane16_swap_b32 v192, v194\n"
    "s_nop 0\n"
    "v_permlane16_swap_b32 v193, v195\n"
    "buffer_store_dwordx4 v[192:195], v13, s[36:39], s70 offen offset:2048 sc1\n"
    "s_waitcnt lgkmcnt(2)\n"
    "v_pk_fma_f32 v[244:245], v[240:241], v[242:243], v[178:179] op_sel:[1,1,0] op_sel_hi:[1,0,1] neg_lo:[1,0,0]\n"
    "v_pk_fma_f32 v[178:179], v[240:241], v[242:243], v[244:245] op_sel_hi:[0,1,1]\n"
    "v_fma_f32 v161, -v227, v179, v160\n"
    "v_fma_f32 v163, v227, v178, v162\n"
    "v_fmac_f32 v161, v226, v178\n"
    "v_fmac_f32 v163, v226, v179\n"
    "v_mov_b32 v160, v178\n"
    "v_mov_b32 v162, v179\n"
    "v_pk_mul_f32 v[164:165], v[224:225], v[162:163] op_sel:[1,0]\n"
    "v_pk_mul_f32 v[166:167], v[224:225], v[160:161] op_sel:[1,0]\n"
    "v_pk_fma_f32 v[160:161], v[224:225], v[160:161], v[164:165] op_sel_hi:[0,1,1] neg_lo:[0,0,1] neg_hi:[0,0,1]\n"
    "v_pk_fma_f32 v[162:163], v[224:225], v[162:163], v[166:167] op_sel_hi:[0,1,1] neg_lo:[1,0,1] neg_hi:[1,0,1]\n"
    "v_pk_fma_f32 v[164:165], v[222:223], v[162:163], v[32:33] op_sel:[1,0,0]\n"
    "v_pk_fma_f32 v[166:167], v[222:223], v[160:161], v[80:81] op_sel:[1,0,0] neg_lo:[1,0,0] neg_hi:[1,0,0]\n"
    "v_pk_fma_f32 v[32:33], v[222:223], v[160:161], v[164:165] op_sel_hi:[0,1,1]\n"
    "v_pk_fma_f32 v[80:81], v[222:223], v[162:163], v[166:167] op_sel_hi:[0,1,1]\n"
    "v_pk_fma_f32 v[164:165], v[222:223], v[80:81], v[34:35] op_sel:[1,0,0]\n"
    "v_pk_fma_f32 v[166:167], v[222:223], v[32:33], v[82:83] op_sel:[1,0,0] neg_lo:[1,0,0] neg_hi:[1,0,0]\n"
    "v_pk_fma_f32 v[34:35], v[222:223], v[32:33], v[164:165] op_sel_hi:[0,1,1]\n"
    "v_pk_fma_f32 v[82:83], v[222:223], v[80:81], v[166:167] op_sel_hi:[0,1,1]\n"
    "v_cvt_pk_bf16_f32 v114, v32, v33\n"
    "v_cvt_pk_bf16_f32 v115, v34, v35\n"
    "v_cvt_pk_bf16_f32 v116, v80, v81\n"
    "v_cvt_pk_bf16_f32 v117, v82, v83\n"
    "v_mfma_f32_16x16x16_bf16 v[128:131], v[114:115], v[112:113], 0\n"
    "v_cvt_pk_bf16_f32 v196, v136, v137\n"
    "v_cvt_pk_bf16_f32 v198, v140, v141\n"
    "v_mfma_f32_16x16x16_bf16 v[132:135], v[116:117], v[112:113], 0\n"
    "v_cvt_pk_bf16_f32 v197, v138, v139\n"
    "v_cvt_pk_bf16_f32 v199, v142, v143\n"
    "v_permlane16_swap_b32 v196, v198\n"
    "s_nop 0\n"
    "v_permlane16_swap_b32_e64 v197, v199\n"
    "buffer_store_dwordx4 v[196:199], v13, s[36:39], s70 offen offset:3072 sc1\n"
    "v_pk_fma_f32 v[164:165], v[222:223], v[82:83], v[36:37] op_sel:[1,0,0]\n"
    "v_pk_fma_f32 v[166:167], v[222:223], v[34:35], v[84:85] op_sel:[1,0,0] neg_lo:[1,0,0] neg_hi:[1,0,0]\n"
    "v_pk_fma_f32 v[36:37], v[222:223], v[34:35], v[164:165] op_sel_hi:[0,1,1]\n"
    "v_pk_fma_f32 v[84:85], v[222:223], v[82:83], v[166:167] op_sel_hi:[0,1,1]\n"
    "v_pk_fma_f32 v[164:165], v[222:223], v[84:85], v[38:39] op_sel:[1,0,0]\n"
    "v_pk_fma_f32 v[166:167], v[222:223], v[36:37], v[86:87] op_sel:[1,0,0] neg_lo:[1,0,0] neg_hi:[1,0,0]\n"
    "v_pk_fma_f32 v[38:39], v[222:223], v[36:37], v[164:165] op_sel_hi:[0,1,1]\n"
    "v_pk_fma_f32 v[86:87], v[222:223], v[84:85], v[166:167] op_sel_hi:[0,1,1]\n"
    "v_cvt_pk_bf16_f32 v114, v36, v37\n"
    "v_cvt_pk_bf16_f32 v115, v38, v39\n"
    "v_cvt_pk_bf16_f32 v116, v84, v85\n"
    "v_cvt_pk_bf16_f32 v117, v86, v87\n"
    "v_mfma_f32_16x16x16_bf16 v[136:139], v[114:115], v[112:113], 0\n"
    "v_cvt_pk_bf16_f32 v200, v128, v129\n"
    "v_cvt_pk_bf16_f32 v202, v132, v133\n"
    "v_mfma_f32_16x16x16_bf16 v[140:143], v[116:117], v[112:113], 0\n"
    "v_cvt_pk_bf16_f32 v201, v130, v131\n"
    "v_cvt_pk_bf16_f32 v203, v134, v135\n"
    "v_permlane16_swap_b32 v200, v202\n"
    "s_nop 0\n"
    "v_permlane16_swap_b32_e64 v201, v203\n"
    "buffer_store_dwordx4 v[200:203], v13, s[36:39], s71 offen offset:0 sc1\n"
    "v_pk_fma_f32 v[164:165], v[222:223], v[86:87], v[40:41] op_sel:[1,0,0]\n"
    "v_pk_fma_f32 v[166:167], v[222:223], v[38:39], v[88:89] op_sel:[1,0,0] neg_lo:[1,0,0] neg_hi:[1,0,0]\n"
    "v_pk_fma_f32 v[40:41], v[222:223], v[38:39], v[164:165] op_sel_hi:[0,1,1]\n"
    "v_pk_fma_f32 v[88:89], v[222:223], v[86:87], v[166:167] op_sel_hi:[0,1,1]\n"
    "v_pk_fma_f32 v[164:165], v[222:223], v[88:89], v[42:43] op_sel:[1,0,0]\n"
    "v_pk_fma_f32 v[166:167], v[222:223], v[40:41], v[90:91] op_sel:[1,0,0] neg_lo:[1,0,0] neg_hi:[1,0,0]\n"
    "v_pk_fma_f32 v[42:43], v[222:223], v[40:41], v[164:165] op_sel_hi:[0,1,1]\n"
    "v_pk_fma_f32 v[90:91], v[222:223], v[88:89], v[166:167] op_sel_hi:[0,1,1]\n"
    "v_cvt_pk_bf16_f32 v114, v40, v41\n"
    "v_cvt_pk_bf16_f32 v115, v42, v43\n"
    "v_cvt_pk_bf16_f32 v116, v88, v89\n"
    "v_cvt_pk_bf16_f32 v117, v90, v91\n"
    "v_mfma_f32_16x16x16_bf16 v[128:131], v[114:115], v[112:113], 0\n"
    "v_cvt_pk_bf16_f32 v204, v136, v137\n"
    "v_cvt_pk_bf16_f32 v206, v140, v141\n"
    "v_mfma_f32_16x16x16_bf16 v[132:135], v[116:117], v[112:113], 0\n"
    "v_cvt_pk_bf16_f32 v205, v138, v139\n"
    "v_cvt_pk_bf16_f32 v207, v142, v143\n"
    "v_permlane16_swap_b32 v204, v206\n"
    "s_nop 0\n"
    "v_permlane16_swap_b32_e64 v205, v207\n"
    "buffer_store_dwordx4 v[204:207], v13, s[36:39], s71 offen offset:1024 sc1\n"
    "v_pk_fma_f32 v[164:165], v[222:223], v[90:91], v[44:45] op_sel:[1,0,0]\n"
    "v_pk_fma_f32 v[166:167], v[222:223], v[42:43], v[92:93] op_sel:[1,0,0] neg_lo:[1,0,0] neg_hi:[1,0,0]\n"
    "v_pk_fma_f32 v[44:45], v[222:223], v[42:43], v[164:165] op_sel_hi:[0,1,1]\n"
    "v_pk_fma_f32 v[92:93], v[222:223], v[90:91], v[166:167] op_sel_hi:[0,1,1]\n"
    "v_pk_fma_f32 v[164:165], v[222:223], v[92:93], v[46:47] op_sel:[1,0,0]\n"
    "v_pk_fma_f32 v[166:167], v[222:223], v[44:45], v[94:95] op_sel:[1,0,0] neg_lo:[1,0,0] neg_hi:[1,0,0]\n"
    "v_pk_fma_f32 v[46:47], v[222:223], v[44:45], v[164:165] op_sel_hi:[0,1,1]\n"
    "v_pk_fma_f32 v[94:95], v[222:223], v[92:93], v[166:167] op_sel_hi:[0,1,1]\n"
    "v_cvt_pk_bf16_f32 v114, v44, v45\n"
    "v_cvt_pk_bf16_f32 v115, v46, v47\n"
    "v_cvt_pk_bf16_f32 v116, v92, v93\n"
    "v_cvt_pk_bf16_f32 v117, v94, v95\n"
    "v_mfma_f32_16x16x16_bf16 v[136:139], v[114:115], v[112:113], 0\n"
    "v_cvt_pk_bf16_f32 v208, v128, v129\n"
    "v_cvt_pk_bf16_f32 v210, v132, v133\n"
    "v_mfma_f32_16x16x16_bf16 v[140:143], v[116:117], v[112:113], 0\n"
    "v_cvt_pk_bf16_f32 v209, v130, v131\n"
    "v_cvt_pk_bf16_f32 v211, v134, v135\n"
    "v_permlane16_swap_b32 v208, v210\n"
    "s_nop 0\n"
    "v_permlane16_swap_b32 v209, v211\n"
    "buffer_store_dwordx4 v[208:211], v13, s[36:39], s71 offen offset:2048 sc1\n"
    "s_waitcnt lgkmcnt(0)\n"
    "v_pk_fma_f32 v[250:251], v[246:247], v[248:249], v[180:181] op_sel:[1,1,0] op_sel_hi:[1,0,1] neg_lo:[1,0,0]\n"
    "v_pk_fma_f32 v[180:181], v[246:247], v[248:249], v[250:251] op_sel_hi:[0,1,1]\n"
    "v_fma_f32 v169, -v233, v181, v168\n"
    "v_fma_f32 v171, v233, v180, v170\n"
    "v_fmac_f32 v169, v232, v180\n"
    "v_fmac_f32 v171, v232, v181\n"
    "v_mov_b32 v168, v180\n"
    "v_mov_b32 v170, v181\n"
    "v_pk_mul_f32 v[172:173], v[230:231], v[170:171] op_sel:[1,0]\n"
    "v_pk_mul_f32 v[174:175], v[230:231], v[168:169] op_sel:[1,0]\n"
    "v_pk_fma_f32 v[168:169], v[230:231], v[168:169], v[172:173] op_sel_hi:[0,1,1] neg_lo:[0,0,1] neg_hi:[0,0,1]\n"
    "v_pk_fma_f32 v[170:171], v[230:231], v[170:171], v[174:175] op_sel_hi:[0,1,1] neg_lo:[1,0,1] neg_hi:[1,0,1]\n"
    "v_pk_fma_f32 v[172:173], v[228:229], v[170:171], v[48:49] op_sel:[1,0,0]\n"
    "v_pk_fma_f32 v[174:175], v[228:229], v[168:169], v[96:97] op_sel:[1,0,0] neg_lo:[1,0,0] neg_hi:[1,0,0]\n"
    "v_pk_fma_f32 v[48:49], v[228:229], v[168:169], v[172:173] op_sel_hi:[0,1,1]\n"
    "v_pk_fma_f32 v[96:97], v[228:229], v[170:171], v[174:175] op_sel_hi:[0,1,1]\n"
    "v_pk_fma_f32 v[172:173], v[228:229], v[96:97], v[50:51] op_sel:[1,0,0]\n"
    "v_pk_fma_f32 v[174:175], v[228:229], v[48:49], v[98:99] op_sel:[1,0,0] neg_lo:[1,0,0] neg_hi:[1,0,0]\n"
    "v_pk_fma_f32 v[50:51], v[228:229], v[48:49], v[172:173] op_sel_hi:[0,1,1]\n"
    "v_pk_fma_f32 v[98:99], v[228:229], v[96:97], v[174:175] op_sel_hi:[0,1,1]\n"
    "v_cvt_pk_bf16_f32 v114, v48, v49\n"
    "v_cvt_pk_bf16_f32 v115, v50, v51\n"
    "v_cvt_pk_bf16_f32 v116, v96, v97\n"
    "v_cvt_pk_bf16_f32 v117, v98, v99\n"
    "v_mfma_f32_16x16x16_bf16 v[128:131], v[114:115], v[112:113], 0\n"
    "v_cvt_pk_bf16_f32 v212, v136, v137\n"
    "v_cvt_pk_bf16_f32 v214, v140, v141\n"
    "v_mfma_f32_16x16x16_bf16 v[132:135], v[116:117], v[112:113], 0\n"
    "v_cvt_pk_bf16_f32 v213, v138, v139\n"
    "v_cvt_pk_bf16_f32 v215, v142, v143\n"
    "v_permlane16_swap_b32 v212, v214\n"
    "s_nop 0\n"
    "v_permlane16_swap_b32_e64 v213, v215\n"
    "buffer_store_dwordx4 v[212:215], v13, s[36:39], s71 offen offset:3072 sc1\n"
    "v_pk_fma_f32 v[172:173], v[228:229], v[98:99], v[52:53] op_sel:[1,0,0]\n"
    "v_pk_fma_f32 v[174:175], v[228:229], v[50:51], v[100:101] op_sel:[1,0,0] neg_lo:[1,0,0] neg_hi:[1,0,0]\n"
    "v_pk_fma_f32 v[52:53], v[228:229], v[50:51], v[172:173] op_sel_hi:[0,1,1]\n"
    "v_pk_fma_f32 v[100:101], v[228:229], v[98:99], v[174:175] op_sel_hi:[0,1,1]\n"
    "v_pk_fma_f32 v[172:173], v[228:229], v[100:101], v[54:55] op_sel:[1,0,0]\n"
    "v_pk_fma_f32 v[174:175], v[228:229], v[52:53], v[102:103] op_sel:[1,0,0] neg_lo:[1,0,0] neg_hi:[1,0,0]\n"
    "v_pk_fma_f32 v[54:55], v[228:229], v[52:53], v[172:173] op_sel_hi:[0,1,1]\n"
    "v_pk_fma_f32 v[102:103], v[228:229], v[100:101], v[174:175] op_sel_hi:[0,1,1]\n"
    "v_cvt_pk_bf16_f32 v114, v52, v53\n"
    "v_cvt_pk_bf16_f32 v115, v54, v55\n"
    "v_cvt_pk_bf16_f32 v116, v100, v101\n"
    "v_cvt_pk_bf16_f32 v117, v102, v103\n"
    "v_mfma_f32_16x16x16_bf16 v[136:139], v[114:115], v[112:113], 0\n"
    "v_cvt_pk_bf16_f32 v184, v128, v129\n"
    "v_cvt_pk_bf16_f32 v186, v132, v133\n"
    "v_mfma_f32_16x16x16_bf16 v[140:143], v[116:117], v[112:113], 0\n"
    "v_cvt_pk_bf16_f32 v185, v130, v131\n"
    "v_cvt_pk_bf16_f32 v187, v134, v135\n"
    "v_permlane16_swap_b32 v184, v186\n"
    "s_nop 0\n"
    "v_permlane16_swap_b32_e64 v185, v187\n"
    "buffer_store_dwordx4 v[184:187], v13, s[36:39], s72 offen offset:0 sc1\n"
    "v_pk_fma_f32 v[172:173], v[228:229], v[102:103], v[56:57] op_sel:[1,0,0]\n"
    "v_pk_fma_f32 v[174:175], v[228:229], v[54:55], v[104:105] op_sel:[1,0,0] neg_lo:[1,0,0] neg_hi:[1,0,0]\n"
    "v_pk_fma_f32 v[56:57], v[228:229], v[54:55], v[172:173] op_sel_hi:[0,1,1]\n"
    "v_pk_fma_f32 v[104:105], v[228:229], v[102:103], v[174:175] op_sel_hi:[0,1,1]\n"
    "v_pk_fma_f32 v[172:173], v[228:229], v[104:105], v[58:59] op_sel:[1,0,0]\n"
    "v_pk_fma_f32 v[174:175], v[228:229], v[56:57], v[106:107] op_sel:[1,0,0] neg_lo:[1,0,0] neg_hi:[1,0,0]\n"
    "v_pk_fma_f32 v[58:59], v[228:229], v[56:57], v[172:173] op_sel_hi:[0,1,1]\n"
    "v_pk_fma_f32 v[106:107], v[228:229], v[104:105], v[174:175] op_sel_hi:[0,1,1]\n"
    "v_cvt_pk_bf16_f32 v114, v56, v57\n"
    "v_cvt_pk_bf16_f32 v115, v58, v59\n"
    "v_cvt_pk_bf16_f32 v116, v104, v105\n"
    "v_cvt_pk_bf16_f32 v117, v106, v107\n"
    "v_mfma_f32_16x16x16_bf16 v[128:131], v[114:115], v[112:113], 0\n"
    "v_cvt_pk_bf16_f32 v188, v136, v137\n"
    "v_cvt_pk_bf16_f32 v190, v140, v141\n"
    "v_mfma_f32_16x16x16_bf16 v[132:135], v[116:117], v[112:113], 0\n"
    "v_cvt_pk_bf16_f32 v189, v138, v139\n"
    "v_cvt_pk_bf16_f32 v191, v142, v143\n"
    "v_permlane16_swap_b32 v188, v190\n"
    "s_nop 0\n"
    "v_permlane16_swap_b32_e64 v189, v191\n"
    "buffer_store_dwordx4 v[188:191], v13, s[36:39], s72 offen offset:1024 sc1\n"
    "v_pk_fma_f32 v[172:173], v[228:229], v[106:107], v[60:61] op_sel:[1,0,0]\n"
    "v_pk_fma_f32 v[174:175], v[228:229], v[58:59], v[108:109] op_sel:[1,0,0] neg_lo:[1,0,0] neg_hi:[1,0,0]\n"
    "v_pk_fma_f32 v[60:61], v[228:229], v[58:59], v[172:173] op_sel_hi:[0,1,1]\n"
    "v_pk_fma_f32 v[108:109], v[228:229], v[106:107], v[174:175] op_sel_hi:[0,1,1]\n"
    "v_pk_fma_f32 v[172:173], v[228:229], v[108:109], v[62:63] op_sel:[1,0,0]\n"
    "v_pk_fma_f32 v[174:175], v[228:229], v[60:61], v[110:111] op_sel:[1,0,0] neg_lo:[1,0,0] neg_hi:[1,0,0]\n"
    "v_pk_fma_f32 v[62:63], v[228:229], v[60:61], v[172:173] op_sel_hi:[0,1,1]\n"
    "v_pk_fma_f32 v[110:111], v[228:229], v[108:109], v[174:175] op_sel_hi:[0,1,1]\n"
    "v_cvt_pk_bf16_f32 v114, v60, v61\n"
    "v_cvt_pk_bf16_f32 v115, v62, v63\n"
    "v_cvt_pk_bf16_f32 v116, v108, v109\n"
    "v_cvt_pk_bf16_f32 v117, v110, v111\n"
    "v_mfma_f32_16x16x16_bf16 v[136:139], v[114:115], v[112:113], 0\n"
    "v_cvt_pk_bf16_f32 v192, v128, v129\n"
    "v_cvt_pk_bf16_f32 v194, v132, v133\n"
    "v_mfma_f32_16x16x16_bf16 v[140:143], v[116:117], v[112:113], 0\n"
    "v_cvt_pk_bf16_f32 v193, v130, v131\n"
    "v_cvt_pk_bf16_f32 v195, v134, v135\n"
    "v_permlane16_swap_b32 v192, v194\n"
    "s_nop 0\n"
    "v_permlane16_swap_b32_e64 v193, v195\n"
    "buffer_store_dwordx4 v[192:195], v13, s[36:39], s72 offen offset:2048 sc1\n"
    "v_cvt_pk_bf16_f32 v196, v136, v137\n"
    "s_nop 0\n"
    "v_cvt_pk_bf16_f32 v198, v140, v141\n"
    "v_cvt_pk_bf16_f32 v197, v138, v139\n"
    "v_cvt_pk_bf16_f32 v199, v142, v143\n"
    "v_permlane16_swap_b32 v196, v198\n"
    "s_nop 0\n"
    "v_permlane16_swap_b32 v197, v199\n"
    "buffer_store_dwordx4 v[196:199], v13, s[36:39], s72 offen offset:3072 sc1\n"
    "s_waitcnt vmcnt(0)\n"
    "v_mov_b32 v8, s34\n"
    "v_mov_b32 v9, s34\n"
    "v_mov_b32 v10, s34\n"
    "v_mov_b32_e64 v11, s34\n"
    "s_mov_b64 exec, 0xff\n"
    "buffer_store_dwordx4 v[8:11], v13, s[36:39], s73 offen sc1\n"
    "s_mov_b64 exec, -1\n"
    "s_barrier\n"
    ".Lp2_4:\n"
    "s_barrier\n"
    "s_and_b32 s60, s35, 1\n"
    "s_lshr_b32 s61, s35, 1\n"
    "s_mul_i32 s62, s58, 221184\n"
    "s_lshl1_add_u32 s67, s60, s61\n"
    "v_lshlrev_b32 v14, 3, v3\n"
    "s_lshl_b32 s65, s67, 10\n"
    "s_add_u32 s62, s62, 2097152\n"
    "s_lshl_b32 s69, s61, 13\n"
    "s_mul_i32 s66, s67, 2560\n"
    "s_add_u32 s63, s62, s69\n"
    "s_add_u32 s14, s56, 0\n"
    "v_add_u32 v14, 40960, v14\n"
    "s_sub_u32 s64, s14, 9\n"
    "v_mul_u32_u24 v199, 160, v4\n"
    "s_cselect_b32 s14, s14, s64\n"
    "v_add3_u32 v199, v199, v14, s66\n"
    "s_lshl_b32 s14, s14, 14\n"
    "ds_read_b64 v[242:243], v199 offset:0\n"
    "s_add_u32 s14, s14, s63\n"
    "s_add_u32 s15, s56, 1\n"
    "ds_read_b64 v[110:111], v14 offset:10240\n"
    "s_sub_u32 s64, s15, 9\n"
    "ds_read_b64 v[244:245], v199 offset:32\n"
    "s_cselect_b32 s15, s15, s64\n"
    "ds_read_b64 v[112:113], v14 offset:10272\n"
    "s_lshl_b32 s15, s15, 14\n"
    "ds_read_b64 v[246:247], v199 offset:64\n"
    "s_add_u32 s15, s15, s63\n"
    "s_add_u32 s16, s56, 2\n"
    "ds_read_b64 v[114:115], v14 offset:10304\n"
    "s_sub_u32 s64, s16, 9\n"
    "ds_read_b64 v[248:249], v199 offset:96\n"
    "s_cselect_b32 s16, s16, s64\n"
    "ds_read_b64 v[116:117], v14 offset:10336\n"
    "s_lshl_b32 s16, s16, 14\n"
    "ds_read_b64 v[250:251], v199 offset:128\n"
    "s_add_u32 s16, s16, s63\n"
    "s_add_u32 s17, s56, 3\n"
    "ds_read_b64 v[118:119], v14 offset:10368\n"
    "s_sub_u32 s64, s17, 9\n"
    "v_lshrrev_b32 v7, 1, v3\n"
    "s_cselect_b32 s17, s17, s64\n"
    "v_and_b32 v8, 1, v3\n"
    "s_lshl_b32 s17, s17, 14\n"
    "v_lshrrev_b32 v9, 1, v4\n"
    "s_add_u32 s17, s17, s63\n"
    "s_add_u32 s18, s56, 4\n"
    "v_mul_u32_u24 v8, 10240, v8\n"
    "s_sub_u32 s64, s18, 9\n"
    "v_lshlrev_b32 v13, 4, v0\n"
    "s_cselect_b32 s18, s18, s64\n"
    "v_add_u32 v194, s65, v13\n"
    "s_lshl_b32 s18, s18, 14\n"
    "v_xor_b32 v195, 1024, v194\n"
    "s_add_u32 s18, s18, s63\n"
    "s_add_u32 s19, s56, 5\n"
    "s_waitcnt lgkmcnt(9)\n"
    "v_lshlrev_b32 v16, 16, v242\n"
    "s_sub_u32 s64, s19, 9\n"
    "v_and_b32 v17, 0xffff0000, v242\n"
    "s_cselect_b32 s19, s19, s64\n"
    "s_waitcnt lgkmcnt(8)\n"
    "v_lshlrev_b32 v120, 16, v110\n"
    "s_lshl_b32 s19, s19, 14\n"
    "v_and_b32 v121, 0xffff0000, v110\n"
    "s_add_u32 s19, s19, s63\n"
    "s_add_u32 s20, s56, 6\n"
    "v_lshlrev_b32 v18, 16, v243\n"
    "s_sub_u32 s64, s20, 9\n"
    "v_and_b32 v19, 0xffff0000, v243\n"
    "s_cselect_b32 s20, s20, s64\n"
    "v_lshlrev_b32 v122, 16, v111\n"
    "s_lshl_b32 s20, s20, 14\n"
    "v_and_b32 v123, 0xffff0000, v111\n"
    "s_add_u32 s20, s20, s63\n"
    "s_add_u32 s21, s56, 7\n"
    "s_waitcnt lgkmcnt(7)\n"
    "v_lshlrev_b32 v20, 16, v244\n"
    "s_sub_u32 s64, s21, 9\n"
    "v_and_b32 v21, 0xffff0000, v244\n"
    "s_cselect_b32 s21, s21, s64\n"
    "s_waitcnt lgkmcnt(6)\n"
    "v_lshlrev_b32 v124, 16, v112\n"
    "s_lshl_b32 s21, s21, 14\n"
    "v_and_b32 v125, 0xffff0000, v112\n"
    "s_add_u32 s21, s21, s63\n"
    "s_add_u32 s22, s56, 8\n"
    "v_lshlrev_b32 v22, 16, v245\n"
    "s_sub_u32 s64, s22, 9\n"
    "v_and_b32 v23, 0xffff0000, v245\n"
    "s_cselect_b32 s22, s22, s64\n"
    "v_lshlrev_b32 v126, 16, v113\n"
    "s_lshl_b32 s22, s22, 14\n"
    "v_and_b32 v127, 0xffff0000, v113\n"
    "s_add_u32 s22, s22, s63\n"
    "s_lshl_b32 s68, s61, 2\n"
    "s_waitcnt lgkmcnt(5)\n"
    "v_lshlrev_b32 v24, 16, v246\n"
    "v_or_b32 v7, s68, v7\n"
    "v_and_b32 v25, 0xffff0000, v246\n"
    "v_xor_b32 v10, v7, v9\n"
    "s_waitcnt lgkmcnt(4)\n"
    "v_lshlrev_b32_e64 v128, 16, v114\n"
    "v_lshl_add_u32 v10, v10, 4, v8\n"
    "v_and_b32 v129, 0xffff0000, v114\n"
    "v_lshl_add_u32 v204, v4, 7, v10\n"
    "v_lshlrev_b32 v26, 16, v247\n"
    "v_xor_b32 v205, 32, v204\n"
    "v_and_b32 v27, 0xffff0000, v247\n"
    "v_lshlrev_b32 v130, 16, v115\n"
    "v_and_b32 v131, 0xffff0000, v115\n"
    "s_waitcnt lgkmcnt(3)\n"
    "v_lshlrev_b32 v28, 16, v248\n"
    "v_and_b32 v29, 0xffff0000, v248\n"
    "s_waitcnt lgkmcnt(2)\n"
    "v_lshlrev_b32 v132, 16, v116\n"
    "v_and_b32 v133, 0xffff0000, v116\n"
    "v_lshlrev_b32 v30, 16, v249\n"
    "v_and_b32 v31, 0xffff0000, v249\n"
    "v_lshlrev_b32 v134, 16, v117\n"
    "v_and_b32 v135, 0xffff0000, v117\n"
    "s_waitcnt lgkmcnt(1)\n"
    "v_lshlrev_b32 v32, 16, v250\n"
    "v_and_b32 v33, 0xffff0000, v250\n"
    "s_waitcnt lgkmcnt(0)\n"
    "v_lshlrev_b32 v136, 16, v118\n"
    "v_and_b32 v137, 0xffff0000, v118\n"
    "v_lshlrev_b32 v34, 16, v251\n"
    "v_and_b32 v35, 0xffff0000, v251\n"
    "v_lshlrev_b32_e64 v138, 16, v119\n"
    "v_and_b32 v139, 0xffff0000, v119\n"
    "ds_read_b128 v[144:147], v204 offset:0\n"
    "ds_read_b128 v[148:151], v204 offset:2048\n"
    "ds_read_b128 v[152:155], v204 offset:4096\n"
    "ds_read_b128 v[156:159], v204 offset:6144\n"
    "ds_read_b128 v[160:163], v204 offset:8192\n"
    "ds_read_b128 v[164:167], v205 offset:0\n"
    "ds_read_b128 v[168:171], v205 offset:2048\n"
    "ds_read_b128 v[172:175], v205 offset:4096\n"
    "ds_read_b128 v[176:179], v205 offset:6144\n"
    "ds_read_b128 v[180:183], v205 offset:8192\n"
    "ds_read_b128 v[56:59], v204 offset:20480\n"
    "ds_read_b128 v[60:63], v204 offset:22528\n"
    "ds_read_b128 v[64:67], v204 offset:24576\n"
    "ds_read_b128 v[68:71], v204 offset:26624\n"
    "ds_read_b128 v[72:75], v204 offset:28672\n"
    "ds_read_b128 v[76:79], v205 offset:20480\n"
    "ds_read_b128 v[80:83], v205 offset:22528\n"
    "ds_read_b128 v[84:87], v205 offset:24576\n"
    "ds_read_b128 v[88:91], v205 offset:26624\n"
    "ds_read_b128 v[92:95], v205 offset:28672\n"
    "v_bfe_u32 v5, v0, 2, 2\n"
    "v_and_b32_e64 v6, 3, v0\n"
    "v_add_u32 v196, 4096, v194\n"
    "v_add_u32 v197, 4096, v195\n"
    "v_pk_mul_f32 v[16:17], v[16:17], v[120:121]\n"
    "v_pk_mul_f32 v[18:19], v[18:19], v[122:123]\n"
    "v_pk_mul_f32 v[20:21], v[20:21], v[124:125]\n"
    "v_pk_mul_f32 v[22:23], v[22:23], v[126:127]\n"
    "v_pk_mul_f32 v[24:25], v[24:25], v[128:129]\n"
    "v_pk_mul_f32 v[26:27], v[26:27], v[130:131]\n"
    "v_pk_mul_f32 v[28:29], v[28:29], v[132:133]\n"
    "v_pk_mul_f32 v[30:31], v[30:31], v[134:135]\n"
    "v_pk_mul_f32 v[32:33], v[32:33], v[136:137]\n"
    "v_pk_mul_f32 v[34:35], v[34:35], v[138:139]\n"
    "s_waitcnt lgkmcnt(0)\n"
    "s_barrier\n"
    "buffer_load_dwordx4 v[96:99], v194, s[36:39], s14 offen sc1\n"
    "buffer_load_dwordx4 v[100:103], v195, s[36:39], s14 offen sc1\n"
    "buffer_load_dwordx4 v[104:107], v196, s[36:39], s14 offen sc1\n"
    "buffer_load_dwordx4 v[108:111], v197, s[36:39], s14 offen sc1\n"
    "buffer_load_dwordx4 v[112:115], v194, s[36:39], s15 offen sc1\n"
    "buffer_load_dwordx4 v[116:119], v195, s[36:39], s15 offen sc1\n"
    "buffer_load_dwordx4 v[120:123], v196, s[36:39], s15 offen sc1\n"
    "buffer_load_dwordx4 v[124:127], v197, s[36:39], s15 offen sc1\n"
    "v_lshlrev_b32 v7, 7, v4\n"
    "v_lshlrev_b32 v12, 4, v4\n"
    "s_lshl_b32 s76, s58, 11\n"
    "s_add_u32 s76, s76, 1966080\n"
    "s_lshl2_add_u32 s66, s58, s67\n"
    "s_lshl_b32 s66, s66, 8\n"
    "s_add_u32 s66, s66, 90112\n"
    "s_xor_b32 s68, s66, 256\n"
    "v_and_b32 v9, 1, v3\n"
    "v_lshl_add_u32 v9, v9, 2, v5\n"
    "v_lshrrev_b32_e64 v10, 1, v3\n"
    "v_lshl_add_u32 v10, v10, 4, v9\n"
    "v_mul_u32_u24 v10, 160, v10\n"
    "s_mul_i32 s69, s61, 5120\n"
    "s_add_u32 s69, s69, 61440\n"
    "v_add_u32 v10, s69, v10\n"
    "v_lshl_add_u32 v200, v6, 3, v10\n"
    "s_lshl_b32 s69, s61, 5\n"
    "s_add_u32 s69, s69, s67\n"
    "s_lshl_b32 s69, s69, 7\n"
    "s_add_u32 s69, s69, 212992\n"
    "v_lshlrev_b32 v14, 3, v4\n"
    "v_and_b32 v15, 2, v3\n"
    "v_add_u32 v15, v15, v3\n"
    "v_lshl_add_u32 v14, v15, 9, v14\n"
    "v_add_u32 v14, s69, v14\n"
    "v_xor_b32 v15, 128, v14\n"
    "s_lshl_b32 s69, s58, 5\n"
    "s_add_u32 s69, s69, s67\n"
    "s_mul_i32 s69, s69, 4608\n"
    "s_and_b32 s70, s54, 3\n"
    "s_mul_i32 s70, s70, 320\n"
    "s_add_u32 s70, s70, 1024\n"
    "s_add_u32 s69, s69, s70\n"
    "s_mul_i32 s70, s35, 5120\n"
    "s_add_u32 s70, s70, 0\n"
    "s_xor_b32 s70, s35, 2\n"
    "s_mul_i32 s70, s70, 5120\n"
    "s_add_u32 s70, s70, 0\n"
    "s_mov_b32 s40, s32\n"
    "s_and_b32 s41, s33, 0xffff\n"
    "s_mov_b32 s42, 4718592\n"
    "s_mov_b32 s43, 0x20000\n"
    "s_lshl2_add_u32 s70, s61, s67\n"
    "s_mul_i32 s71, s70, 5376\n"
    "s_xor_b32 s70, s70, 1\n"
    "s_mul_i32 s70, s70, 5376\n"
    "v_mul_u32_u24 v9, 336, v4\n"
    "v_lshl_add_u32 v9, v3, 4, v9\n"
    "v_add_u32 v8, s71, v9\n"
    "v_add_u32 v9, s70, v9\n"
    "s_mul_i32 s70, s67, 5376\n"
    "v_add_u32 v10, 0, v0\n"
    "v_mul_u32_u24 v11, 3277, v10\n"
    "v_lshrrev_b32 v11, 16, v11\n"
    "v_add_lshl_u32 v206, v10, v11, 4\n"
    "v_add_u32 v206, s70, v206\n"
    "v_mul_u32_u24 v211, 20, v11\n"
    "v_sub_u32 v10, v10, v211\n"
    "v_lshrrev_b32 v211, 2, v11\n"
    "v_lshlrev_b32_e64 v211, 1, v211\n"
    "v_and_or_b32 v211, v11, 1, v211\n"
    "v_bfe_u32 v11, v11, 1, 1\n"
    "v_lshl_add_u32 v211, v211, 3, v11\n"
    "v_mul_u32_u24 v211, 2304, v211\n"
    "v_lshl_add_u32 v211, v10, 4, v211\n"
    "v_add_u32 v10, 64, v0\n"
    "v_mul_u32_u24 v11, 3277, v10\n"
    "v_lshrrev_b32 v11, 16, v11\n"
    "v_add_lshl_u32 v207, v10, v11, 4\n"
    "v_add_u32 v207, s70, v207\n"
    "v_mul_u32_u24 v212, 20, v11\n"
    "v_sub_u32 v10, v10, v212\n"
    "v_lshrrev_b32 v212, 2, v11\n"
    "v_lshlrev_b32_e64 v212, 1, v212\n"
    "v_and_or_b32 v212, v11, 1, v212\n"
    "v_bfe_u32 v11, v11, 1, 1\n"
    "v_lshl_add_u32 v212, v212, 3, v11\n"
    "v_mul_u32_u24 v212, 2304, v212\n"
    "v_lshl_add_u32 v212, v10, 4, v212\n"
    "v_add_u32 v10, 128, v0\n"
    "v_mul_u32_u24 v11, 3277, v10\n"
    "v_lshrrev_b32 v11, 16, v11\n"
    "v_add_lshl_u32 v208, v10, v11, 4\n"
    "v_add_u32 v208, s70, v208\n"
    "v_mul_u32_u24 v213, 20, v11\n"
    "v_sub_u32 v10, v10, v213\n"
    "v_lshrrev_b32 v213, 2, v11\n"
    "v_lshlrev_b32 v213, 1, v213\n"
    "v_and_or_b32 v213, v11, 1, v213\n"
    "v_bfe_u32 v11, v11, 1, 1\n"
    "v_lshl_add_u32 v213, v213, 3, v11\n"
    "v_mul_u32_u24 v213, 2304, v213\n"
    "v_lshl_add_u32 v213, v10, 4, v213\n"
    "v_add_u32 v10, 192, v0\n"
    "v_mul_u32_u24 v11, 3277, v10\n"
    "v_lshrrev_b32 v11, 16, v11\n"
    "v_add_lshl_u32 v209, v10, v11, 4\n"
    "v_add_u32 v209, s70, v209\n"
    "v_mul_u32_u24 v214, 20, v11\n"
    "v_sub_u32 v10, v10, v214\n"
    "v_lshrrev_b32 v214, 2, v11\n"
    "v_lshlrev_b32 v214, 1, v214\n"
    "v_and_or_b32 v214, v11, 1, v214\n"
    "v_bfe_u32 v11, v11, 1, 1\n"
    "v_lshl_add_u32 v214, v214, 3, v11\n"
    "v_mul_u32_u24 v214, 2304, v214\n"
    "v_lshl_add_u32 v214, v10, 4, v214\n"
    "v_add_u32 v10, 256, v0\n"
    "s_waitcnt vmcnt(7)\n"
    "v_mfma_f32_16x16x32_bf16 v[16:19], v[144:147], v[96:99], v[16:19]\n"
    "v_mul_u32_u24 v11, 3277, v10\n"
    "v_lshrrev_b32 v11, 16, v11\n"
    "s_waitcnt vmcnt(6)\n"
    "v_mfma_f32_16x16x32_bf16 v[36:39], v[144:147], v[100:103], 0\n"
    "v_add_lshl_u32 v210, v10, v11, 4\n"
    "v_add_u32 v210, s70, v210\n"
    "v_mfma_f32_16x16x32_bf16 v[20:23], v[148:151], v[96:99], v[20:23]\n"
    "buffer_load_dwordx4 v[128:131], v194, s[36:39], s16 offen sc1\n"
    "v_mfma_f32_16x16x32_bf16 v[40:43], v[148:151], v[100:103], 0\n"
    "v_mul_u32_u24 v215, 20, v11\n"
    "v_sub_u32 v10, v10, v215\n"
    "v_mfma_f32_16x16x32_bf16 v[24:27], v[152:155], v[96:99], v[24:27]\n"
    "v_lshrrev_b32 v215, 2, v11\n"
    "v_lshlrev_b32 v215, 1, v215\n"
    "v_mfma_f32_16x16x32_bf16 v[44:47], v[152:155], v[100:103], 0\n"
    "v_and_or_b32 v215, v11, 1, v215\n"
    "v_bfe_u32 v11, v11, 1, 1\n"
    "v_mfma_f32_16x16x32_bf16 v[28:31], v[156:159], v[96:99], v[28:31]\n"
    "v_lshl_add_u32 v215, v215, 3, v11\n"
    "v_mul_u32_u24 v215, 2304, v215\n"
    "v_mfma_f32_16x16x32_bf16 v[48:51], v[156:159], v[100:103], 0\n"
    "buffer_load_dwordx4 v[132:135], v195, s[36:39], s16 offen sc1\n"
    "v_mfma_f32_16x16x32_bf16 v[32:35], v[160:163], v[96:99], v[32:35]\n"
    "v_lshl_add_u32 v215, v10, 4, v215\n"
    "s_lshl_b32 s65, s61, 12\n"
    "v_mfma_f32_16x16x32_bf16 v[52:55], v[160:163], v[100:103], 0\n"
    "s_add_u32 s63, s62, s65\n"
    "s_add_u32 s63, s63, 147456\n"
    "s_waitcnt vmcnt(7)\n"
    "v_mfma_f32_16x16x32_bf16 v[16:19], v[164:167], v[104:107], v[16:19]\n"
    "s_add_u32 s23, s80, 0\n"
    "s_and_b32 s23, s23, 7\n"
    "s_waitcnt vmcnt(6)\n"
    "v_mfma_f32_16x16x32_bf16 v[36:39], v[164:167], v[108:111], v[36:39]\n"
    "s_lshl_b32 s23, s23, 13\n"
    "s_add_u32 s23, s23, s63\n"
    "v_mfma_f32_16x16x32_bf16 v[20:23], v[168:171], v[104:107], v[20:23]\n"
    "buffer_load_dwordx4 v[136:139], v196, s[36:39], s16 offen sc1\n"
    "v_mfma_f32_16x16x32_bf16 v[40:43], v[168:171], v[108:111], v[40:43]\n"
    "s_add_u32 s24, s80, 1\n"
    "s_and_b32 s24, s24, 7\n"
    "v_mfma_f32_16x16x32_bf16 v[24:27], v[172:175], v[104:107], v[24:27]\n"
    "s_lshl_b32 s24, s24, 13\n"
    "s_add_u32 s24, s24, s63\n"
    "v_mfma_f32_16x16x32_bf16 v[44:47], v[172:175], v[108:111], v[44:47]\n"
    "s_add_u32 s25, s80, 2\n"
    "s_and_b32 s25, s25, 7\n"
    "v_mfma_f32_16x16x32_bf16 v[28:31], v[176:179], v[104:107], v[28:31]\n"
    "s_lshl_b32 s25, s25, 13\n"
    "s_add_u32 s25, s25, s63\n"
    "v_mfma_f32_16x16x32_bf16 v[48:51], v[176:179], v[108:111], v[48:51]\n"
    "buffer_load_dwordx4 v[140:143], v197, s[36:39], s16 offen sc1\n"
    "v_mfma_f32_16x16x32_bf16 v[32:35], v[180:183], v[104:107], v[32:35]\n"
    "s_add_u32 s26, s80, 3\n"
    "s_and_b32 s26, s26, 7\n"
    "v_mfma_f32_16x16x32_bf16 v[52:55], v[180:183], v[108:111], v[52:55]\n"
    "s_lshl_b32 s26, s26, 13\n"
    "s_add_u32 s26, s26, s63\n"
    "s_waitcnt vmcnt(7)\n"
    "v_mfma_f32_16x16x32_bf16 v[16:19], v[56:59], v[112:115], v[16:19]\n"
    "s_add_u32 s27, s80, 4\n"
    "s_and_b32 s27, s27, 7\n"
    "s_waitcnt vmcnt(6)\n"
    "v_mfma_f32_16x16x32_bf16 v[36:39], v[56:59], v[116:119], v[36:39]\n"
    "s_lshl_b32 s27, s27, 13\n"
    "s_add_u32 s27, s27, s63\n"
    "v_mfma_f32_16x16x32_bf16 v[20:23], v[60:63], v[112:115], v[20:23]\n"
    "buffer_load_dwordx4 v[96:99], v194, s[36:39], s17 offen sc1\n"
    "v_mfma_f32_16x16x32_bf16 v[40:43], v[60:63], v[116:119], v[40:43]\n"
    "s_add_u32 s28, s80, 5\n"
    "s_and_b32 s28, s28, 7\n"
    "v_mfma_f32_16x16x32_bf16 v[24:27], v[64:67], v[112:115], v[24:27]\n"
    "s_lshl_b32 s28, s28, 13\n"
    "s_add_u32 s28, s28, s63\n"
    "v_mfma_f32_16x16x32_bf16 v[44:47], v[64:67], v[116:119], v[44:47]\n"
    "s_add_u32 s29, s80, 6\n"
    "s_and_b32 s29, s29, 7\n"
    "v_mfma_f32_16x16x32_bf16 v[28:31], v[68:71], v[112:115], v[28:31]\n"
    "s_lshl_b32 s29, s29, 13\n"
    "s_add_u32 s29, s29, s63\n"
    "v_mfma_f32_16x16x32_bf16 v[48:51], v[68:71], v[116:119], v[48:51]\n"
    "buffer_load_dwordx4 v[100:103], v195, s[36:39], s17 offen sc1\n"
    "v_mfma_f32_16x16x32_bf16 v[32:35], v[72:75], v[112:115], v[32:35]\n"
    "s_add_u32 s30, s80, 7\n"
    "s_and_b32 s30, s30, 7\n"
    "v_mfma_f32_16x16x32_bf16 v[52:55], v[72:75], v[116:119], v[52:55]\n"
    "s_lshl_b32 s30, s30, 13\n"
    "s_add_u32 s30, s30, s63\n"
    "s_waitcnt lgkmcnt(0)\n"
    "s_barrier\n"
    "s_waitcnt vmcnt(7)\n"
    "v_mfma_f32_16x16x32_bf16 v[16:19], v[76:79], v[120:123], v[16:19]\n"
    "ds_read_b128 v[144:147], v204 offset:0\n"
    "s_waitcnt vmcnt(6)\n"
    "v_mfma_f32_16x16x32_bf16 v[36:39], v[76:79], v[124:127], v[36:39]\n"
    "ds_read_b128 v[148:151], v204 offset:2048\n"
    "v_mfma_f32_16x16x32_bf16 v[20:23], v[80:83], v[120:123], v[20:23]\n"
    "buffer_load_dwordx4 v[104:107], v196, s[36:39], s17 offen sc1\n"
    "v_mfma_f32_16x16x32_bf16 v[40:43], v[80:83], v[124:127], v[40:43]\n"
    "ds_read_b128 v[152:155], v204 offset:4096\n"
    "v_mfma_f32_16x16x32_bf16 v[24:27], v[84:87], v[120:123], v[24:27]\n"
    "ds_read_b128 v[156:159], v204 offset:6144\n"
    "v_mfma_f32_16x16x32_bf16 v[44:47], v[84:87], v[124:127], v[44:47]\n"
    "ds_read_b128 v[160:163], v204 offset:8192\n"
    "v_mfma_f32_16x16x32_bf16 v[28:31], v[88:91], v[120:123], v[28:31]\n"
    "v_mfma_f32_16x16x32_bf16 v[48:51], v[88:91], v[124:127], v[48:51]\n"
    "buffer_load_dwordx4 v[108:111], v197, s[36:39], s17 offen sc1\n"
    "v_mfma_f32_16x16x32_bf16 v[32:35], v[92:95], v[120:123], v[32:35]\n"
    "v_mfma_f32_16x16x32_bf16 v[52:55], v[92:95], v[124:127], v[52:55]\n"
    "s_waitcnt vmcnt(7) lgkmcnt(4)\n"
    "v_mfma_f32_16x16x32_bf16 v[16:19], v[144:147], v[128:131], v[16:19]\n"
    "ds_read_b128 v[164:167], v205 offset:0\n"
    "s_waitcnt vmcnt(6)\n"
    "v_mfma_f32_16x16x32_bf16 v[36:39], v[144:147], v[132:135], v[36:39]\n"
    "ds_read_b128 v[168:171], v205 offset:2048\n"
    "s_waitcnt lgkmcnt(5)\n"
    "v_mfma_f32_16x16x32_bf16 v[20:23], v[148:151], v[128:131], v[20:23]\n"
    "buffer_load_dwordx4 v[112:115], v194, s[36:39], s18 offen sc1\n"
    "v_mfma_f32_16x16x32_bf16 v[40:43], v[148:151], v[132:135], v[40:43]\n"
    "ds_read_b128 v[172:175], v205 offset:4096\n"
    "s_waitcnt lgkmcnt(5)\n"
    "v_mfma_f32_16x16x32_bf16 v[24:27], v[152:155], v[128:131], v[24:27]\n"
    "ds_read_b128 v[176:179], v205 offset:6144\n"
    "v_mfma_f32_16x16x32_bf16 v[44:47], v[152:155], v[132:135], v[44:47]\n"
    "ds_read_b128 v[180:183], v205 offset:8192\n"
    "s_waitcnt lgkmcnt(6)\n"
    "v_mfma_f32_16x16x32_bf16 v[28:31], v[156:159], v[128:131], v[28:31]\n"
    "v_mfma_f32_16x16x32_bf16 v[48:51], v[156:159], v[132:135], v[48:51]\n"
    "buffer_load_dwordx4 v[116:119], v195, s[36:39], s18 offen sc1\n"
    "s_waitcnt lgkmcnt(5)\n"
    "v_mfma_f32_16x16x32_bf16 v[32:35], v[160:163], v[128:131], v[32:35]\n"
    "v_mfma_f32_16x16x32_bf16 v[52:55], v[160:163], v[132:135], v[52:55]\n"
    "s_waitcnt lgkmcnt(0)\n"
    "s_barrier\n"
    "s_waitcnt vmcnt(7)\n"
    "v_mfma_f32_16x16x32_bf16 v[16:19], v[164:167], v[136:139], v[16:19]\n"
    "ds_read_b128 v[144:147], v204 offset:20480\n"
    "s_waitcnt vmcnt(6)\n"
    "v_mfma_f32_16x16x32_bf16 v[36:39], v[164:167], v[140:143], v[36:39]\n"
    "ds_read_b128 v[148:151], v204 offset:22528\n"
    "v_mfma_f32_16x16x32_bf16 v[20:23], v[168:171], v[136:139], v[20:23]\n"
    "buffer_load_dwordx4 v[120:123], v196, s[36:39], s18 offen sc1\n"
    "v_mfma_f32_16x16x32_bf16 v[40:43], v[168:171], v[140:143], v[40:43]\n"
    "ds_read_b128 v[152:155], v204 offset:24576\n"
    "v_mfma_f32_16x16x32_bf16 v[24:27], v[172:175], v[136:139], v[24:27]\n"
    "ds_read_b128 v[156:159], v204 offset:26624\n"
    "v_mfma_f32_16x16x32_bf16 v[44:47], v[172:175], v[140:143], v[44:47]\n"
    "ds_read_b128 v[160:163], v204 offset:28672\n"
    "v_mfma_f32_16x16x32_bf16 v[28:31], v[176:179], v[136:139], v[28:31]\n"
    "v_mfma_f32_16x16x32_bf16 v[48:51], v[176:179], v[140:143], v[48:51]\n"
    "buffer_load_dwordx4 v[124:127], v197, s[36:39], s18 offen sc1\n"
    "v_mfma_f32_16x16x32_bf16 v[32:35], v[180:183], v[136:139], v[32:35]\n"
    "v_mfma_f32_16x16x32_bf16 v[52:55], v[180:183], v[140:143], v[52:55]\n"
    "s_waitcnt vmcnt(7) lgkmcnt(4)\n"
    "v_mfma_f32_16x16x32_bf16 v[16:19], v[144:147], v[96:99], v[16:19]\n"
    "ds_read_b128 v[164:167], v205 offset:20480\n"
    "s_waitcnt vmcnt(6)\n"
    "v_mfma_f32_16x16x32_bf16 v[36:39], v[144:147], v[100:103], v[36:39]\n"
    "ds_read_b128 v[168:171], v205 offset:22528\n"
    "s_waitcnt lgkmcnt(5)\n"
    "v_mfma_f32_16x16x32_bf16 v[20:23], v[148:151], v[96:99], v[20:23]\n"
    "buffer_load_dwordx4 v[128:131], v194, s[36:39], s19 offen sc1\n"
    "v_mfma_f32_16x16x32_bf16 v[40:43], v[148:151], v[100:103], v[40:43]\n"
    "ds_read_b128 v[172:175], v205 offset:24576\n"
    "s_waitcnt lgkmcnt(5)\n"
    "v_mfma_f32_16x16x32_bf16 v[24:27], v[152:155], v[96:99], v[24:27]\n"
    "ds_read_b128 v[176:179], v205 offset:26624\n"
    "v_mfma_f32_16x16x32_bf16 v[44:47], v[152:155], v[100:103], v[44:47]\n"
    "ds_read_b128 v[180:183], v205 offset:28672\n"
    "s_waitcnt lgkmcnt(6)\n"
    "v_mfma_f32_16x16x32_bf16 v[28:31], v[156:159], v[96:99], v[28:31]\n"
    "v_mfma_f32_16x16x32_bf16 v[48:51], v[156:159], v[100:103], v[48:51]\n"
    "buffer_load_dwordx4 v[132:135], v195, s[36:39], s19 offen sc1\n"
    "s_waitcnt lgkmcnt(5)\n"
    "v_mfma_f32_16x16x32_bf16 v[32:35], v[160:163], v[96:99], v[32:35]\n"
    "v_mfma_f32_16x16x32_bf16 v[52:55], v[160:163], v[100:103], v[52:55]\n"
    "s_waitcnt lgkmcnt(0)\n"
    "s_barrier\n"
    "s_waitcnt vmcnt(7)\n"
    "v_mfma_f32_16x16x32_bf16 v[16:19], v[164:167], v[104:107], v[16:19]\n"
    "ds_read_b128 v[144:147], v204 offset:40960\n"
    "s_waitcnt vmcnt(6)\n"
    "v_mfma_f32_16x16x32_bf16 v[36:39], v[164:167], v[108:111], v[36:39]\n"
    "ds_read_b128 v[148:151], v204 offset:43008\n"
    "v_mfma_f32_16x16x32_bf16 v[20:23], v[168:171], v[104:107], v[20:23]\n"
    "buffer_load_dwordx4 v[136:139], v196, s[36:39], s19 offen sc1\n"
    "v_mfma_f32_16x16x32_bf16 v[40:43], v[168:171], v[108:111], v[40:43]\n"
    "ds_read_b128 v[152:155], v204 offset:45056\n"
    "v_mfma_f32_16x16x32_bf16 v[24:27], v[172:175], v[104:107], v[24:27]\n"
    "ds_read_b128 v[156:159], v204 offset:47104\n"
    "v_mfma_f32_16x16x32_bf16 v[44:47], v[172:175], v[108:111], v[44:47]\n"
    "ds_read_b128 v[160:163], v204 offset:49152\n"
    "v_mfma_f32_16x16x32_bf16 v[28:31], v[176:179], v[104:107], v[28:31]\n"
    "v_mfma_f32_16x16x32_bf16 v[48:51], v[176:179], v[108:111], v[48:51]\n"
    "buffer_load_dwordx4 v[140:143], v197, s[36:39], s19 offen sc1\n"
    "v_mfma_f32_16x16x32_bf16 v[32:35], v[180:183], v[104:107], v[32:35]\n"
    "v_mfma_f32_16x16x32_bf16 v[52:55], v[180:183], v[108:111], v[52:55]\n"
    "s_waitcnt vmcnt(7) lgkmcnt(4)\n"
    "v_mfma_f32_16x16x32_bf16 v[16:19], v[144:147], v[112:115], v[16:19]\n"
    "ds_read_b128 v[164:167], v205 offset:40960\n"
    "s_waitcnt vmcnt(6)\n"
    "v_mfma_f32_16x16x32_bf16 v[36:39], v[144:147], v[116:119], v[36:39]\n"
    "ds_read_b128 v[168:171], v205 offset:43008\n"
    "s_waitcnt lgkmcnt(5)\n"
    "v_mfma_f32_16x16x32_bf16 v[20:23], v[148:151], v[112:115], v[20:23]\n"
    "buffer_load_dwordx4 v[96:99], v194, s[36:39], s20 offen sc1\n"
    "v_mfma_f32_16x16x32_bf16 v[40:43], v[148:151], v[116:119], v[40:43]\n"
    "ds_read_b128 v[172:175], v205 offset:45056\n"
    "s_waitcnt lgkmcnt(5)\n"
    "v_mfma_f32_16x16x32_bf16 v[24:27], v[152:155], v[112:115], v[24:27]\n"
    "ds_read_b128 v[176:179], v205 offset:47104\n"
    "v_mfma_f32_16x16x32_bf16 v[44:47], v[152:155], v[116:119], v[44:47]\n"
    "ds_read_b128 v[180:183], v205 offset:49152\n"
    "s_waitcnt lgkmcnt(6)\n"
    "v_mfma_f32_16x16x32_bf16 v[28:31], v[156:159], v[112:115], v[28:31]\n"
    "v_mfma_f32_16x16x32_bf16 v[48:51], v[156:159], v[116:119], v[48:51]\n"
    "buffer_load_dwordx4 v[100:103], v195, s[36:39], s20 offen sc1\n"
    "s_waitcnt lgkmcnt(5)\n"
    "v_mfma_f32_16x16x32_bf16 v[32:35], v[160:163], v[112:115], v[32:35]\n"
    "v_mfma_f32_16x16x32_bf16 v[52:55], v[160:163], v[116:119], v[52:55]\n"
    "s_waitcnt lgkmcnt(0)\n"
    "s_barrier\n"
    "s_waitcnt vmcnt(7)\n"
    "v_mfma_f32_16x16x32_bf16 v[16:19], v[164:167], v[120:123], v[16:19]\n"
    "ds_read_b128 v[144:147], v204 offset:0\n"
    "s_waitcnt vmcnt(6)\n"
    "v_mfma_f32_16x16x32_bf16 v[36:39], v[164:167], v[124:127], v[36:39]\n"
    "buffer_load_dwordx4 v[104:107], v196, s[36:39], s20 offen sc1\n"
    "v_mfma_f32_16x16x32_bf16 v[20:23], v[168:171], v[120:123], v[20:23]\n"
    "ds_read_b128 v[148:151], v204 offset:2048\n"
    "v_mfma_f32_16x16x32_bf16 v[40:43], v[168:171], v[124:127], v[40:43]\n"
    "buffer_load_dwordx4 v[108:111], v197, s[36:39], s20 offen sc1\n"
    "v_mfma_f32_16x16x32_bf16 v[24:27], v[172:175], v[120:123], v[24:27]\n"
    "ds_read_b128 v[152:155], v204 offset:4096\n"
    "v_mfma_f32_16x16x32_bf16 v[44:47], v[172:175], v[124:127], v[44:47]\n"
    "ds_read_b128 v[156:159], v204 offset:6144\n"
    "v_mfma_f32_16x16x32_bf16 v[28:31], v[176:179], v[120:123], v[28:31]\n"
    "buffer_load_dwordx4 v[184:187], v12, s[36:39], s66 offen sc1\n"
    "v_mfma_f32_16x16x32_bf16 v[48:51], v[176:179], v[124:127], v[48:51]\n"
    "ds_read_b128 v[160:163], v204 offset:8192\n"
    "v_mfma_f32_16x16x32_bf16 v[32:35], v[180:183], v[120:123], v[32:35]\n"
    "buffer_load_dwordx4 v[188:191], v12, s[36:39], s68 offen sc1\n"
    "v_mfma_f32_16x16x32_bf16 v[52:55], v[180:183], v[124:127], v[52:55]\n"
    "s_waitcnt vmcnt(9) lgkmcnt(4)\n"
    "v_mfma_f32_16x16x32_bf16 v[16:19], v[144:147], v[128:131], v[16:19]\n"
    "ds_read_b128 v[164:167], v205 offset:0\n"
    "s_waitcnt vmcnt(8)\n"
    "v_mfma_f32_16x16x32_bf16 v[36:39], v[144:147], v[132:135], v[36:39]\n"
    "ds_read_b128 v[168:171], v205 offset:2048\n"
    "s_waitcnt lgkmcnt(5)\n"
    "v_mfma_f32_16x16x32_bf16 v[20:23], v[148:151], v[128:131], v[20:23]\n"
    "buffer_load_dwordx4 v[112:115], v194, s[36:39], s21 offen sc1\n"
    "v_mfma_f32_16x16x32_bf16 v[40:43], v[148:151], v[132:135], v[40:43]\n"
    "ds_read_b128 v[172:175], v205 offset:4096\n"
    "s_waitcnt lgkmcnt(5)\n"
    "v_mfma_f32_16x16x32_bf16 v[24:27], v[152:155], v[128:131], v[24:27]\n"
    "ds_read_b128 v[176:179], v205 offset:6144\n"
    "v_mfma_f32_16x16x32_bf16 v[44:47], v[152:155], v[132:135], v[44:47]\n"
    "ds_read_b128 v[180:183], v205 offset:8192\n"
    "s_waitcnt lgkmcnt(6)\n"
    "v_mfma_f32_16x16x32_bf16 v[28:31], v[156:159], v[128:131], v[28:31]\n"
    "v_mfma_f32_16x16x32_bf16 v[48:51], v[156:159], v[132:135], v[48:51]\n"
    "buffer_load_dwordx4 v[116:119], v195, s[36:39], s21 offen sc1\n"
    "s_waitcnt lgkmcnt(5)\n"
    "v_mfma_f32_16x16x32_bf16 v[32:35], v[160:163], v[128:131], v[32:35]\n"
    "v_mfma_f32_16x16x32_bf16 v[52:55], v[160:163], v[132:135], v[52:55]\n"
    "s_waitcnt lgkmcnt(0)\n"
    "s_barrier\n"
    "s_waitcnt vmcnt(9)\n"
    "v_mfma_f32_16x16x32_bf16 v[16:19], v[164:167], v[136:139], v[16:19]\n"
    "ds_read_b128 v[144:147], v204 offset:20480\n"
    "s_waitcnt vmcnt(8)\n"
    "v_mfma_f32_16x16x32_bf16 v[36:39], v[164:167], v[140:143], v[36:39]\n"
    "ds_read_b128 v[148:151], v204 offset:22528\n"
    "v_mfma_f32_16x16x32_bf16 v[20:23], v[168:171], v[136:139], v[20:23]\n"
    "buffer_load_dwordx4 v[120:123], v196, s[36:39], s21 offen sc1\n"
    "v_mfma_f32_16x16x32_bf16 v[40:43], v[168:171], v[140:143], v[40:43]\n"
    "ds_read_b128 v[152:155], v204 offset:24576\n"
    "v_mfma_f32_16x16x32_bf16 v[24:27], v[172:175], v[136:139], v[24:27]\n"
    "ds_read_b128 v[156:159], v204 offset:26624\n"
    "v_mfma_f32_16x16x32_bf16 v[44:47], v[172:175], v[140:143], v[44:47]\n"
    "ds_read_b128 v[160:163], v204 offset:28672\n"
    "v_mfma_f32_16x16x32_bf16 v[28:31], v[176:179], v[136:139], v[28:31]\n"
    "v_mfma_f32_16x16x32_bf16 v[48:51], v[176:179], v[140:143], v[48:51]\n"
    "buffer_load_dwordx4 v[124:127], v197, s[36:39], s21 offen sc1\n"
    "v_mfma_f32_16x16x32_bf16 v[32:35], v[180:183], v[136:139], v[32:35]\n"
    "v_mfma_f32_16x16x32_bf16 v[52:55], v[180:183], v[140:143], v[52:55]\n"
    "s_waitcnt vmcnt(9) lgkmcnt(4)\n"
    "v_mfma_f32_16x16x32_bf16 v[16:19], v[144:147], v[96:99], v[16:19]\n"
    "ds_read_b128 v[164:167], v205 offset:20480\n"
    "s_waitcnt vmcnt(8)\n"
    "v_mfma_f32_16x16x32_bf16 v[36:39], v[144:147], v[100:103], v[36:39]\n"
    "ds_read_b128 v[168:171], v205 offset:22528\n"
    "s_waitcnt lgkmcnt(5)\n"
    "v_mfma_f32_16x16x32_bf16 v[20:23], v[148:151], v[96:99], v[20:23]\n"
    "buffer_load_dwordx4 v[128:131], v194, s[36:39], s22 offen sc1\n"
    "v_mfma_f32_16x16x32_bf16 v[40:43], v[148:151], v[100:103], v[40:43]\n"
    "ds_read_b128 v[172:175], v205 offset:24576\n"
    "s_waitcnt lgkmcnt(5)\n"
    "v_mfma_f32_16x16x32_bf16 v[24:27], v[152:155], v[96:99], v[24:27]\n"
    "ds_read_b128 v[176:179], v205 offset:26624\n"
    "v_mfma_f32_16x16x32_bf16 v[44:47], v[152:155], v[100:103], v[44:47]\n"
    "ds_read_b128 v[180:183], v205 offset:28672\n"
    "s_waitcnt lgkmcnt(6)\n"
    "v_mfma_f32_16x16x32_bf16 v[28:31], v[156:159], v[96:99], v[28:31]\n"
    "v_mfma_f32_16x16x32_bf16 v[48:51], v[156:159], v[100:103], v[48:51]\n"
    "buffer_load_dwordx4 v[132:135], v195, s[36:39], s22 offen sc1\n"
    "s_waitcnt lgkmcnt(5)\n"
    "v_mfma_f32_16x16x32_bf16 v[32:35], v[160:163], v[96:99], v[32:35]\n"
    "v_mfma_f32_16x16x32_bf16 v[52:55], v[160:163], v[100:103], v[52:55]\n"
    "s_waitcnt lgkmcnt(0)\n"
    "s_barrier\n"
    "s_waitcnt vmcnt(9)\n"
    "v_mfma_f32_16x16x32_bf16 v[16:19], v[164:167], v[104:107], v[16:19]\n"
    "ds_read_b128 v[144:147], v204 offset:40960\n"
    "s_waitcnt vmcnt(8)\n"
    "v_mfma_f32_16x16x32_bf16 v[36:39], v[164:167], v[108:111], v[36:39]\n"
    "ds_read_b128 v[148:151], v204 offset:43008\n"
    "v_mfma_f32_16x16x32_bf16 v[20:23], v[168:171], v[104:107], v[20:23]\n"
    "buffer_load_dwordx4 v[136:139], v196, s[36:39], s22 offen sc1\n"
    "v_mfma_f32_16x16x32_bf16 v[40:43], v[168:171], v[108:111], v[40:43]\n"
    "ds_read_b128 v[152:155], v204 offset:45056\n"
    "v_mfma_f32_16x16x32_bf16 v[24:27], v[172:175], v[104:107], v[24:27]\n"
    "ds_read_b128 v[156:159], v204 offset:47104\n"
    "v_mfma_f32_16x16x32_bf16 v[44:47], v[172:175], v[108:111], v[44:47]\n"
    "ds_read_b128 v[160:163], v204 offset:49152\n"
    "v_mfma_f32_16x16x32_bf16 v[28:31], v[176:179], v[104:107], v[28:31]\n"
    "v_add_u32 v11, 154624, v12\n"
    "ds_read_b64 v[192:193], v11\n"
    "v_mfma_f32_16x16x32_bf16 v[48:51], v[176:179], v[108:111], v[48:51]\n"
    "buffer_load_dwordx4 v[140:143], v197, s[36:39], s22 offen sc1\n"
    "v_mfma_f32_16x16x32_bf16 v[32:35], v[180:183], v[104:107], v[32:35]\n"
    "v_mfma_f32_16x16x32_bf16 v[52:55], v[180:183], v[108:111], v[52:55]\n"
    "s_waitcnt lgkmcnt(0)\n"
    "v_xor_b32 v192, s34, v192\n"
    "v_xor_b32 v193, s34, v193\n"
    "s_waitcnt vmcnt(9)\n"
    "v_xor_b32 v185, s34, v185\n"
    "v_xor_b32 v187, s34, v187\n"
    "s_waitcnt vmcnt(8)\n"
    "v_xor_b32 v189, s34, v189\n"
    "v_xor_b32_e64 v191, s34, v191\n"
    "v_or3_b32 v192, v192, v193, v185\n"
    "v_or3_b32 v193, v187, v189, v191\n"
    "v_or_b32 v192, v192, v193\n"
    "v_cmp_ne_u32 vcc, 0, v192\n"
    "s_nop 0\n"
    "s_cbranch_vccz .Lo_ready5\n"
    "s_mov_b32 s77, 262144\n"
    ".Lspin8:\n"
    "buffer_load_dwordx2 v[192:193], v7, s[36:39], s76 offen sc1\n"
    "buffer_load_dwordx4 v[184:187], v12, s[36:39], s66 offen sc1\n"
    "buffer_load_dwordx4 v[188:191], v12, s[36:39], s68 offen sc1\n"
    "s_waitcnt vmcnt(0)\n"
    "v_xor_b32 v192, s34, v192\n"
    "v_xor_b32 v193, s34, v193\n"
    "v_xor_b32 v185, s34, v185\n"
    "v_xor_b32 v187, s34, v187\n"
    "v_xor_b32 v189, s34, v189\n"
    "v_xor_b32_e64 v191, s34, v191\n"
    "v_or3_b32 v192, v192, v193, v185\n"
    "v_or3_b32 v193, v187, v189, v191\n"
    "v_or_b32 v192, v192, v193\n"
    "v_cmp_ne_u32 vcc, 0, v192\n"
    "s_nop 0\n"
    "s_cbranch_vccz .Lspun8\n"
    "s_sleep 1\n"
    "s_sub_u32 s77, s77, 1\n"
    "s_cmp_lg_u32 s77, 0\n"
    "s_cbranch_scc1 .Lspin8\n"
    ".Lspun8:\n"
    ".Lo_ready5:\n"
    "buffer_load_dwordx4 v[96:99], v194, s[36:39], s23 offen sc1\n"
    "s_waitcnt vmcnt(8)\n"
    "v_mfma_f32_16x16x32_bf16 v[16:19], v[144:147], v[112:115], v[16:19]\n"
    "ds_read_b128 v[164:167], v205 offset:40960\n"
    "s_waitcnt vmcnt(7)\n"
    "v_mfma_f32_16x16x32_bf16 v[36:39], v[144:147], v[116:119], v[36:39]\n"
    "ds_read_b128 v[168:171], v205 offset:43008\n"
    "v_mfma_f32_16x16x32_bf16 v[20:23], v[148:151], v[112:115], v[20:23]\n"
    "buffer_load_dwordx4 v[100:103], v195, s[36:39], s23 offen sc1\n"
    "v_mfma_f32_16x16x32_bf16 v[40:43], v[148:151], v[116:119], v[40:43]\n"
    "ds_read_b128 v[172:175], v205 offset:45056\n"
    "v_mfma_f32_16x16x32_bf16 v[24:27], v[152:155], v[112:115], v[24:27]\n"
    "ds_read_b128 v[176:179], v205 offset:47104\n"
    "v_mfma_f32_16x16x32_bf16 v[44:47], v[152:155], v[116:119], v[44:47]\n"
    "ds_read_b128 v[180:183], v205 offset:49152\n"
    "v_mfma_f32_16x16x32_bf16 v[28:31], v[156:159], v[112:115], v[28:31]\n"
    "v_mfma_f32_16x16x32_bf16 v[48:51], v[156:159], v[116:119], v[48:51]\n"
    "buffer_load_dwordx4 v[104:107], v194, s[36:39], s24 offen sc1\n"
    "v_mfma_f32_16x16x32_bf16 v[32:35], v[160:163], v[112:115], v[32:35]\n"
    "v_mfma_f32_16x16x32_bf16 v[52:55], v[160:163], v[116:119], v[52:55]\n"
    "s_waitcnt lgkmcnt(0)\n"
    "s_barrier\n"
    "s_waitcnt vmcnt(8)\n"
    "v_mfma_f32_16x16x32_bf16 v[16:19], v[164:167], v[120:123], v[16:19]\n"
    "ds_read_b128 v[144:147], v204 offset:0\n"
    "s_waitcnt vmcnt(7)\n"
    "v_mfma_f32_16x16x32_bf16 v[36:39], v[164:167], v[124:127], v[36:39]\n"
    "ds_read_b128 v[148:151], v204 offset:2048\n"
    "v_mfma_f32_16x16x32_bf16 v[20:23], v[168:171], v[120:123], v[20:23]\n"
    "buffer_load_dwordx4 v[108:111], v195, s[36:39], s24 offen sc1\n"
    "v_mfma_f32_16x16x32_bf16 v[40:43], v[168:171], v[124:127], v[40:43]\n"
    "ds_read_b128 v[152:155], v204 offset:4096\n"
    "v_mfma_f32_16x16x32_bf16 v[24:27], v[172:175], v[120:123], v[24:27]\n"
    "ds_read_b128 v[156:159], v204 offset:6144\n"
    "v_mfma_f32_16x16x32_bf16 v[44:47], v[172:175], v[124:127], v[44:47]\n"
    "ds_read_b128 v[160:163], v204 offset:8192\n"
    "v_mfma_f32_16x16x32_bf16 v[28:31], v[176:179], v[120:123], v[28:31]\n"
    "v_sub_f32 v185, 1.0, v184\n"
    "v_sub_f32 v189, 1.0, v188\n"
    "v_mfma_f32_16x16x32_bf16 v[48:51], v[176:179], v[124:127], v[48:51]\n"
    "buffer_load_dwordx4 v[216:219], v194, s[36:39], s25 offen sc1\n"
    "v_mfma_f32_16x16x32_bf16 v[32:35], v[180:183], v[120:123], v[32:35]\n"
    "v_mfma_f32_16x16x32_bf16 v[52:55], v[180:183], v[124:127], v[52:55]\n"
    "s_waitcnt vmcnt(8) lgkmcnt(4)\n"
    "v_mfma_f32_16x16x32_bf16 v[16:19], v[144:147], v[128:131], v[16:19]\n"
    "ds_read_b128 v[164:167], v205 offset:0\n"
    "s_waitcnt vmcnt(7)\n"
    "v_mfma_f32_16x16x32_bf16 v[36:39], v[144:147], v[132:135], v[36:39]\n"
    "buffer_load_dwordx4 v[220:223], v195, s[36:39], s25 offen sc1\n"
    "s_waitcnt lgkmcnt(4)\n"
    "v_mfma_f32_16x16x32_bf16 v[20:23], v[148:151], v[128:131], v[20:23]\n"
    "ds_read_b128 v[168:171], v205 offset:2048\n"
    "v_mfma_f32_16x16x32_bf16 v[40:43], v[148:151], v[132:135], v[40:43]\n"
    "ds_read_b128 v[172:175], v205 offset:4096\n"
    "s_waitcnt lgkmcnt(5)\n"
    "v_mfma_f32_16x16x32_bf16 v[24:27], v[152:155], v[128:131], v[24:27]\n"
    "ds_read_b128 v[176:179], v205 offset:6144\n"
    "v_mfma_f32_16x16x32_bf16 v[44:47], v[152:155], v[132:135], v[44:47]\n"
    "buffer_load_dwordx4 v[224:227], v194, s[36:39], s26 offen sc1\n"
    "s_waitcnt lgkmcnt(5)\n"
    "v_mfma_f32_16x16x32_bf16 v[28:31], v[156:159], v[128:131], v[28:31]\n"
    "ds_read_b128 v[180:183], v205 offset:8192\n"
    "v_mfma_f32_16x16x32_bf16 v[48:51], v[156:159], v[132:135], v[48:51]\n"
    "s_waitcnt lgkmcnt(5)\n"
    "v_mfma_f32_16x16x32_bf16 v[32:35], v[160:163], v[128:131], v[32:35]\n"
    "buffer_load_dwordx4 v[228:231], v195, s[36:39], s26 offen sc1\n"
    "v_mfma_f32_16x16x32_bf16 v[52:55], v[160:163], v[132:135], v[52:55]\n"
    "s_waitcnt lgkmcnt(0)\n"
    "s_barrier\n"
    "s_waitcnt vmcnt(9)\n"
    "v_mfma_f32_16x16x32_bf16 v[16:19], v[164:167], v[136:139], v[16:19]\n"
    "ds_read_b64_tr_b16 v[144:145], v200 offset:0\n"
    "s_waitcnt vmcnt(8)\n"
    "v_mfma_f32_16x16x32_bf16 v[36:39], v[164:167], v[140:143], v[36:39]\n"
    "ds_read_b64_tr_b16 v[146:147], v200 offset:1280\n"
    "v_mfma_f32_16x16x32_bf16 v[20:23], v[168:171], v[136:139], v[20:23]\n"
    "ds_read_b64_tr_b16 v[148:149], v200 offset:32\n"
    "buffer_load_dwordx4 v[232:235], v194, s[36:39], s27 offen sc1\n"
    "v_mfma_f32_16x16x32_bf16 v[40:43], v[168:171], v[140:143], v[40:43]\n"
    "ds_read_b64_tr_b16 v[150:151], v200 offset:1312\n"
    "v_mfma_f32_16x16x32_bf16 v[24:27], v[172:175], v[136:139], v[24:27]\n"
    "ds_read_b64_tr_b16 v[152:153], v200 offset:64\n"
    "v_mfma_f32_16x16x32_bf16 v[44:47], v[172:175], v[140:143], v[44:47]\n"
    "ds_read_b64_tr_b16 v[154:155], v200 offset:1344\n"
    "v_mfma_f32_16x16x32_bf16 v[28:31], v[176:179], v[136:139], v[28:31]\n"
    "ds_read_b64_tr_b16 v[156:157], v200 offset:96\n"
    "v_mfma_f32_16x16x32_bf16 v[48:51], v[176:179], v[140:143], v[48:51]\n"
    "ds_read_b64_tr_b16 v[158:159], v200 offset:1376\n"
    "buffer_load_dwordx4 v[236:239], v195, s[36:39], s27 offen sc1\n"
    "v_mfma_f32_16x16x32_bf16 v[32:35], v[180:183], v[136:139], v[32:35]\n"
    "ds_read_b64_tr_b16 v[160:161], v200 offset:128\n"
    "v_mfma_f32_16x16x32_bf16 v[52:55], v[180:183], v[140:143], v[52:55]\n"
    "ds_read_b64_tr_b16 v[162:163], v200 offset:1408\n"
    "s_waitcnt vmcnt(9) lgkmcnt(8)\n"
    "v_mfma_f32_16x16x32_bf16 v[56:59], v[144:147], v[96:99], 0\n"
    "ds_read_b64_tr_b16 v[164:165], v200 offset:10240\n"
    "s_waitcnt vmcnt(8)\n"
    "v_mfma_f32_16x16x32_bf16 v[76:79], v[144:147], v[100:103], 0\n"
    "ds_read_b64_tr_b16 v[166:167], v200 offset:11520\n"
    "buffer_load_dwordx4 v[240:243], v194, s[36:39], s28 offen sc1\n"
    "s_waitcnt lgkmcnt(8)\n"
    "v_mfma_f32_16x16x32_bf16 v[60:63], v[148:151], v[96:99], 0\n"
    "ds_read_b64_tr_b16 v[168:169], v200 offset:10272\n"
    "v_mfma_f32_16x16x32_bf16 v[80:83], v[148:151], v[100:103], 0\n"
    "ds_read_b64_tr_b16 v[170:171], v200 offset:11552\n"
    "s_waitcnt lgkmcnt(8)\n"
    "v_mfma_f32_16x16x32_bf16 v[64:67], v[152:155], v[96:99], 0\n"
    "ds_read_b64_tr_b16 v[172:173], v200 offset:10304\n"
    "v_mfma_f32_16x16x32_bf16 v[84:87], v[152:155], v[100:103], 0\n"
    "ds_read_b64_tr_b16 v[174:175], v200 offset:11584\n"
    "s_waitcnt lgkmcnt(8)\n"
    "v_mfma_f32_16x16x32_bf16 v[68:71], v[156:159], v[96:99], 0\n"
    "ds_read_b64_tr_b16 v[176:177], v200 offset:10336\n"
    "buffer_load_dwordx4 v[244:247], v195, s[36:39], s28 offen sc1\n"
    "v_mfma_f32_16x16x32_bf16 v[88:91], v[156:159], v[100:103], 0\n"
    "ds_read_b64_tr_b16 v[178:179], v200 offset:11616\n"
    "s_waitcnt lgkmcnt(8)\n"
    "v_mfma_f32_16x16x32_bf16 v[72:75], v[160:163], v[96:99], 0\n"
    "ds_read_b64_tr_b16 v[180:181], v200 offset:10368\n"
    "v_mfma_f32_16x16x32_bf16 v[92:95], v[160:163], v[100:103], 0\n"
    "ds_read_b64_tr_b16 v[182:183], v200 offset:11648\n"
    "s_waitcnt vmcnt(9) lgkmcnt(8)\n"
    "v_mfma_f32_16x16x32_bf16 v[56:59], v[164:167], v[104:107], v[56:59]\n"
    "ds_read_b64_tr_b16 v[144:145], v200 offset:20480\n"
    "v_mul_f32 v55, v55, v188\n"
    "s_waitcnt vmcnt(8)\n"
    "v_mfma_f32_16x16x32_bf16 v[76:79], v[164:167], v[108:111], v[76:79]\n"
    "ds_read_b64_tr_b16 v[146:147], v200 offset:21760\n"
    "buffer_load_dwordx4 v[112:115], v194, s[36:39], s29 offen sc1\n"
    "s_waitcnt lgkmcnt(8)\n"
    "v_mfma_f32_16x16x32_bf16 v[60:63], v[168:171], v[104:107], v[60:63]\n"
    "ds_read_b64_tr_b16 v[148:149], v200 offset:20512\n"
    "v_mul_f32 v54, v54, v188\n"
    "v_mfma_f32_16x16x32_bf16 v[80:83], v[168:171], v[108:111], v[80:83]\n"
    "ds_read_b64_tr_b16 v[150:151], v200 offset:21792\n"
    "v_mul_f32 v53, v53, v188\n"
    "s_waitcnt lgkmcnt(8)\n"
    "v_mfma_f32_16x16x32_bf16 v[64:67], v[172:175], v[104:107], v[64:67]\n"
    "ds_read_b64_tr_b16 v[152:153], v200 offset:20544\n"
    "v_mul_f32 v52, v52, v188\n"
    "v_mfma_f32_16x16x32_bf16 v[84:87], v[172:175], v[108:111], v[84:87]\n"
    "ds_read_b64_tr_b16 v[154:155], v200 offset:21824\n"
    "v_mul_f32 v51, v51, v188\n"
    "s_waitcnt lgkmcnt(8)\n"
    "v_mfma_f32_16x16x32_bf16 v[68:71], v[176:179], v[104:107], v[68:71]\n"
    "ds_read_b64_tr_b16 v[156:157], v200 offset:20576\n"
    "buffer_load_dwordx4 v[116:119], v195, s[36:39], s29 offen sc1\n"
    "v_mfma_f32_16x16x32_bf16 v[88:91], v[176:179], v[108:111], v[88:91]\n"
    "ds_read_b64_tr_b16 v[158:159], v200 offset:21856\n"
    "v_mul_f32 v50, v50, v188\n"
    "s_waitcnt lgkmcnt(8)\n"
    "v_mfma_f32_16x16x32_bf16 v[72:75], v[180:183], v[104:107], v[72:75]\n"
    "ds_read_b64_tr_b16 v[160:161], v200 offset:20608\n"
    "v_mfma_f32_16x16x32_bf16 v[92:95], v[180:183], v[108:111], v[92:95]\n"
    "ds_read_b64_tr_b16 v[162:163], v200 offset:21888\n"
    "s_waitcnt vmcnt(9) lgkmcnt(8)\n"
    "v_mfma_f32_16x16x32_bf16 v[56:59], v[144:147], v[216:219], v[56:59]\n"
    "ds_read_b64_tr_b16 v[164:165], v200 offset:30720\n"
    "v_mul_f32 v49, v49, v188\n"
    "s_waitcnt vmcnt(8)\n"
    "v_mfma_f32_16x16x32_bf16 v[76:79], v[144:147], v[220:223], v[76:79]\n"
    "ds_read_b64_tr_b16 v[166:167], v200 offset:32000\n"
    "buffer_load_dwordx4 v[120:123], v194, s[36:39], s30 offen sc1\n"
    "s_waitcnt lgkmcnt(8)\n"
    "v_mfma_f32_16x16x32_bf16 v[60:63], v[148:151], v[216:219], v[60:63]\n"
    "ds_read_b64_tr_b16 v[168:169], v200 offset:30752\n"
    "v_mul_f32 v48, v48, v188\n"
    "v_mfma_f32_16x16x32_bf16 v[80:83], v[148:151], v[220:223], v[80:83]\n"
    "ds_read_b64_tr_b16 v[170:171], v200 offset:32032\n"
    "v_mul_f32 v47, v47, v188\n"
    "s_waitcnt lgkmcnt(8)\n"
    "v_mfma_f32_16x16x32_bf16 v[64:67], v[152:155], v[216:219], v[64:67]\n"
    "ds_read_b64_tr_b16 v[172:173], v200 offset:30784\n"
    "v_mul_f32 v46, v46, v188\n"
    "v_mfma_f32_16x16x32_bf16 v[84:87], v[152:155], v[220:223], v[84:87]\n"
    "ds_read_b64_tr_b16 v[174:175], v200 offset:32064\n"
    "v_mul_f32 v45, v45, v188\n"
    "s_waitcnt lgkmcnt(8)\n"
    "v_mfma_f32_16x16x32_bf16 v[68:71], v[156:159], v[216:219], v[68:71]\n"
    "ds_read_b64_tr_b16 v[176:177], v200 offset:30816\n"
    "buffer_load_dwordx4 v[124:127], v195, s[36:39], s30 offen sc1\n"
    "v_mfma_f32_16x16x32_bf16 v[88:91], v[156:159], v[220:223], v[88:91]\n"
    "ds_read_b64_tr_b16 v[178:179], v200 offset:32096\n"
    "v_mul_f32 v44, v44, v188\n"
    "s_waitcnt lgkmcnt(8)\n"
    "v_mfma_f32_16x16x32_bf16 v[72:75], v[160:163], v[216:219], v[72:75]\n"
    "ds_read_b64_tr_b16 v[180:181], v200 offset:30848\n"
    "v_mfma_f32_16x16x32_bf16 v[92:95], v[160:163], v[220:223], v[92:95]\n"
    "ds_read_b64_tr_b16 v[182:183], v200 offset:32128\n"
    "s_waitcnt vmcnt(9) lgkmcnt(8)\n"
    "v_mfma_f32_16x16x32_bf16 v[56:59], v[164:167], v[224:227], v[56:59]\n"
    "ds_read_b64_tr_b16 v[144:145], v200 offset:40960\n"
    "v_mul_f32 v43, v43, v188\n"
    "s_waitcnt vmcnt(8)\n"
    "v_mfma_f32_16x16x32_bf16 v[76:79], v[164:167], v[228:231], v[76:79]\n"
    "ds_read_b64_tr_b16 v[146:147], v200 offset:42240\n"
    "buffer_load_dwordx2 v[128:129], v14, s[36:39], s62 offen offset:0 sc1\n"
    "s_waitcnt lgkmcnt(8)\n"
    "v_mfma_f32_16x16x32_bf16 v[60:63], v[168:171], v[224:227], v[60:63]\n"
    "ds_read_b64_tr_b16 v[148:149], v200 offset:40992\n"
    "v_mul_f32 v42, v42, v188\n"
    "v_mfma_f32_16x16x32_bf16 v[80:83], v[168:171], v[228:231], v[80:83]\n"
    "ds_read_b64_tr_b16 v[150:151], v200 offset:42272\n"
    "v_mul_f32 v41, v41, v188\n"
    "s_waitcnt lgkmcnt(8)\n"
    "v_mfma_f32_16x16x32_bf16 v[64:67], v[172:175], v[224:227], v[64:67]\n"
    "ds_read_b64_tr_b16 v[152:153], v200 offset:41024\n"
    "v_mul_f32 v40, v40, v188\n"
    "v_mfma_f32_16x16x32_bf16 v[84:87], v[172:175], v[228:231], v[84:87]\n"
    "ds_read_b64_tr_b16 v[154:155], v200 offset:42304\n"
    "v_mul_f32 v39, v39, v188\n"
    "s_waitcnt lgkmcnt(8)\n"
    "v_mfma_f32_16x16x32_bf16 v[68:71], v[176:179], v[224:227], v[68:71]\n"
    "ds_read_b64_tr_b16 v[156:157], v200 offset:41056\n"
    "buffer_load_dwordx2 v[130:131], v14, s[36:39], s62 offen offset:1024 sc1\n"
    "v_mfma_f32_16x16x32_bf16 v[88:91], v[176:179], v[228:231], v[88:91]\n"
    "ds_read_b64_tr_b16 v[158:159], v200 offset:42336\n"
    "v_mul_f32 v38, v38, v188\n"
    "s_waitcnt lgkmcnt(8)\n"
    "v_mfma_f32_16x16x32_bf16 v[72:75], v[180:183], v[224:227], v[72:75]\n"
    "ds_read_b64_tr_b16 v[160:161], v200 offset:41088\n"
    "v_mfma_f32_16x16x32_bf16 v[92:95], v[180:183], v[228:231], v[92:95]\n"
    "ds_read_b64_tr_b16 v[162:163], v200 offset:42368\n"
    "s_waitcnt vmcnt(9) lgkmcnt(8)\n"
    "v_mfma_f32_16x16x32_bf16 v[56:59], v[144:147], v[232:235], v[56:59]\n"
    "ds_read_b64_tr_b16 v[164:165], v200 offset:51200\n"
    "v_mul_f32 v37, v37, v188\n"
    "s_waitcnt vmcnt(8)\n"
    "v_mfma_f32_16x16x32_bf16 v[76:79], v[144:147], v[236:239], v[76:79]\n"
    "ds_read_b64_tr_b16 v[166:167], v200 offset:52480\n"
    "buffer_load_dwordx2 v[132:133], v15, s[36:39], s62 offen offset:0 sc1\n"
    "s_waitcnt lgkmcnt(8)\n"
    "v_mfma_f32_16x16x32_bf16 v[60:63], v[148:151], v[232:235], v[60:63]\n"
    "ds_read_b64_tr_b16 v[168:169], v200 offset:51232\n"
    "v_mul_f32 v36, v36, v188\n"
    "v_mfma_f32_16x16x32_bf16 v[80:83], v[148:151], v[236:239], v[80:83]\n"
    "ds_read_b64_tr_b16 v[170:171], v200 offset:52512\n"
    "v_mul_f32 v35, v35, v184\n"
    "s_waitcnt lgkmcnt(8)\n"
    "v_mfma_f32_16x16x32_bf16 v[64:67], v[152:155], v[232:235], v[64:67]\n"
    "ds_read_b64_tr_b16 v[172:173], v200 offset:51264\n"
    "v_mul_f32 v34, v34, v184\n"
    "v_mfma_f32_16x16x32_bf16 v[84:87], v[152:155], v[236:239], v[84:87]\n"
    "ds_read_b64_tr_b16 v[174:175], v200 offset:52544\n"
    "v_mul_f32 v33, v33, v184\n"
    "s_waitcnt lgkmcnt(8)\n"
    "v_mfma_f32_16x16x32_bf16 v[68:71], v[156:159], v[232:235], v[68:71]\n"
    "ds_read_b64_tr_b16 v[176:177], v200 offset:51296\n"
    "buffer_load_dwordx2 v[134:135], v15, s[36:39], s62 offen offset:1024 sc1\n"
    "v_mfma_f32_16x16x32_bf16 v[88:91], v[156:159], v[236:239], v[88:91]\n"
    "ds_read_b64_tr_b16 v[178:179], v200 offset:52576\n"
    "v_mul_f32 v32, v32, v184\n"
    "s_waitcnt lgkmcnt(8)\n"
    "v_mfma_f32_16x16x32_bf16 v[72:75], v[160:163], v[232:235], v[72:75]\n"
    "ds_read_b64_tr_b16 v[180:181], v200 offset:51328\n"
    "v_mfma_f32_16x16x32_bf16 v[92:95], v[160:163], v[236:239], v[92:95]\n"
    "ds_read_b64_tr_b16 v[182:183], v200 offset:52608\n"
    "v_add_u32 v200, 61440, v200\n"
    "s_waitcnt vmcnt(9) lgkmcnt(8)\n"
    "v_mfma_f32_16x16x32_bf16 v[56:59], v[164:167], v[240:243], v[56:59]\n"
    "ds_read_b64_tr_b16 v[144:145], v200 offset:0\n"
    "v_mul_f32 v31, v31, v184\n"
    "s_waitcnt vmcnt(8)\n"
    "v_mfma_f32_16x16x32_bf16 v[76:79], v[164:167], v[244:247], v[76:79]\n"
    "ds_read_b64_tr_b16 v[146:147], v200 offset:1280\n"
    "s_waitcnt lgkmcnt(8)\n"
    "v_mfma_f32_16x16x32_bf16 v[60:63], v[168:171], v[240:243], v[60:63]\n"
    "ds_read_b64_tr_b16 v[148:149], v200 offset:32\n"
    "v_mul_f32 v30, v30, v184\n"
    "v_mfma_f32_16x16x32_bf16 v[80:83], v[168:171], v[244:247], v[80:83]\n"
    "ds_read_b64_tr_b16 v[150:151], v200 offset:1312\n"
    "v_mul_f32 v29, v29, v184\n"
    "s_waitcnt lgkmcnt(8)\n"
    "v_mfma_f32_16x16x32_bf16 v[64:67], v[172:175], v[240:243], v[64:67]\n"
    "ds_read_b64_tr_b16 v[152:153], v200 offset:64\n"
    "v_mul_f32 v28, v28, v184\n"
    "v_mfma_f32_16x16x32_bf16 v[84:87], v[172:175], v[244:247], v[84:87]\n"
    "ds_read_b64_tr_b16 v[154:155], v200 offset:1344\n"
    "v_mul_f32 v27, v27, v184\n"
    "s_waitcnt lgkmcnt(8)\n"
    "v_mfma_f32_16x16x32_bf16 v[68:71], v[176:179], v[240:243], v[68:71]\n"
    "ds_read_b64_tr_b16 v[156:157], v200 offset:96\n"
    "v_mfma_f32_16x16x32_bf16 v[88:91], v[176:179], v[244:247], v[88:91]\n"
    "ds_read_b64_tr_b16 v[158:159], v200 offset:1376\n"
    "v_mul_f32 v26, v26, v184\n"
    "s_waitcnt lgkmcnt(8)\n"
    "v_mfma_f32_16x16x32_bf16 v[72:75], v[180:183], v[240:243], v[72:75]\n"
    "ds_read_b64_tr_b16 v[160:161], v200 offset:128\n"
    "v_mfma_f32_16x16x32_bf16 v[92:95], v[180:183], v[244:247], v[92:95]\n"
    "ds_read_b64_tr_b16 v[162:163], v200 offset:1408\n"
    "s_waitcnt vmcnt(7) lgkmcnt(8)\n"
    "v_mfma_f32_16x16x32_bf16 v[56:59], v[144:147], v[112:115], v[56:59]\n"
    "ds_read_b64_tr_b16 v[164:165], v200 offset:10240\n"
    "v_mul_f32 v25, v25, v184\n"
    "s_waitcnt vmcnt(6)\n"
    "v_mfma_f32_16x16x32_bf16 v[76:79], v[144:147], v[116:119], v[76:79]\n"
    "ds_read_b64_tr_b16 v[166:167], v200 offset:11520\n"
    "s_waitcnt lgkmcnt(8)\n"
    "v_mfma_f32_16x16x32_bf16 v[60:63], v[148:151], v[112:115], v[60:63]\n"
    "ds_read_b64_tr_b16 v[168:169], v200 offset:10272\n"
    "v_mul_f32 v24, v24, v184\n"
    "v_mfma_f32_16x16x32_bf16 v[80:83], v[148:151], v[116:119], v[80:83]\n"
    "ds_read_b64_tr_b16 v[170:171], v200 offset:11552\n"
    "v_mul_f32 v23, v23, v184\n"
    "s_waitcnt lgkmcnt(8)\n"
    "v_mfma_f32_16x16x32_bf16 v[64:67], v[152:155], v[112:115], v[64:67]\n"
    "ds_read_b64_tr_b16 v[172:173], v200 offset:10304\n"
    "v_mul_f32 v22, v22, v184\n"
    "v_mfma_f32_16x16x32_bf16 v[84:87], v[152:155], v[116:119], v[84:87]\n"
    "ds_read_b64_tr_b16 v[174:175], v200 offset:11584\n"
    "v_mul_f32 v21, v21, v184\n"
    "s_waitcnt lgkmcnt(8)\n"
    "v_mfma_f32_16x16x32_bf16 v[68:71], v[156:159], v[112:115], v[68:71]\n"
    "ds_read_b64_tr_b16 v[176:177], v200 offset:10336\n"
    "v_mfma_f32_16x16x32_bf16 v[88:91], v[156:159], v[116:119], v[88:91]\n"
    "ds_read_b64_tr_b16 v[178:179], v200 offset:11616\n"
    "v_mul_f32 v20, v20, v184\n"
    "s_waitcnt lgkmcnt(8)\n"
    "v_mfma_f32_16x16x32_bf16 v[72:75], v[160:163], v[112:115], v[72:75]\n"
    "ds_read_b64_tr_b16 v[180:181], v200 offset:10368\n"
    "v_mfma_f32_16x16x32_bf16 v[92:95], v[160:163], v[116:119], v[92:95]\n"
    "ds_read_b64_tr_b16 v[182:183], v200 offset:11648\n"
    "s_waitcnt vmcnt(5) lgkmcnt(8)\n"
    "v_mfma_f32_16x16x32_bf16 v[56:59], v[164:167], v[120:123], v[56:59]\n"
    "ds_read_b64_tr_b16 v[144:145], v200 offset:20480\n"
    "v_mul_f32 v19, v19, v184\n"
    "s_waitcnt vmcnt(4)\n"
    "v_mfma_f32_16x16x32_bf16 v[76:79], v[164:167], v[124:127], v[76:79]\n"
    "ds_read_b64_tr_b16 v[146:147], v200 offset:21760\n"
    "s_waitcnt lgkmcnt(8)\n"
    "v_mfma_f32_16x16x32_bf16 v[60:63], v[168:171], v[120:123], v[60:63]\n"
    "ds_read_b64_tr_b16 v[148:149], v200 offset:20512\n"
    "v_mul_f32 v18, v18, v184\n"
    "v_mfma_f32_16x16x32_bf16 v[80:83], v[168:171], v[124:127], v[80:83]\n"
    "ds_read_b64_tr_b16 v[150:151], v200 offset:21792\n"
    "v_mul_f32 v17, v17, v184\n"
    "s_waitcnt lgkmcnt(8)\n"
    "v_mfma_f32_16x16x32_bf16 v[64:67], v[172:175], v[120:123], v[64:67]\n"
    "ds_read_b64_tr_b16 v[152:153], v200 offset:20544\n"
    "v_mul_f32 v16, v16, v184\n"
    "v_mfma_f32_16x16x32_bf16 v[84:87], v[172:175], v[124:127], v[84:87]\n"
    "ds_read_b64_tr_b16 v[154:155], v200 offset:21824\n"
    "s_waitcnt lgkmcnt(8)\n"
    "v_mfma_f32_16x16x32_bf16 v[68:71], v[176:179], v[120:123], v[68:71]\n"
    "ds_read_b64_tr_b16 v[156:157], v200 offset:20576\n"
    "v_mfma_f32_16x16x32_bf16 v[88:91], v[176:179], v[124:127], v[88:91]\n"
    "ds_read_b64_tr_b16 v[158:159], v200 offset:21856\n"
    "s_waitcnt lgkmcnt(8)\n"
    "v_mfma_f32_16x16x32_bf16 v[72:75], v[180:183], v[120:123], v[72:75]\n"
    "ds_read_b64_tr_b16 v[160:161], v200 offset:20608\n"
    "v_mfma_f32_16x16x32_bf16 v[92:95], v[180:183], v[124:127], v[92:95]\n"
    "ds_read_b64_tr_b16 v[162:163], v200 offset:21888\n"
    "s_waitcnt vmcnt(2) lgkmcnt(8)\n"
    "v_mfma_f32_16x16x32_bf16 v[56:59], v[144:147], v[128:131], v[56:59]\n"
    "s_waitcnt vmcnt(0)\n"
    "v_mfma_f32_16x16x32_bf16 v[76:79], v[144:147], v[132:135], v[76:79]\n"
    "s_waitcnt lgkmcnt(6)\n"
    "v_mfma_f32_16x16x32_bf16 v[60:63], v[148:151], v[128:131], v[60:63]\n"
    "s_nop 3\n"
    "v_fmac_f32 v16, v56, v185\n"
    "v_mfma_f32_16x16x32_bf16 v[80:83], v[148:151], v[132:135], v[80:83]\n"
    "v_fmac_f32 v17, v57, v185\n"
    "v_fmac_f32 v18, v58, v185\n"
    "s_waitcnt lgkmcnt(4)\n"
    "v_mfma_f32_16x16x32_bf16 v[64:67], v[152:155], v[128:131], v[64:67]\n"
    "v_fmac_f32 v19, v59, v185\n"
    "v_fmac_f32 v36, v76, v189\n"
    "v_mfma_f32_16x16x32_bf16 v[84:87], v[152:155], v[132:135], v[84:87]\n"
    "ds_write_b128 v8, v[16:19] offset:0\n"
    "s_waitcnt lgkmcnt(3)\n"
    "v_mfma_f32_16x16x32_bf16 v[68:71], v[156:159], v[128:131], v[68:71]\n"
    "v_fmac_f32 v37, v77, v189\n"
    "v_fmac_f32 v38, v78, v189\n"
    "v_mfma_f32_16x16x32_bf16 v[88:91], v[156:159], v[132:135], v[88:91]\n"
    "v_fmac_f32 v39, v79, v189\n"
    "v_fmac_f32 v20, v60, v185\n"
    "s_waitcnt lgkmcnt(1)\n"
    "v_mfma_f32_16x16x32_bf16 v[72:75], v[160:163], v[128:131], v[72:75]\n"
    "ds_write_b128 v9, v[36:39] offset:0\n"
    "v_mfma_f32_16x16x32_bf16 v[92:95], v[160:163], v[132:135], v[92:95]\n"
    "v_fmac_f32 v21, v61, v185\n"
    "v_fmac_f32 v22, v62, v185\n"
    "v_fmac_f32 v23, v63, v185\n"
    "v_fmac_f32 v40, v80, v189\n"
    "v_fmac_f32 v41, v81, v189\n"
    "v_fmac_f32 v42, v82, v189\n"
    "v_fmac_f32_e64 v43, v83, v189\n"
    "ds_write_b128 v8, v[20:23] offset:64\n"
    "v_pk_fma_f32 v[24:25], v[64:65], v[184:185], v[24:25] op_sel:[0,1,0]\n"
    "v_pk_fma_f32 v[26:27], v[66:67], v[184:185], v[26:27] op_sel:[0,1,0]\n"
    "v_pk_fma_f32 v[44:45], v[84:85], v[188:189], v[44:45] op_sel:[0,1,0]\n"
    "v_pk_fma_f32 v[46:47], v[86:87], v[188:189], v[46:47] op_sel:[0,1,0]\n"
    "v_pk_fma_f32 v[28:29], v[68:69], v[184:185], v[28:29] op_sel:[0,1,0]\n"
    "v_pk_fma_f32 v[30:31], v[70:71], v[184:185], v[30:31] op_sel:[0,1,0]\n"
    "v_pk_fma_f32 v[48:49], v[88:89], v[188:189], v[48:49] op_sel:[0,1,0]\n"
    "v_pk_fma_f32 v[50:51], v[90:91], v[188:189], v[50:51] op_sel:[0,1,0]\n"
    "v_pk_fma_f32 v[32:33], v[72:73], v[184:185], v[32:33] op_sel:[0,1,0]\n"
    "ds_write_b128 v9, v[40:43] offset:64\n"
    "v_pk_fma_f32 v[34:35], v[74:75], v[184:185], v[34:35] op_sel:[0,1,0]\n"
    "v_pk_fma_f32 v[52:53], v[92:93], v[188:189], v[52:53] op_sel:[0,1,0]\n"
    "v_pk_fma_f32 v[54:55], v[94:95], v[188:189], v[54:55] op_sel:[0,1,0]\n"
    "ds_write_b128 v8, v[24:27] offset:128\n"
    "ds_write_b128 v9, v[44:47] offset:128\n"
    "ds_write_b128 v8, v[28:31] offset:192\n"
    "ds_write_b128 v9, v[48:51] offset:192\n"
    "ds_write_b128 v8, v[32:35] offset:256\n"
    "ds_write_b128 v9, v[52:55] offset:256\n"
    "s_waitcnt lgkmcnt(0)\n"
    "s_barrier\n"
    "ds_read_b128 v[16:19], v206\n"
    "ds_read_b128 v[56:59], v206 offset:21504\n"
    "ds_read_b128 v[20:23], v207\n"
    "ds_read_b128 v[60:63], v207 offset:21504\n"
    "ds_read_b128 v[24:27], v208\n"
    "ds_read_b128 v[64:67], v208 offset:21504\n"
    "ds_read_b128 v[28:31], v209\n"
    "ds_read_b128 v[68:71], v209 offset:21504\n"
    "ds_read_b128 v[32:35], v210\n"
    "ds_read_b128 v[72:75], v210 offset:21504\n"
    "s_waitcnt lgkmcnt(8)\n"
    "v_pk_add_f32 v[16:17], v[16:17], v[56:57]\n"
    "v_pk_add_f32 v[18:19], v[18:19], v[58:59]\n"
    "buffer_store_dwordx4 v[16:19], v211, s[40:43], s69 offen offset:0 sc1\n"
    "s_waitcnt lgkmcnt(6)\n"
    "v_pk_add_f32 v[20:21], v[20:21], v[60:61]\n"
    "v_pk_add_f32 v[22:23], v[22:23], v[62:63]\n"
    "buffer_store_dwordx4 v[20:23], v212, s[40:43], s69 offen offset:0 sc1\n"
    "s_waitcnt lgkmcnt(4)\n"
    "v_pk_add_f32 v[24:25], v[24:25], v[64:65]\n"
    "v_pk_add_f32 v[26:27], v[26:27], v[66:67]\n"
    "buffer_store_dwordx4 v[24:27], v213, s[40:43], s69 offen offset:0 sc1\n"
    "s_waitcnt lgkmcnt(2)\n"
    "v_pk_add_f32 v[28:29], v[28:29], v[68:69]\n"
    "v_pk_add_f32 v[30:31], v[30:31], v[70:71]\n"
    "buffer_store_dwordx4 v[28:31], v214, s[40:43], s69 offen offset:0 sc1\n"
    "s_waitcnt lgkmcnt(0)\n"
    "v_pk_add_f32 v[32:33], v[32:33], v[72:73]\n"
    "v_pk_add_f32 v[34:35], v[34:35], v[74:75]\n"
    "buffer_store_dwordx4 v[32:35], v215, s[40:43], s69 offen offset:0 sc1\n"
    "s_endpgm\n"
    "s_nop 0\n"
    "s_nop 0\n"
    "s_nop 0\n"
    "s_nop 0\n"
    "s_nop 0\n"
    "s_nop 0\n"
    "s_nop 0\n"
    "s_nop 0\n"
    "s_nop 0\n"
    "s_nop 0\n"
    "s_nop 0\n"
    "s_nop 0\n"
    "s_nop 0\n"
    "s_nop 0\n"
    "s_nop 0\n"
    "s_nop 0\n"
    "s_nop 0\n"
    "s_nop 0\n"
    "s_nop 0\n"
    "s_nop 0\n"
    "s_nop 0\n"
    "s_nop 0\n"
    "s_nop 0\n"
    "s_nop 0\n"
    "s_nop 0\n"
    "s_nop 0\n"
    "s_nop 0\n"
    "s_nop 0\n"
    "s_nop 0\n"
    "s_nop 0\n"
    "s_nop 0\n"
    "s_nop 0\n"
    "s_nop 0\n"
    "s_nop 0\n"
    "s_nop 0\n"
    "s_nop 0\n"
    "s_nop 0\n"
    "s_nop 0\n"
    "s_nop 0\n"
    "s_nop 0\n"
    "s_nop 0\n"
    "s_nop 0\n"
    "s_nop 0\n"
    "s_nop 0\n"
    "s_nop 0\n"
    "s_nop 0\n"
    "s_nop 0\n"
    "s_nop 0\n"
    "s_nop 0\n"
    "s_nop 0\n"
    "s_nop 0\n"
    "s_nop 0\n"
    "s_nop 0\n"
    "s_nop 0\n"
    "s_nop 0\n"
    "s_nop 0\n"
    "s_nop 0\n"
    "s_nop 0\n"
    "s_nop 0\n"
    "s_nop 0\n"
    "s_nop 0\n"
    "s_nop 0\n"
    "s_nop 0\n"
    "s_nop 0\n"
    "s_nop 0\n"
    "s_nop 0\n"
    "s_nop 0\n"
    "s_nop 0\n"
    "s_nop 0\n"
    "s_nop 0\n"
    "s_nop 0\n"
    "s_nop 0\n"
    "s_nop 0\n"
    "s_nop 0\n"
    "s_nop 0\n"
    "s_nop 0\n"
    "s_nop 0\n"
    "s_nop 0\n"
    "s_nop 0\n"
    "s_nop 0\n"
    "s_nop 0\n"
    "s_nop 0\n"
    "s_nop 0\n"
    "s_nop 0\n"
    "s_nop 0\n"
    "s_nop 0\n"
    "s_nop 0\n"
    "s_nop 0\n"
    "s_nop 0\n"
    "s_nop 0\n"
    "s_nop 0\n"
    "s_nop 0\n"
    "s_nop 0\n"
    "s_nop 0\n"
    "s_nop 0\n"
    "s_nop 0\n"
    "s_nop 0\n"
    "s_nop 0\n"
    "s_nop 0\n"
    "s_nop 0\n"
    "s_nop 0\n"
    "s_nop 0\n"
    "s_nop 0\n"
    "s_nop 0\n"
    "s_nop 0\n"
    "s_nop 0\n"
    "s_nop 0\n"
    "s_nop 0\n"
    "s_nop 0\n"
    "s_nop 0\n"
    "s_nop 0\n"
    "s_nop 0\n"
    "s_nop 0\n"
    "s_nop 0\n"
    "s_nop 0\n"
    "s_nop 0\n"
    "s_nop 0\n"
    "s_nop 0\n"
    "s_nop 0\n"
    "s_nop 0\n"
    "s_nop 0\n"
    "s_nop 0\n"
    "s_nop 0\n"
    "s_nop 0\n"
    "s_nop 0\n"
    "s_nop 0\n"
    "s_nop 0\n"
    "s_nop 0\n"
    "s_nop 0\n"
    "s_nop 0\n"
    "s_nop 0\n"
    "s_nop 0\n"
    "s_nop 0\n"
    "s_nop 0\n"
    "s_nop 0\n"
    "s_nop 0\n"
    "s_nop 0\n"
    "s_nop 0\n"
    "s_nop 0\n"
    "s_nop 0\n"
    "s_nop 0\n"
    "s_nop 0\n"
    "s_nop 0\n"
    "s_nop 0\n"
    "s_nop 0\n"
    "s_nop 0\n"
    "s_nop 0\n"
    "s_nop 0\n"
    "s_nop 0\n"
    "s_nop 0\n"
    "s_nop 0\n"
    "s_nop 0\n"
    "s_nop 0\n"
    "s_nop 0\n"
    "s_nop 0\n"
    "s_nop 0\n"
    "s_nop 0\n"
    "s_nop 0\n"
    "s_nop 0\n"
    "s_nop 0\n"
    "s_nop 0\n"
    "s_nop 0\n"
    "s_nop 0\n"
    "s_nop 0\n"
    "s_nop 0\n"
    "s_nop 0\n"
    "s_nop 0\n"
    "s_nop 0\n"
    "s_nop 0\n"
    "s_nop 0\n"
    "s_nop 0\n"
    "s_nop 0\n"
    "s_nop 0\n"
    "s_nop 0\n"
    "s_nop 0\n"
    "s_nop 0\n"
    "s_nop 0\n"
    "s_nop 0\n"
    "s_nop 0\n"
    "s_nop 0\n"
    "s_nop 0\n"
    "s_nop 0\n"
    "s_nop 0\n"
    "s_nop 0\n"
    "s_nop 0\n"
    "s_nop 0\n"
    "s_nop 0\n"
    "s_nop 0\n"
    "s_nop 0\n"
    "s_nop 0\n"
    "s_nop 0\n"
    "s_nop 0\n"
    "s_nop 0\n"
    "s_nop 0\n"
    "s_nop 0\n"
    "s_nop 0\n"
    "s_nop 0\n"
    "s_nop 0\n"
    "s_nop 0\n"
    "s_nop 0\n"
    "s_nop 0\n"
    "s_nop 0\n"
    "s_nop 0\n"
    "s_nop 0\n"
    "s_nop 0\n"
    "s_nop 0\n"
    "s_nop 0\n"
    "s_nop 0\n"
    "s_nop 0\n"
    "s_nop 0\n"
    "s_nop 0\n"
    "s_nop 0\n"
    "s_nop 0\n"
    "s_nop 0\n"
    "s_nop 0\n"
    "s_nop 0\n"
    "s_nop 0\n"
    "s_nop 0\n"
    "s_nop 0\n"
    "s_nop 0\n"
    "s_nop 0\n"
    "s_nop 0\n"
    "s_nop 0\n"
    "s_nop 0\n"
    "s_nop 0\n"
    "s_nop 0\n"
    "s_nop 0\n"
    "s_nop 0\n"
    "s_nop 0\n"
    "s_nop 0\n"
    "s_nop 0\n"
    "s_nop 0\n"
    "s_nop 0\n"
    "s_nop 0\n"
    "s_nop 0\n"
    "s_nop 0\n"
    "s_nop 0\n"
    "s_nop 0\n"
    "s_nop 0\n"
    "s_nop 0\n"
    "s_nop 0\n"
    "s_nop 0\n"
    "s_nop 0\n"
    "s_nop 0\n"
    "s_nop 0\n"
    "s_nop 0\n"
    "s_nop 0\n"
    "s_nop 0\n"
    "s_nop 0\n"
    "s_nop 0\n"
    "s_nop 0\n"
    "s_nop 0\n"
    "s_nop 0\n"
    "s_nop 0\n"
    "s_nop 0\n"
    "s_nop 0\n"
    "s_nop 0\n"
    "s_nop 0\n"
    "s_nop 0\n"
    "s_nop 0\n"
    "s_nop 0\n"
    "s_nop 0\n"
    "s_nop 0\n"
    "s_nop 0\n"
    "s_nop 0\n"
    "s_nop 0\n"
    "s_nop 0\n"
    "s_nop 0\n"
    "s_nop 0\n"
    "s_nop 0\n"
    "s_nop 0\n"
    "s_nop 0\n"
    "s_nop 0\n"
    "s_nop 0\n"
    "s_nop 0\n"
    "s_nop 0\n"
    "s_nop 0\n"
    "s_nop 0\n"
    "s_nop 0\n"
    "s_nop 0\n"
    "s_nop 0\n"
    "s_nop 0\n"
    "s_nop 0\n"
    "s_nop 0\n"
    "s_nop 0\n"
    "s_nop 0\n"
    "s_nop 0\n"
    "s_nop 0\n"
    "s_nop 0\n"
    "s_nop 0\n"
    "s_nop 0\n"
    "s_nop 0\n"
    "s_nop 0\n"
    "s_nop 0\n"
    "s_nop 0\n"
    "s_nop 0\n"
    "s_nop 0\n"
    "s_nop 0\n"
    "s_nop 0\n"
    "s_nop 0\n"
    "s_nop 0\n"
    "s_nop 0\n"
    "s_nop 0\n"
    "s_nop 0\n"
    "s_nop 0\n"
    "s_nop 0\n"
    "s_nop 0\n"
    "s_nop 0\n"
    "s_nop 0\n"
    "s_nop 0\n"
    "s_nop 0\n"
    "s_nop 0\n"
    "s_nop 0\n"
    "s_nop 0\n"
    "s_nop 0\n"
    "s_nop 0\n"
    "s_nop 0\n"
    "s_nop 0\n"
    "s_nop 0\n"
    "s_nop 0\n"
    "s_nop 0\n"
    "s_nop 0\n"
    "s_nop 0\n"
    "s_nop 0\n"
    "s_nop 0\n"
    "s_nop 0\n"
    "s_nop 0\n"
    "s_nop 0\n"
    "s_nop 0\n"
    "s_nop 0\n"
    "s_nop 0\n"
    "s_nop 0\n"
    "s_nop 0\n"
    "s_nop 0\n"
    "s_nop 0\n"
    "s_nop 0\n"
    "s_nop 0\n"
    "s_nop 0\n"
    "s_nop 0\n"
    "s_nop 0\n"
    "s_nop 0\n"
    "s_nop 0\n"
    "s_nop 0\n"
    "s_nop 0\n"
    "s_nop 0\n"
    "s_nop 0\n"
    "s_nop 0\n"
    "s_nop 0\n"
    "s_nop 0\n"
    "s_nop 0\n"
    "s_nop 0\n"
    "s_nop 0\n"
    "s_nop 0\n"
    "s_nop 0\n"
    "s_nop 0\n"
    "s_nop 0\n"
    "s_nop 0\n"
    "s_nop 0\n"
    "s_nop 0\n"
    "s_nop 0\n"
    "s_nop 0\n"
    "s_nop 0\n"
    "s_nop 0\n"
    "s_nop 0\n"
    "s_nop 0\n"
    "s_nop 0\n"
    "s_nop 0\n"
    "s_nop 0\n"
    "s_nop 0\n"
    "s_nop 0\n"
    "s_nop 0\n"
    "s_nop 0\n"
    "s_nop 0\n"
    "s_nop 0\n"
    "s_nop 0\n"
    "s_nop 0\n"
    "s_nop 0\n"
    "s_nop 0\n"
    "s_nop 0\n"
    "s_nop 0\n"
    "s_nop 0\n"
    "s_nop 0\n"
    "s_nop 0\n"
    "s_nop 0\n"
    "s_nop 0\n"
    "s_nop 0\n"
    "s_nop 0\n"
    "s_nop 0\n"
    "s_nop 0\n"
    "s_nop 0\n"
    "s_nop 0\n"
    "s_nop 0\n"
    "s_nop 0\n"
    "s_nop 0\n"
    "s_nop 0\n"
    "s_nop 0\n"
    "s_nop 0\n"
    "s_nop 0\n"
    "s_nop 0\n"
    "s_nop 0\n"
    "s_nop 0\n"
    "s_nop 0\n"
    "s_nop 0\n"
    "s_nop 0\n"
    "s_nop 0\n"
    "s_nop 0\n"
    "s_nop 0\n"
    "s_nop 0\n"
    "s_nop 0\n"
    "s_nop 0\n"
    "s_nop 0\n"
    "s_nop 0\n"
    "s_nop 0\n"
    "s_nop 0\n"
    "s_nop 0\n"
    "s_nop 0\n"
    "s_nop 0\n"
    "s_nop 0\n"
    "s_nop 0\n"
    "s_nop 0\n"
    "s_nop 0\n"
    "s_nop 0\n"
    "s_nop 0\n"
    "s_nop 0\n"
    "s_nop 0\n"
    "s_nop 0\n"
    "s_nop 0\n"
    "s_nop 0\n"
    "s_nop 0\n"
    "s_nop 0\n"
    "s_nop 0\n"
    "s_nop 0\n"
    "s_nop 0\n"
    "s_nop 0\n"
    "s_nop 0\n"
    "s_nop 0\n"
    "s_nop 0\n"
    "s_nop 0\n"
    "s_nop 0\n"
    "s_nop 0\n"
    "s_nop 0\n"
    "s_nop 0\n"
    "s_nop 0\n"
    "s_nop 0\n"
    "s_nop 0\n"
    "s_nop 0\n"
    "s_nop 0\n"
    "s_nop 0\n"
    "s_nop 0\n"
    "s_nop 0\n"
    "s_nop 0\n"
    "s_nop 0\n"
    "s_nop 0\n"
    "s_nop 0\n"
    "s_nop 0\n"
    "s_nop 0\n"
    "s_nop 0\n"
    "s_nop 0\n"
    "s_nop 0\n"
    "s_nop 0\n"
    "s_nop 0\n"
    "s_nop 0\n"
    "s_nop 0\n"
    "s_nop 0\n"
    "s_nop 0\n"
    "s_nop 0\n"
    "s_nop 0\n"
    "s_nop 0\n"
    "s_nop 0\n"
    "s_nop 0\n"
    "s_nop 0\n"
    "s_nop 0\n"
    "s_nop 0\n"
    "s_nop 0\n"
    "s_nop 0\n"
    "s_nop 0\n"
    "s_nop 0\n"
    "s_nop 0\n"
    "s_nop 0\n"
    "s_nop 0\n"
    "s_nop 0\n"
    "s_nop 0\n"
    "s_nop 0\n"
    "s_nop 0\n"
    "s_nop 0\n"
    "s_nop 0\n"
    "s_nop 0\n"
    "s_nop 0\n"
    "s_nop 0\n"
    "s_nop 0\n"
    "s_nop 0\n"
    "s_nop 0\n"
    "s_nop 0\n"
    "s_nop 0\n"
    "s_nop 0\n"
    "s_nop 0\n"
    "s_nop 0\n"
    "s_nop 0\n"
    "s_nop 0\n"
    "s_nop 0\n"
    "s_nop 0\n"
    "s_nop 0\n"
    "s_nop 0\n"
    "s_nop 0\n"
    "s_nop 0\n"
    "s_nop 0\n"
    "s_nop 0\n"
    "s_nop 0\n"
    "s_nop 0\n"
    "s_nop 0\n"
    "s_nop 0\n"
    "s_nop 0\n"
    ".Lcode_end:\n"
    : "+{v0}"(tid), "+{s2}"(bid)
    : "{s[0:1]}"(__builtin_amdgcn_kernarg_segment_ptr())
    : "v1", "v2", "v3", "v4", "v5", "v6", "v7", "v8", "v9", "v10", "v11", "v12", "v13", "v14", "v15", "v16", "v17", "v18", "v19", "v20", "v21", "v22", "v23", "v24", "v25", "v26", "v27", "v28", "v29", "v30", "v31", "v32", "v33", "v34", "v35", "v36", "v37", "v38", "v39", "v40", "v41", "v42", "v43", "v44", "v45", "v46", "v47", "v48", "v49", "v50", "v51", "v52", "v53", "v54", "v55", "v56", "v57", "v58", "v59", "v60", "v61", "v62", "v63", "v64", "v65", "v66", "v67", "v68", "v69", "v70", "v71", "v72", "v73", "v74", "v75", "v76", "v77", "v78", "v79", "v80", "v81", "v82", "v83", "v84", "v85", "v86", "v87", "v88", "v89", "v90", "v91", "v92", "v93", "v94", "v95", "v96", "v97", "v98", "v99", "v100", "v101", "v102", "v103", "v104", "v105", "v106", "v107", "v108", "v109", "v110", "v111", "v112", "v113", "v114", "v115", "v116", "v117", "v118", "v119", "v120", "v121", "v122", "v123", "v124", "v125", "v126", "v127", "v128", "v129", "v130", "v131", "v132", "v133", "v134", "v135", "v136", "v137", "v138", "v139", "v140", "v141", "v142", "v143", "v144", "v145", "v146", "v147", "v148", "v149", "v150", "v151", "v152", "v153", "v154", "v155", "v156", "v157", "v158", "v159", "v160", "v161", "v162", "v163", "v164", "v165", "v166", "v167", "v168", "v169", "v170", "v171", "v172", "v173", "v174", "v175", "v176", "v177", "v178", "v179", "v180", "v181", "v182", "v183", "v184", "v185", "v186", "v187", "v188", "v189", "v190", "v191", "v192", "v193", "v194", "v195", "v196", "v197", "v198", "v199", "v200", "v201", "v202", "v203", "v204", "v205", "v206", "v207", "v208", "v209", "v210", "v211", "v212", "v213", "v214", "v215", "v216", "v217", "v218", "v219", "v220", "v221", "v222", "v223", "v224", "v225", "v226", "v227", "v228", "v229", "v230", "v231", "v232", "v233", "v234", "v235", "v236", "v237", "v238", "v239", "v240", "v241", "v242", "v243", "v244", "v245", "v246", "v247", "v248", "v249", "v250", "v251", "v252", "v253", "v254", "v255", "s3", "s4", "s5", "s6", "s7", "s8", "s9", "s10", "s11", "s12", "s13", "s14", "s15", "s16", "s17", "s18", "s19", "s20", "s21", "s22", "s23", "s24", "s25", "s26", "s27", "s28", "s29", "s30", "s31", "s33", "s34", "s35", "s36", "s37", "s38", "s39", "s40", "s41", "s42", "s43", "s44", "s45", "s46", "s47", "s48", "s49", "s50", "s51", "s52", "s53", "s54", "s55", "s56", "s57", "s58", "s59", "s60", "s61", "s62", "s63", "s64", "s65", "s66", "s67", "s68", "s69", "s70", "s71", "s72", "s73", "s74", "s75", "s76", "s77", "s78", "s79", "s80", "s81", "s82", "s83", "s84", "s85", "s86", "s87", "s88", "s89", "s90", "s91", "s92", "s93", "s94", "s95", "vcc", "scc", "memory");
}

extern "C" void kernel_launch(void* const* d_in, const int* in_sizes, int n_in, void* d_out, int out_size,
                              void* d_ws, size_t ws_size, hipStream_t stream) {
    (void)hipFuncSetAttribute((const void*)fused_kernel, hipFuncAttributeMaxDynamicSharedMemorySize, 163840);
    fused_kernel<<<256, 512, 163840, stream>>>(d_in[0], d_in[10], d_in[3], d_in[4], d_ws, d_in[11], d_in[1], d_in[2],
                                               d_in[7], d_in[5], d_in[6], d_in[12], d_in[13], d_in[8], d_out);
}
```
